```python
import torch

def _softmax(x, axis=-1):
    return torch.softmax(x, dim=axis)
def _gelu(x):
    return torch.nn.functional.gelu(x, approximate="tanh")
def _silu(x):
    return torch.nn.functional.silu(x)
def _sigmoid(x):
    return torch.sigmoid(x)
def _layernorm(x, axis=-1, eps=1e-5):
    m = x.mean(dim=axis, keepdim=True)
    v = x.var(dim=axis, keepdim=True, unbiased=False)
    return (x - m) / torch.sqrt(v + eps)
def _scatter_add(tgt, idx, src, axis=0):
    return tgt.clone().index_add_(axis, idx, src)
def _segment_sum(src, seg, n):
    out = torch.zeros((n,) + src.shape[1:], dtype=src.dtype)
    return out.index_add_(0, seg, src)
def _pad(x, pw):
    flat = tuple(v for pair in reversed(pw) for v in pair)
    return torch.nn.functional.pad(x, flat)

kernel_name = 'standalone__mha_block_cached__near_ridge'

def setup_inputs():

    _w = (lambda a: (lambda t: t.to(torch.bfloat16) if t.is_floating_point() else t)(torch.as_tensor(a)))
    B, Sq, D = 8, 256, 640
    H, Sk, Dh = 32, 1024, 192
    return {
        "x": _w((lambda *s: torch.randn(*s, dtype=torch.bfloat16) * 0.8)(B, Sq, D)),
        "k_cache": _w((lambda *s: torch.randn(*s, dtype=torch.bfloat16) * 0.8)(B, H, Sk, Dh)),
        "v_cache": _w((lambda *s: torch.randn(*s, dtype=torch.bfloat16) * 0.05)(B, H, Sk, Dh)),
        "wq": _w((lambda *s: torch.randn(*s, dtype=torch.bfloat16) * 0.8)(D, H * Dh) * (D ** -0.5)),
        "wo": _w((lambda *s: torch.randn(*s, dtype=torch.bfloat16) * 0.05)(H * Dh, D) * ((H * Dh) ** -0.5)),
    }

def reference(x, k_cache, v_cache, wq, wo):
    B, Sq, D = x.shape
    H, Dh = 32, 192
    q = (lambda x, *d: x.permute(*d))((x @ wq).reshape(B, Sq, H, Dh), 0, 2, 1, 3)
    scores = torch.einsum("bhqd,bhkd->bhqk", q, k_cache) * (Dh ** -0.5)
    p = _softmax(scores, axis=-1)
    o = torch.einsum("bhqk,bhkd->bhqd", p, v_cache)
    return (lambda x, *d: x.permute(*d))(o, 0, 2, 1, 3).reshape(B, Sq, H * Dh) @ wo
```

```cpp
#include <hip/hip_runtime.h>
#include <hip/hip_bf16.h>
typedef __hip_bfloat16 bf16;
constexpr int B=8,SQ=256,D=640,H=32,SK=1024,DH=192,HD=H*DH;
__global__ void k_q(const bf16* x,const bf16* wq,float* q){
  int idx=blockIdx.x*256+threadIdx.x;
  int n=idx%HD, m=idx/HD; float a=0;
  for(int c=0;c<D;c++) a+=__bfloat162float(x[m*D+c])*__bfloat162float(wq[(size_t)c*HD+n]);
  q[idx]=__bfloat162float(__float2bfloat16(a));
}
__global__ void k_attn(const float* q,const bf16* k,const bf16* v,float* o){
  __shared__ float p[SK]; __shared__ float qs[DH]; __shared__ float red[256];
  int s=blockIdx.x%SQ, bh=blockIdx.x/SQ, h=bh%H, b=bh/H, t=threadIdx.x;
  if(t<DH) qs[t]=q[(size_t)(b*SQ+s)*HD+h*DH+t];
  __syncthreads();
  float mx=-1e30f;
  for(int j=t;j<SK;j+=256){ float a=0; const bf16* kr=k+((size_t)bh*SK+j)*DH; for(int d=0;d<DH;d++) a+=qs[d]*__bfloat162float(kr[d]); a*=0.07216878364870322f; p[j]=a; mx=fmaxf(mx,a);}
  red[t]=mx; __syncthreads();
  for(int o2=128;o2>0;o2>>=1){ if(t<o2) red[t]=fmaxf(red[t],red[t+o2]); __syncthreads(); }
  mx=red[0]; __syncthreads();
  float sm=0; for(int j=t;j<SK;j+=256){ float e=__expf(p[j]-mx); p[j]=e; sm+=e; }
  red[t]=sm; __syncthreads();
  for(int o2=128;o2>0;o2>>=1){ if(t<o2) red[t]+=red[t+o2]; __syncthreads(); }
  sm=red[0];
  if(t<DH){ float a=0; for(int j=0;j<SK;j++) a+=p[j]*__bfloat162float(v[((size_t)bh*SK+j)*DH+t]); o[(size_t)(b*SQ+s)*HD+h*DH+t]=__bfloat162float(__float2bfloat16(a/sm)); }
}
__global__ void k_o(const float* o,const bf16* wo,bf16* out){
  int idx=blockIdx.x*256+threadIdx.x; int n=idx%D, m=idx/D; float a=0;
  for(int c=0;c<HD;c++) a+=o[(size_t)m*HD+c]*__bfloat162float(wo[(size_t)c*D+n]);
  out[idx]=__float2bfloat16(a);
}
extern "C" void kernel_launch(void* const* d_in, const int* in_sizes, int n_in, void* d_out, int out_size, void* d_ws, size_t ws_size, hipStream_t stream){
  const bf16* x=(const bf16*)d_in[0]; const bf16* k=(const bf16*)d_in[1]; const bf16* v=(const bf16*)d_in[2]; const bf16* wq=(const bf16*)d_in[3]; const bf16* wo=(const bf16*)d_in[4];
  float* q=(float*)d_ws; float* o=q+(size_t)B*SQ*HD;
  k_q<<<B*SQ*HD/256,256,0,stream>>>(x,wq,q);
  k_attn<<<B*H*SQ,256,0,stream>>>(q,k,v,o);
  k_o<<<B*SQ*D/256,256,0,stream>>>(o,wo,(bf16*)d_out);
}
```

```cpp
#include <hip/hip_runtime.h>
#include <stdint.h>

__global__ __launch_bounds__(256, 1) void mha_fused(const char* x, const char* kc, const char* vc, const char* wq, const char* wo,
                                                    char* out, char* ws) {
  __shared__ __attribute__((aligned(128))) char lds[163840];
  unsigned lb = (unsigned)(size_t)lds, bi = blockIdx.x, tid = threadIdx.x;
  uint64_t ka = (uint64_t)__builtin_amdgcn_kernarg_segment_ptr();
  asm volatile(
"s_load_dwordx8 s[8:15], s[4:5], 0\n"
"s_load_dwordx4 s[16:19], s[4:5], 32\n"
"s_load_dwordx2 s[20:21], s[4:5], 48\n"
"v_readfirstlane_b32 s77, v0\n"
"v_and_b32 v40, 63, v0\n"
"v_lshrrev_b32 v41, 3, v40\n"
"v_and_b32 v42, 7, v40\n"
"v_lshrrev_b32 v43, 4, v40\n"
"s_lshr_b32 s77, s77, 6\n"
"v_xor_b32 v45, v42, v43\n"
"v_mul_u32_u24 v46, 1280, v41\n"
"s_and_b32 s93, s6, 4\n"
"s_lshr_b32 s3, s6, 6\n"
"s_add_u32 s93, s93, s3\n"
"v_lshl_add_u32 v36, v45, 4, v46\n"
"v_xor_b32 v45, 64, v36\n"
"v_add_u32 v37, 9216, v45\n"
"v_add_u32 v38, 18432, v36\n"
"v_add_u32 v39, 27648, v45\n"
"s_lshl_b32 s3, s93, 2\n"
"s_add_u32 s3, s3, s77\n"
"s_mul_i32 s68, s3, 81920\n"
"s_mov_b32 s54, 81920\n"
"s_mov_b32 s55, 131072\n"
"s_lshl_b32 s3, s77, 13\n"
"s_add_u32 s38, s3, 24576\n"
"s_add_u32 s39, s3, 73728\n"
"s_cmp_eq_u32 s77, 3\n"
"s_cselect_b32 s38, 147456, s38\n"
"s_cselect_b32 s39, 155648, s39\n"
"s_add_u32 s38, s38, s7\n"
"s_add_u32 s39, s39, s7\n"
"s_and_b32 s94, s6, 3\n"
"s_lshl_b32 s94, s94, 3\n"
"s_bfe_u32 s3, s6, 196611\n"
"s_add_u32 s94, s94, s3\n"
"s_mul_i32 s3, s94, 384\n"
"s_mul_i32 s33, s77, 98304\n"
"s_add_u32 s3, s3, s33\n"
"s_sub_u32 s50, 7864320, s3\n"
"s_mov_b32 s51, 131072\n"
"s_mul_i32 s33, s77, 3072\n"
"s_add_u32 s76, s33, s7\n"
"v_lshlrev_b32 v47, 4, v42\n"
"v_mul_u32_u24 v48, 43, v41\n"
"v_lshrrev_b32 v48, 7, v48\n"
"v_mul_u32_u24 v46, 3, v48\n"
"v_sub_u32 v51, v41, v46\n"
"v_lshrrev_b32 v28, 1, v48\n"
"v_lshlrev_b32 v46, 12, v46\n"
"v_lshl_add_u32 v46, v51, 7, v46\n"
"v_and_b32 v45, 1, v28\n"
"v_lshlrev_b32 v45, 6, v45\n"
"v_xor_b32 v45, v45, v47\n"
"v_add_u32 v23, v45, v46\n"
"v_add_u32 v45, 8, v41\n"
"v_mul_u32_u24 v49, 43, v45\n"
"v_lshrrev_b32 v49, 7, v49\n"
"v_mul_u32_u24 v46, 3, v49\n"
"v_sub_u32 v52, v45, v46\n"
"v_lshrrev_b32 v29, 1, v49\n"
"v_lshlrev_b32 v46, 12, v46\n"
"v_lshl_add_u32 v46, v52, 7, v46\n"
"v_and_b32 v45, 1, v29\n"
"v_lshlrev_b32 v45, 6, v45\n"
"v_xor_b32 v45, v45, v47\n"
"v_add_u32 v45, 4294966272, v45\n"
"v_add_u32 v24, v45, v46\n"
"v_add_u32 v45, 16, v41\n"
"v_mul_u32_u24 v50, 43, v45\n"
"v_lshrrev_b32 v50, 7, v50\n"
"v_mul_u32_u24 v46, 3, v50\n"
"v_sub_u32 v53, v45, v46\n"
"v_lshrrev_b32 v30, 1, v50\n"
"v_lshlrev_b32 v46, 12, v46\n"
"v_lshl_add_u32 v46, v53, 7, v46\n"
"v_and_b32 v45, 1, v30\n"
"v_lshlrev_b32 v45, 6, v45\n"
"v_xor_b32 v45, v45, v47\n"
"v_add_u32 v45, 4294965248, v45\n"
"v_add_u32 v25, v45, v46\n"
"s_waitcnt lgkmcnt(0)\n"
"s_add_u32 s52, s8, s68\n"
"s_addc_u32 s53, s9, 0\n"
"s_mov_b32 s25, 0\n"
"s_add_u32 m0, s38, 0\n"
"s_nop 0\n"
"buffer_load_dwordx4 v36, s[52:55], s25 offen lds\n"
"buffer_load_dwordx4 v37, s[52:55], s25 offen offset:1024 lds\n"
"buffer_load_dwordx4 v38, s[52:55], s25 offen offset:2048 lds\n"
"buffer_load_dwordx4 v39, s[52:55], s25 offen offset:3072 lds\n"
"s_add_u32 s48, s14, s3\n"
"s_addc_u32 s49, s15, 0\n"
"s_mov_b32 s24, 0\n"
"s_add_u32 m0, s76, 122880\n"
"s_nop 0\n"
"buffer_load_dwordx4 v23, s[48:51], s24 offen lds\n"
"buffer_load_dwordx4 v24, s[48:51], s24 offen offset:1024 lds\n"
"buffer_load_dwordx4 v25, s[48:51], s24 offen offset:2048 lds\n"
"s_mov_b32 s25, 40960\n"
"s_add_u32 m0, s38, 4096\n"
"s_nop 0\n"
"buffer_load_dwordx4 v36, s[52:55], s25 offen lds\n"
"buffer_load_dwordx4 v37, s[52:55], s25 offen offset:1024 lds\n"
"buffer_load_dwordx4 v38, s[52:55], s25 offen offset:2048 lds\n"
"buffer_load_dwordx4 v39, s[52:55], s25 offen offset:3072 lds\n"
"s_mov_b32 s24, 393216\n"
"s_add_u32 m0, s76, 135168\n"
"s_nop 0\n"
"buffer_load_dwordx4 v23, s[48:51], s24 offen lds\n"
"buffer_load_dwordx4 v24, s[48:51], s24 offen offset:1024 lds\n"
"buffer_load_dwordx4 v25, s[48:51], s24 offen offset:2048 lds\n"
"v_lshlrev_b32 v18, 4, v40\n"
"v_and_b32 v44, 15, v40\n"
"s_lshl_b32 s3, s77, 11\n"
"s_bfe_u32 s95, s6, 196611\n"
"s_lshl_b32 s95, s95, 13\n"
"s_add_u32 s3, s3, s95\n"
"v_lshlrev_b32 v45, 7, v44\n"
"s_getpc_b64 s[64:65]\n"
".Lmha_pc:\n"
"s_mov_b32 s66, .Lmha_cend-.Lmha_pc\n"
"s_mov_b32 s67, 131072\n"
"buffer_load_dword v62, v45, s[64:67], s3 offen\n"
"v_bfe_u32 v45, v40, 1, 3\n"
"v_xor_b32 v45, v45, v43\n"
"v_lshlrev_b32 v45, 4, v45\n"
"v_lshl_add_u32 v46, v44, 7, v45\n"
"v_xor_b32 v47, 64, v46\n"
"v_add_u32 v32, s38, v46\n"
"v_add_u32 v33, s38, v47\n"
"v_add_u32 v34, s39, v46\n"
"v_add_u32 v35, s39, v47\n"
"v_lshlrev_b32 v45, 8, v44\n"
"v_add3_u32 v0, v45, v46, s7\n"
"v_add3_u32 v1, v45, v47, s7\n"
"v_add_u32 v2, 36864, v0\n"
"v_add_u32 v3, 36864, v1\n"
"v_bfe_u32 v45, v40, 2, 2\n"
"v_lshl_add_u32 v45, v43, 3, v45\n"
"v_mul_u32_u24 v45, 384, v45\n"
"v_and_b32 v46, 3, v40\n"
"v_lshl_add_u32 v45, v46, 4, v45\n"
"v_bfe_u32 v47, v40, 3, 1\n"
"v_lshl_add_u32 v45, v47, 6, v45\n"
"s_add_u32 s3, s7, 49152\n"
"v_xor_b32 v47, 64, v45\n"
"v_add_u32 v19, s3, v45\n"
"v_add_u32 v20, s3, v47\n"
"v_add_u32 v21, 61440, v19\n"
"v_add_u32 v22, 61440, v20\n"
"s_and_b32 s3, s77, 1\n"
"s_lshl_b32 s3, s3, 6\n"
"v_xor_b32 v47, s3, v18\n"
"s_lshl_b32 s33, s93, 5\n"
"s_add_u32 s95, s33, s94\n"
"s_mul_i32 s95, s95, 393216\n"
"s_mul_i32 s27, s77, 3072\n"
"s_add_u32 s95, s95, s27\n"
"s_sub_u32 s27, 393216, s27\n"
"v_lshlrev_b32 v45, 4, v28\n"
"v_xor_b32 v12, v45, v47\n"
"v_lshlrev_b32 v45, 4, v29\n"
"v_xor_b32 v13, v45, v47\n"
"v_lshlrev_b32 v45, 4, v30\n"
"v_xor_b32 v14, v45, v47\n"
"s_add_u32 s40, s10, s95\n"
"s_addc_u32 s41, s11, 0\n"
"s_mov_b32 s42, s27\n"
"s_mov_b32 s43, 131072\n"
"s_add_u32 s44, s12, s95\n"
"s_addc_u32 s45, s13, 0\n"
"s_mov_b32 s46, s27\n"
"s_mov_b32 s47, 131072\n"
"s_mov_b32 s24, 786432\n"
"s_add_u32 m0, s76, 98304\n"
"s_nop 0\n"
"buffer_load_dwordx4 v23, s[48:51], s24 offen lds\n"
"buffer_load_dwordx4 v24, s[48:51], s24 offen offset:1024 lds\n"
"buffer_load_dwordx4 v25, s[48:51], s24 offen offset:2048 lds\n"
"s_mov_b32 s25, 128\n"
"s_add_u32 m0, s39, 0\n"
"s_nop 0\n"
"buffer_load_dwordx4 v36, s[52:55], s25 offen lds\n"
"buffer_load_dwordx4 v37, s[52:55], s25 offen offset:1024 lds\n"
"buffer_load_dwordx4 v38, s[52:55], s25 offen offset:2048 lds\n"
"buffer_load_dwordx4 v39, s[52:55], s25 offen offset:3072 lds\n"
"s_mov_b32 s24, 1179648\n"
"s_add_u32 m0, s76, 49152\n"
"s_nop 0\n"
"buffer_load_dwordx4 v23, s[48:51], s24 offen lds\n"
"buffer_load_dwordx4 v24, s[48:51], s24 offen offset:1024 lds\n"
"buffer_load_dwordx4 v25, s[48:51], s24 offen offset:2048 lds\n"
"s_mov_b32 s25, 41088\n"
"s_add_u32 m0, s39, 4096\n"
"s_nop 0\n"
"buffer_load_dwordx4 v36, s[52:55], s25 offen lds\n"
"buffer_load_dwordx4 v37, s[52:55], s25 offen offset:1024 lds\n"
"buffer_load_dwordx4 v38, s[52:55], s25 offen offset:2048 lds\n"
"buffer_load_dwordx4 v39, s[52:55], s25 offen offset:3072 lds\n"
"v_mul_u32_u24 v31, 380000, v40\n"
"buffer_load_dword v62, v31, s[40:43], 0 offen nt\n"
"s_waitcnt vmcnt(19)\n"
"ds_read_b128 a[0:3], v32 offset:0\n"
"ds_read_b128 a[16:19], v33 offset:0\n"
"ds_read_b128 a[4:7], v32 offset:2048\n"
"ds_read_b128 a[20:23], v33 offset:2048\n"
"ds_read_b128 a[8:11], v32 offset:4096\n"
"ds_read_b128 a[24:27], v33 offset:4096\n"
"ds_read_b128 a[12:15], v32 offset:6144\n"
"ds_read_b128 a[28:31], v33 offset:6144\n"
"s_waitcnt vmcnt(16)\n"
"s_barrier\n"
"ds_read_b64_tr_b16 a[192:193], v21 offset:12288\n"
"ds_read_b64_tr_b16 a[194:195], v21 offset:13824\n"
"ds_read_b64_tr_b16 a[196:197], v21 offset:24576\n"
"ds_read_b64_tr_b16 a[198:199], v21 offset:26112\n"
"ds_read_b64_tr_b16 a[200:201], v21 offset:12296\n"
"ds_read_b64_tr_b16 a[202:203], v21 offset:13832\n"
"ds_read_b64_tr_b16 a[204:205], v21 offset:24584\n"
"ds_read_b64_tr_b16 a[206:207], v21 offset:26120\n"
"ds_read_b64_tr_b16 a[208:209], v22 offset:12288\n"
"ds_read_b64_tr_b16 a[210:211], v22 offset:13824\n"
"ds_read_b64_tr_b16 a[212:213], v22 offset:24576\n"
"ds_read_b64_tr_b16 a[214:215], v22 offset:26112\n"
"ds_read_b64_tr_b16 a[216:217], v22 offset:12296\n"
"ds_read_b64_tr_b16 a[218:219], v22 offset:13832\n"
"ds_read_b64_tr_b16 a[220:221], v22 offset:24584\n"
"ds_read_b64_tr_b16 a[222:223], v22 offset:26120\n"
"s_waitcnt lgkmcnt(8)\n"
"v_mfma_f32_16x16x32_bf16 v[64:67], a[192:195], a[0:3], 0\n"
"ds_read_b64_tr_b16 a[224:225], v21 offset:12416\n"
"ds_read_b64_tr_b16 a[226:227], v21 offset:13952\n"
"v_mfma_f32_16x16x32_bf16 v[64:67], a[196:199], a[16:19], v[64:67]\n"
"ds_read_b64_tr_b16 a[228:229], v21 offset:24704\n"
"ds_read_b64_tr_b16 a[230:231], v21 offset:26240\n"
"v_mfma_f32_16x16x32_bf16 v[68:71], a[196:199], a[20:23], 0\n"
"s_mov_b32 s24, 1572864\n"
"s_add_u32 m0, s76, 110592\n"
"v_mfma_f32_16x16x32_bf16 v[68:71], a[192:195], a[4:7], v[68:71]\n"
"buffer_load_dwordx4 v23, s[48:51], s24 offen lds\n"
"v_mfma_f32_16x16x32_bf16 v[72:75], a[192:195], a[8:11], 0\n"
"v_lshrrev_b32 v45, 2, v40\n"
"v_mfma_f32_16x16x32_bf16 v[72:75], a[196:199], a[24:27], v[72:75]\n"
"v_mul_u32_u24 v45, 384, v45\n"
"v_mfma_f32_16x16x32_bf16 v[76:79], a[196:199], a[28:31], 0\n"
"v_lshl_add_u32 v45, v46, 3, v45\n"
"v_mfma_f32_16x16x32_bf16 v[76:79], a[192:195], a[12:15], v[76:79]\n"
"buffer_load_dwordx4 v24, s[48:51], s24 offen offset:1024 lds\n"
"v_mfma_f32_16x16x32_bf16 v[80:83], a[200:203], a[0:3], 0\n"
"ds_read_b64_tr_b16 a[232:233], v21 offset:12424\n"
"ds_read_b64_tr_b16 a[234:235], v21 offset:13960\n"
"v_mfma_f32_16x16x32_bf16 v[80:83], a[204:207], a[16:19], v[80:83]\n"
"ds_read_b64_tr_b16 a[236:237], v21 offset:24712\n"
"ds_read_b64_tr_b16 a[238:239], v21 offset:26248\n"
"v_mfma_f32_16x16x32_bf16 v[84:87], a[204:207], a[20:23], 0\n"
"v_bfe_u32 v47, v40, 3, 2\n"
"v_mfma_f32_16x16x32_bf16 v[84:87], a[200:203], a[4:7], v[84:87]\n"
"buffer_load_dwordx4 v25, s[48:51], s24 offen offset:2048 lds\n"
"v_mfma_f32_16x16x32_bf16 v[88:91], a[200:203], a[8:11], 0\n"
"v_lshl_add_u32 v45, v47, 5, v45\n"
"v_mfma_f32_16x16x32_bf16 v[88:91], a[204:207], a[24:27], v[88:91]\n"
"s_add_u32 s3, s7, 61440\n"
"v_mfma_f32_16x16x32_bf16 v[92:95], a[204:207], a[28:31], 0\n"
"v_add_u32 v4, s3, v45\n"
"v_mfma_f32_16x16x32_bf16 v[92:95], a[200:203], a[12:15], v[92:95]\n"
"s_waitcnt lgkmcnt(8)\n"
"s_waitcnt lgkmcnt(15)\n"
"v_mfma_f32_16x16x32_bf16 v[96:99], a[208:211], a[0:3], 0\n"
"ds_read_b64_tr_b16 a[192:193], v22 offset:12416\n"
"ds_read_b64_tr_b16 a[194:195], v22 offset:13952\n"
"v_mfma_f32_16x16x32_bf16 v[96:99], a[212:215], a[16:19], v[96:99]\n"
"ds_read_b64_tr_b16 a[196:197], v22 offset:24704\n"
"ds_read_b64_tr_b16 a[198:199], v22 offset:26240\n"
"v_mfma_f32_16x16x32_bf16 v[100:103], a[212:215], a[20:23], 0\n"
"s_mov_b32 s25, 256\n"
"s_add_u32 m0, s38, 0\n"
"v_mfma_f32_16x16x32_bf16 v[100:103], a[208:211], a[4:7], v[100:103]\n"
"buffer_load_dwordx4 v36, s[52:55], s25 offen lds\n"
"v_mfma_f32_16x16x32_bf16 v[104:107], a[208:211], a[8:11], 0\n"
"v_add_u32 v8, 49152, v4\n"
"v_mfma_f32_16x16x32_bf16 v[104:107], a[212:215], a[24:27], v[104:107]\n"
"v_xor_b32 v47, 32, v45\n"
"v_mfma_f32_16x16x32_bf16 v[108:111], a[212:215], a[28:31], 0\n"
"v_add_u32 v5, s3, v47\n"
"v_mfma_f32_16x16x32_bf16 v[108:111], a[208:211], a[12:15], v[108:111]\n"
"buffer_load_dwordx4 v37, s[52:55], s25 offen offset:1024 lds\n"
"v_mfma_f32_16x16x32_bf16 v[112:115], a[216:219], a[0:3], 0\n"
"ds_read_b64_tr_b16 a[200:201], v22 offset:12424\n"
"ds_read_b64_tr_b16 a[202:203], v22 offset:13960\n"
"v_mfma_f32_16x16x32_bf16 v[112:115], a[220:223], a[16:19], v[112:115]\n"
"ds_read_b64_tr_b16 a[204:205], v22 offset:24712\n"
"ds_read_b64_tr_b16 a[206:207], v22 offset:26248\n"
"v_mfma_f32_16x16x32_bf16 v[116:119], a[220:223], a[20:23], 0\n"
"v_add_u32 v9, 49152, v5\n"
"v_mfma_f32_16x16x32_bf16 v[116:119], a[216:219], a[4:7], v[116:119]\n"
"buffer_load_dwordx4 v38, s[52:55], s25 offen offset:2048 lds\n"
"v_mfma_f32_16x16x32_bf16 v[120:123], a[216:219], a[8:11], 0\n"
"v_xor_b32 v47, 64, v45\n"
"v_mfma_f32_16x16x32_bf16 v[120:123], a[220:223], a[24:27], v[120:123]\n"
"v_add_u32 v6, s3, v47\n"
"v_mfma_f32_16x16x32_bf16 v[124:127], a[220:223], a[28:31], 0\n"
"v_add_u32 v10, 49152, v6\n"
"v_mfma_f32_16x16x32_bf16 v[124:127], a[216:219], a[12:15], v[124:127]\n"
"s_waitcnt lgkmcnt(8)\n"
"buffer_load_dwordx4 v39, s[52:55], s25 offen offset:3072 lds\n"
"v_mfma_f32_16x16x32_bf16 v[128:131], a[224:227], a[0:3], 0\n"
"ds_read_b64_tr_b16 a[208:209], v21 offset:12544\n"
"ds_read_b64_tr_b16 a[210:211], v21 offset:14080\n"
"v_mfma_f32_16x16x32_bf16 v[128:131], a[228:231], a[16:19], v[128:131]\n"
"ds_read_b64_tr_b16 a[212:213], v21 offset:24832\n"
"ds_read_b64_tr_b16 a[214:215], v21 offset:26368\n"
"v_mfma_f32_16x16x32_bf16 v[132:135], a[228:231], a[20:23], 0\n"
"v_xor_b32 v47, 96, v45\n"
"v_mfma_f32_16x16x32_bf16 v[132:135], a[224:227], a[4:7], v[132:135]\n"
"s_mov_b32 s25, 41216\n"
"s_add_u32 m0, s38, 4096\n"
"v_mfma_f32_16x16x32_bf16 v[136:139], a[224:227], a[8:11], 0\n"
"buffer_load_dwordx4 v36, s[52:55], s25 offen lds\n"
"v_mfma_f32_16x16x32_bf16 v[136:139], a[228:231], a[24:27], v[136:139]\n"
"v_add_u32 v7, s3, v47\n"
"v_mfma_f32_16x16x32_bf16 v[140:143], a[228:231], a[28:31], 0\n"
"v_add_u32 v11, 49152, v7\n"
"v_mfma_f32_16x16x32_bf16 v[140:143], a[224:227], a[12:15], v[140:143]\n"
"s_lshr_b32 s78, s94, 2\n"
"v_mfma_f32_16x16x32_bf16 v[144:147], a[232:235], a[0:3], 0\n"
"ds_read_b64_tr_b16 a[216:217], v21 offset:12552\n"
"ds_read_b64_tr_b16 a[218:219], v21 offset:14088\n"
"v_mfma_f32_16x16x32_bf16 v[144:147], a[236:239], a[16:19], v[144:147]\n"
"ds_read_b64_tr_b16 a[220:221], v21 offset:24840\n"
"ds_read_b64_tr_b16 a[222:223], v21 offset:26376\n"
"v_mfma_f32_16x16x32_bf16 v[148:151], a[236:239], a[20:23], 0\n"
"buffer_load_dwordx4 v37, s[52:55], s25 offen offset:1024 lds\n"
"v_mfma_f32_16x16x32_bf16 v[148:151], a[232:235], a[4:7], v[148:151]\n"
"s_and_b32 s79, s94, 3\n"
"v_mfma_f32_16x16x32_bf16 v[152:155], a[232:235], a[8:11], 0\n"
"s_sub_u32 s95, s78, 1\n"
"v_mfma_f32_16x16x32_bf16 v[152:155], a[236:239], a[24:27], v[152:155]\n"
"v_lshlrev_b32 v45, 5, v28\n"
"v_mfma_f32_16x16x32_bf16 v[156:159], a[236:239], a[28:31], 0\n"
"buffer_load_dwordx4 v38, s[52:55], s25 offen offset:2048 lds\n"
"v_mfma_f32_16x16x32_bf16 v[156:159], a[232:235], a[12:15], v[156:159]\n"
"s_waitcnt lgkmcnt(8)\n"
"v_mfma_f32_16x16x32_bf16 v[160:163], a[192:195], a[0:3], 0\n"
"ds_read_b64_tr_b16 a[224:225], v22 offset:12544\n"
"ds_read_b64_tr_b16 a[226:227], v22 offset:14080\n"
"v_mfma_f32_16x16x32_bf16 v[160:163], a[196:199], a[16:19], v[160:163]\n"
"ds_read_b64_tr_b16 a[228:229], v22 offset:24832\n"
"ds_read_b64_tr_b16 a[230:231], v22 offset:26368\n"
"v_mfma_f32_16x16x32_bf16 v[164:167], a[196:199], a[20:23], 0\n"
"s_waitcnt vmcnt(11)\n"
"ds_read_b128 a[32:35], v34 offset:0\n"
"v_mfma_f32_16x16x32_bf16 v[164:167], a[192:195], a[4:7], v[164:167]\n"
"ds_read_b128 a[48:51], v35 offset:0\n"
"ds_read_b128 a[36:39], v34 offset:2048\n"
"v_mfma_f32_16x16x32_bf16 v[168:171], a[192:195], a[8:11], 0\n"
"ds_read_b128 a[52:55], v35 offset:2048\n"
"ds_read_b128 a[40:43], v34 offset:4096\n"
"v_mfma_f32_16x16x32_bf16 v[168:171], a[196:199], a[24:27], v[168:171]\n"
"ds_read_b128 a[56:59], v35 offset:4096\n"
"ds_read_b128 a[44:47], v34 offset:6144\n"
"v_mfma_f32_16x16x32_bf16 v[172:175], a[196:199], a[28:31], 0\n"
"ds_read_b128 a[60:63], v35 offset:6144\n"
"buffer_load_dwordx4 v39, s[52:55], s25 offen offset:3072 lds\n"
"v_mfma_f32_16x16x32_bf16 v[172:175], a[192:195], a[12:15], v[172:175]\n"
"v_xor_b32 v15, v45, v18\n"
"v_mfma_f32_16x16x32_bf16 v[176:179], a[200:203], a[0:3], 0\n"
"ds_read_b64_tr_b16 a[232:233], v22 offset:12552\n"
"ds_read_b64_tr_b16 a[234:235], v22 offset:14088\n"
"v_mfma_f32_16x16x32_bf16 v[176:179], a[204:207], a[16:19], v[176:179]\n"
"ds_read_b64_tr_b16 a[236:237], v22 offset:24840\n"
"ds_read_b64_tr_b16 a[238:239], v22 offset:26376\n"
"v_mfma_f32_16x16x32_bf16 v[180:183], a[204:207], a[20:23], 0\n"
"s_mov_b32 s24, 1966080\n"
"s_add_u32 m0, s76, 61440\n"
"v_mfma_f32_16x16x32_bf16 v[180:183], a[200:203], a[4:7], v[180:183]\n"
"buffer_load_dwordx4 v23, s[48:51], s24 offen lds\n"
"v_mfma_f32_16x16x32_bf16 v[184:187], a[200:203], a[8:11], 0\n"
"v_bfe_u32 v45, v15, 5, 2\n"
"v_mfma_f32_16x16x32_bf16 v[184:187], a[204:207], a[24:27], v[184:187]\n"
"v_lshl_add_u32 v45, v51, 2, v45\n"
"v_mfma_f32_16x16x32_bf16 v[188:191], a[204:207], a[28:31], 0\n"
"v_add_u32 v46, s95, v45\n"
"v_mfma_f32_16x16x32_bf16 v[188:191], a[200:203], a[12:15], v[188:191]\n"
"s_waitcnt vmcnt(63) lgkmcnt(0)\n"
"s_barrier\n"
"v_mfma_f32_16x16x32_bf16 v[192:195], a[208:211], a[0:3], 0\n"
"ds_read_b64_tr_b16 a[192:193], v19 offset:49152\n"
"ds_read_b64_tr_b16 a[194:195], v19 offset:50688\n"
"v_mfma_f32_16x16x32_bf16 v[192:195], a[212:215], a[16:19], v[192:195]\n"
"ds_read_b64_tr_b16 a[196:197], v19 offset:0\n"
"ds_read_b64_tr_b16 a[198:199], v19 offset:1536\n"
"v_mfma_f32_16x16x32_bf16 v[196:199], a[212:215], a[20:23], 0\n"
"buffer_load_dwordx4 v24, s[48:51], s24 offen offset:1024 lds\n"
"v_mfma_f32_16x16x32_bf16 v[196:199], a[208:211], a[4:7], v[196:199]\n"
"v_and_b32 v46, 7, v46\n"
"v_mfma_f32_16x16x32_bf16 v[200:203], a[208:211], a[8:11], 0\n"
"v_add_u32 v28, 8, v45\n"
"v_mfma_f32_16x16x32_bf16 v[200:203], a[212:215], a[24:27], v[200:203]\n"
"v_cmp_gt_u32 vcc, 2, v45\n"
"v_mfma_f32_16x16x32_bf16 v[204:207], a[212:215], a[28:31], 0\n"
"buffer_load_dwordx4 v25, s[48:51], s24 offen offset:2048 lds\n"
"v_mfma_f32_16x16x32_bf16 v[204:207], a[208:211], a[12:15], v[204:207]\n"
"v_cndmask_b32 v46, v46, v28, vcc\n"
"v_mfma_f32_16x16x32_bf16 v[208:211], a[216:219], a[0:3], 0\n"
"ds_read_b64_tr_b16 a[200:201], v19 offset:49160\n"
"ds_read_b64_tr_b16 a[202:203], v19 offset:50696\n"
"v_mfma_f32_16x16x32_bf16 v[208:211], a[220:223], a[16:19], v[208:211]\n"
"ds_read_b64_tr_b16 a[204:205], v19 offset:8\n"
"ds_read_b64_tr_b16 a[206:207], v19 offset:1544\n"
"v_mfma_f32_16x16x32_bf16 v[212:215], a[220:223], a[20:23], 0\n"
"s_waitcnt lgkmcnt(15)\n"
"s_mov_b32 s25, 384\n"
"v_mfma_f32_16x16x32_bf16 v[212:215], a[216:219], a[4:7], v[212:215]\n"
"s_add_u32 m0, s39, 0\n"
"s_nop 0\n"
"buffer_load_dwordx4 v36, s[52:55], s25 offen lds\n"
"v_mfma_f32_16x16x32_bf16 v[216:219], a[216:219], a[8:11], 0\n"
"v_mul_u32_u24 v48, 1280, v48\n"
"v_mfma_f32_16x16x32_bf16 v[216:219], a[220:223], a[24:27], v[216:219]\n"
"v_lshl_add_u32 v46, v46, 5, v48\n"
"v_mfma_f32_16x16x32_bf16 v[220:223], a[220:223], a[28:31], 0\n"
"v_and_b32 v48, 16, v18\n"
"v_mfma_f32_16x16x32_bf16 v[220:223], a[216:219], a[12:15], v[220:223]\n"
"buffer_load_dwordx4 v37, s[52:55], s25 offen offset:1024 lds\n"
"v_mfma_f32_16x16x32_bf16 v[224:227], a[224:227], a[0:3], 0\n"
"ds_read_b64_tr_b16 a[208:209], v20 offset:49152\n"
"ds_read_b64_tr_b16 a[210:211], v20 offset:50688\n"
"v_mfma_f32_16x16x32_bf16 v[224:227], a[228:231], a[16:19], v[224:227]\n"
"ds_read_b64_tr_b16 a[212:213], v20 offset:0\n"
"ds_read_b64_tr_b16 a[214:215], v20 offset:1536\n"
"v_mfma_f32_16x16x32_bf16 v[228:231], a[228:231], a[20:23], 0\n"
"v_add_u32 v46, v46, v48\n"
"v_mfma_f32_16x16x32_bf16 v[228:231], a[224:227], a[4:7], v[228:231]\n"
"buffer_load_dwordx4 v38, s[52:55], s25 offen offset:2048 lds\n"
"v_mfma_f32_16x16x32_bf16 v[232:235], a[224:227], a[8:11], 0\n"
"v_cmp_gt_u32 vcc, 10, v45\n"
"v_mfma_f32_16x16x32_bf16 v[232:235], a[228:231], a[24:27], v[232:235]\n"
"s_nop 0\n"
"v_cndmask_b32 v56, 2.0, v46, vcc\n"
"v_mfma_f32_16x16x32_bf16 v[236:239], a[228:231], a[28:31], 0\n"
"v_lshlrev_b32 v45, 5, v29\n"
"v_mfma_f32_16x16x32_bf16 v[236:239], a[224:227], a[12:15], v[236:239]\n"
"buffer_load_dwordx4 v39, s[52:55], s25 offen offset:3072 lds\n"
"v_mfma_f32_16x16x32_bf16 v[240:243], a[232:235], a[0:3], 0\n"
"ds_read_b64_tr_b16 a[216:217], v20 offset:49160\n"
"ds_read_b64_tr_b16 a[218:219], v20 offset:50696\n"
"v_mfma_f32_16x16x32_bf16 v[240:243], a[236:239], a[16:19], v[240:243]\n"
"ds_read_b64_tr_b16 a[220:221], v20 offset:8\n"
"ds_read_b64_tr_b16 a[222:223], v20 offset:1544\n"
"v_mfma_f32_16x16x32_bf16 v[244:247], a[236:239], a[20:23], 0\n"
"v_xor_b32 v16, v45, v18\n"
"v_mfma_f32_16x16x32_bf16 v[244:247], a[232:235], a[4:7], v[244:247]\n"
"s_mov_b32 s25, 41344\n"
"s_add_u32 m0, s39, 4096\n"
"v_mfma_f32_16x16x32_bf16 v[248:251], a[232:235], a[8:11], 0\n"
"buffer_load_dwordx4 v36, s[52:55], s25 offen lds\n"
"v_mfma_f32_16x16x32_bf16 v[248:251], a[236:239], a[24:27], v[248:251]\n"
"v_bfe_u32 v45, v16, 5, 2\n"
"v_mfma_f32_16x16x32_bf16 v[252:255], a[236:239], a[28:31], 0\n"
"v_lshl_add_u32 v45, v52, 2, v45\n"
"v_mfma_f32_16x16x32_bf16 v[252:255], a[232:235], a[12:15], v[252:255]\n"
"s_waitcnt lgkmcnt(8)\n"
"v_mfma_f32_16x16x32_bf16 v[64:67], a[192:195], a[32:35], v[64:67]\n"
"ds_read_b64_tr_b16 a[224:225], v19 offset:49280\n"
"ds_read_b64_tr_b16 a[226:227], v19 offset:50816\n"
"v_mfma_f32_16x16x32_bf16 v[64:67], a[196:199], a[48:51], v[64:67]\n"
"ds_read_b64_tr_b16 a[228:229], v19 offset:128\n"
"ds_read_b64_tr_b16 a[230:231], v19 offset:1664\n"
"v_mfma_f32_16x16x32_bf16 v[68:71], a[196:199], a[52:55], v[68:71]\n"
"buffer_load_dwordx4 v37, s[52:55], s25 offen offset:1024 lds\n"
"v_mfma_f32_16x16x32_bf16 v[68:71], a[192:195], a[36:39], v[68:71]\n"
"v_add_u32 v46, s95, v45\n"
"v_mfma_f32_16x16x32_bf16 v[72:75], a[192:195], a[40:43], v[72:75]\n"
"v_and_b32 v46, 7, v46\n"
"v_mfma_f32_16x16x32_bf16 v[72:75], a[196:199], a[56:59], v[72:75]\n"
"v_add_u32 v29, 8, v45\n"
"v_mfma_f32_16x16x32_bf16 v[76:79], a[196:199], a[60:63], v[76:79]\n"
"buffer_load_dwordx4 v38, s[52:55], s25 offen offset:2048 lds\n"
"v_mfma_f32_16x16x32_bf16 v[76:79], a[192:195], a[44:47], v[76:79]\n"
"v_cmp_gt_u32 vcc, 2, v45\n"
"v_mfma_f32_16x16x32_bf16 v[80:83], a[200:203], a[32:35], v[80:83]\n"
"ds_read_b64_tr_b16 a[232:233], v19 offset:49288\n"
"ds_read_b64_tr_b16 a[234:235], v19 offset:50824\n"
"v_mfma_f32_16x16x32_bf16 v[80:83], a[204:207], a[48:51], v[80:83]\n"
"ds_read_b64_tr_b16 a[236:237], v19 offset:136\n"
"ds_read_b64_tr_b16 a[238:239], v19 offset:1672\n"
"v_mfma_f32_16x16x32_bf16 v[84:87], a[204:207], a[52:55], v[84:87]\n"
"buffer_load_dwordx4 v39, s[52:55], s25 offen offset:3072 lds\n"
"v_mfma_f32_16x16x32_bf16 v[84:87], a[200:203], a[36:39], v[84:87]\n"
"v_cndmask_b32 v46, v46, v29, vcc\n"
"v_mfma_f32_16x16x32_bf16 v[88:91], a[200:203], a[40:43], v[88:91]\n"
"v_mul_u32_u24 v49, 1280, v49\n"
"v_mfma_f32_16x16x32_bf16 v[88:91], a[204:207], a[56:59], v[88:91]\n"
"v_lshl_add_u32 v46, v46, 5, v49\n"
"v_mfma_f32_16x16x32_bf16 v[92:95], a[204:207], a[60:63], v[92:95]\n"
"s_mov_b32 s24, 2359296\n"
"s_add_u32 m0, s76, 122880\n"
"v_mfma_f32_16x16x32_bf16 v[92:95], a[200:203], a[44:47], v[92:95]\n"
"s_waitcnt lgkmcnt(8)\n"
"buffer_load_dwordx4 v23, s[48:51], s24 offen lds\n"
"v_mfma_f32_16x16x32_bf16 v[96:99], a[208:211], a[32:35], v[96:99]\n"
"ds_read_b64_tr_b16 a[192:193], v20 offset:49280\n"
"ds_read_b64_tr_b16 a[194:195], v20 offset:50816\n"
"v_mfma_f32_16x16x32_bf16 v[96:99], a[212:215], a[48:51], v[96:99]\n"
"ds_read_b64_tr_b16 a[196:197], v20 offset:128\n"
"ds_read_b64_tr_b16 a[198:199], v20 offset:1664\n"
"v_mfma_f32_16x16x32_bf16 v[100:103], a[212:215], a[52:55], v[100:103]\n"
"v_and_b32 v49, 16, v18\n"
"v_mfma_f32_16x16x32_bf16 v[100:103], a[208:211], a[36:39], v[100:103]\n"
"buffer_load_dwordx4 v24, s[48:51], s24 offen offset:1024 lds\n"
"v_mfma_f32_16x16x32_bf16 v[104:107], a[208:211], a[40:43], v[104:107]\n"
"v_add_u32 v49, 4294966272, v49\n"
"v_mfma_f32_16x16x32_bf16 v[104:107], a[212:215], a[56:59], v[104:107]\n"
"v_add_u32 v46, v46, v49\n"
"v_mfma_f32_16x16x32_bf16 v[108:111], a[212:215], a[60:63], v[108:111]\n"
"v_cmp_gt_u32 vcc, 10, v45\n"
"v_mfma_f32_16x16x32_bf16 v[108:111], a[208:211], a[44:47], v[108:111]\n"
"buffer_load_dwordx4 v25, s[48:51], s24 offen offset:2048 lds\n"
"v_mfma_f32_16x16x32_bf16 v[112:115], a[216:219], a[32:35], v[112:115]\n"
"ds_read_b64_tr_b16 a[200:201], v20 offset:49288\n"
"ds_read_b64_tr_b16 a[202:203], v20 offset:50824\n"
"v_mfma_f32_16x16x32_bf16 v[112:115], a[220:223], a[48:51], v[112:115]\n"
"ds_read_b64_tr_b16 a[204:205], v20 offset:136\n"
"ds_read_b64_tr_b16 a[206:207], v20 offset:1672\n"
"v_mfma_f32_16x16x32_bf16 v[116:119], a[220:223], a[52:55], v[116:119]\n"
"v_cndmask_b32 v57, 2.0, v46, vcc\n"
"v_mfma_f32_16x16x32_bf16 v[116:119], a[216:219], a[36:39], v[116:119]\n"
"s_mov_b32 s24, 2752512\n"
"s_add_u32 m0, s76, 135168\n"
"v_mfma_f32_16x16x32_bf16 v[120:123], a[216:219], a[40:43], v[120:123]\n"
"buffer_load_dwordx4 v23, s[48:51], s24 offen lds\n"
"v_mfma_f32_16x16x32_bf16 v[120:123], a[220:223], a[56:59], v[120:123]\n"
"v_lshlrev_b32 v45, 5, v30\n"
"v_mfma_f32_16x16x32_bf16 v[124:127], a[220:223], a[60:63], v[124:127]\n"
"v_xor_b32 v17, v45, v18\n"
"v_mfma_f32_16x16x32_bf16 v[124:127], a[216:219], a[44:47], v[124:127]\n"
"s_waitcnt lgkmcnt(8)\n"
"v_mfma_f32_16x16x32_bf16 v[128:131], a[224:227], a[32:35], v[128:131]\n"
"ds_read_b64_tr_b16 a[208:209], v19 offset:49408\n"
"ds_read_b64_tr_b16 a[210:211], v19 offset:50944\n"
"v_mfma_f32_16x16x32_bf16 v[128:131], a[228:231], a[48:51], v[128:131]\n"
"ds_read_b64_tr_b16 a[212:213], v19 offset:256\n"
"ds_read_b64_tr_b16 a[214:215], v19 offset:1792\n"
"v_mfma_f32_16x16x32_bf16 v[132:135], a[228:231], a[52:55], v[132:135]\n"
"s_waitcnt vmcnt(15)\n"
"ds_read_b128 a[64:67], v32 offset:0\n"
"v_mfma_f32_16x16x32_bf16 v[132:135], a[224:227], a[36:39], v[132:135]\n"
"ds_read_b128 a[80:83], v33 offset:0\n"
"ds_read_b128 a[68:71], v32 offset:2048\n"
"v_mfma_f32_16x16x32_bf16 v[136:139], a[224:227], a[40:43], v[136:139]\n"
"ds_read_b128 a[84:87], v33 offset:2048\n"
"ds_read_b128 a[72:75], v32 offset:4096\n"
"v_mfma_f32_16x16x32_bf16 v[136:139], a[228:231], a[56:59], v[136:139]\n"
"ds_read_b128 a[88:91], v33 offset:4096\n"
"ds_read_b128 a[76:79], v32 offset:6144\n"
"v_mfma_f32_16x16x32_bf16 v[140:143], a[228:231], a[60:63], v[140:143]\n"
"ds_read_b128 a[92:95], v33 offset:6144\n"
"buffer_load_dwordx4 v24, s[48:51], s24 offen offset:1024 lds\n"
"v_mfma_f32_16x16x32_bf16 v[140:143], a[224:227], a[44:47], v[140:143]\n"
"v_bfe_u32 v45, v17, 5, 2\n"
"v_mfma_f32_16x16x32_bf16 v[144:147], a[232:235], a[32:35], v[144:147]\n"
"ds_read_b64_tr_b16 a[216:217], v19 offset:49416\n"
"ds_read_b64_tr_b16 a[218:219], v19 offset:50952\n"
"v_mfma_f32_16x16x32_bf16 v[144:147], a[236:239], a[48:51], v[144:147]\n"
"ds_read_b64_tr_b16 a[220:221], v19 offset:264\n"
"ds_read_b64_tr_b16 a[222:223], v19 offset:1800\n"
"v_mfma_f32_16x16x32_bf16 v[148:151], a[236:239], a[52:55], v[148:151]\n"
"buffer_load_dwordx4 v25, s[48:51], s24 offen offset:2048 lds\n"
"v_mfma_f32_16x16x32_bf16 v[148:151], a[232:235], a[36:39], v[148:151]\n"
"v_lshl_add_u32 v45, v53, 2, v45\n"
"v_mfma_f32_16x16x32_bf16 v[152:155], a[232:235], a[40:43], v[152:155]\n"
"v_add_u32 v46, s95, v45\n"
"v_mfma_f32_16x16x32_bf16 v[152:155], a[236:239], a[56:59], v[152:155]\n"
"v_and_b32 v46, 7, v46\n"
"v_mfma_f32_16x16x32_bf16 v[156:159], a[236:239], a[60:63], v[156:159]\n"
"s_waitcnt lgkmcnt(4)\n"
"s_mov_b32 s25, 512\n"
"v_mfma_f32_16x16x32_bf16 v[156:159], a[232:235], a[44:47], v[156:159]\n"
"s_waitcnt lgkmcnt(15)\n"
"s_add_u32 m0, s38, 0\n"
"v_mfma_f32_16x16x32_bf16 v[160:163], a[192:195], a[32:35], v[160:163]\n"
"ds_read_b64_tr_b16 a[224:225], v20 offset:49408\n"
"ds_read_b64_tr_b16 a[226:227], v20 offset:50944\n"
"v_mfma_f32_16x16x32_bf16 v[160:163], a[196:199], a[48:51], v[160:163]\n"
"ds_read_b64_tr_b16 a[228:229], v20 offset:256\n"
"ds_read_b64_tr_b16 a[230:231], v20 offset:1792\n"
"v_mfma_f32_16x16x32_bf16 v[164:167], a[196:199], a[52:55], v[164:167]\n"
"buffer_load_dwordx4 v36, s[52:55], s25 offen lds\n"
"v_mfma_f32_16x16x32_bf16 v[164:167], a[192:195], a[36:39], v[164:167]\n"
"v_add_u32 v30, 8, v45\n"
"v_mfma_f32_16x16x32_bf16 v[168:171], a[192:195], a[40:43], v[168:171]\n"
"v_cmp_gt_u32 vcc, 2, v45\n"
"v_mfma_f32_16x16x32_bf16 v[168:171], a[196:199], a[56:59], v[168:171]\n"
"s_nop 0\n"
"v_cndmask_b32 v46, v46, v30, vcc\n"
"v_mfma_f32_16x16x32_bf16 v[172:175], a[196:199], a[60:63], v[172:175]\n"
"buffer_load_dwordx4 v37, s[52:55], s25 offen offset:1024 lds\n"
"v_mfma_f32_16x16x32_bf16 v[172:175], a[192:195], a[44:47], v[172:175]\n"
"v_mul_u32_u24 v50, 1280, v50\n"
"v_mfma_f32_16x16x32_bf16 v[176:179], a[200:203], a[32:35], v[176:179]\n"
"ds_read_b64_tr_b16 a[232:233], v20 offset:49416\n"
"ds_read_b64_tr_b16 a[234:235], v20 offset:50952\n"
"v_mfma_f32_16x16x32_bf16 v[176:179], a[204:207], a[48:51], v[176:179]\n"
"ds_read_b64_tr_b16 a[236:237], v20 offset:264\n"
"ds_read_b64_tr_b16 a[238:239], v20 offset:1800\n"
"v_mfma_f32_16x16x32_bf16 v[180:183], a[204:207], a[52:55], v[180:183]\n"
"buffer_load_dwordx4 v38, s[52:55], s25 offen offset:2048 lds\n"
"v_mfma_f32_16x16x32_bf16 v[180:183], a[200:203], a[36:39], v[180:183]\n"
"v_lshl_add_u32 v46, v46, 5, v50\n"
"v_mfma_f32_16x16x32_bf16 v[184:187], a[200:203], a[40:43], v[184:187]\n"
"v_and_b32 v50, 16, v18\n"
"v_mfma_f32_16x16x32_bf16 v[184:187], a[204:207], a[56:59], v[184:187]\n"
"v_add_u32 v50, 4294965248, v50\n"
"v_mfma_f32_16x16x32_bf16 v[188:191], a[204:207], a[60:63], v[188:191]\n"
"buffer_load_dwordx4 v39, s[52:55], s25 offen offset:3072 lds\n"
"v_mfma_f32_16x16x32_bf16 v[188:191], a[200:203], a[44:47], v[188:191]\n"
"s_waitcnt vmcnt(18) lgkmcnt(0)\n"
"s_barrier\n"
"v_mfma_f32_16x16x32_bf16 v[192:195], a[208:211], a[32:35], v[192:195]\n"
"ds_read_b64_tr_b16 a[192:193], v21 offset:0\n"
"ds_read_b64_tr_b16 a[194:195], v21 offset:1536\n"
"v_mfma_f32_16x16x32_bf16 v[192:195], a[212:215], a[48:51], v[192:195]\n"
"ds_read_b64_tr_b16 a[196:197], v19 offset:12288\n"
"ds_read_b64_tr_b16 a[198:199], v19 offset:13824\n"
"v_mfma_f32_16x16x32_bf16 v[196:199], a[212:215], a[52:55], v[196:199]\n"
"s_mov_b32 s25, 41472\n"
"s_add_u32 m0, s38, 4096\n"
"v_mfma_f32_16x16x32_bf16 v[196:199], a[208:211], a[36:39], v[196:199]\n"
"buffer_load_dwordx4 v36, s[52:55], s25 offen lds\n"
"v_mfma_f32_16x16x32_bf16 v[200:203], a[208:211], a[40:43], v[200:203]\n"
"v_add_u32 v46, v46, v50\n"
"v_mfma_f32_16x16x32_bf16 v[200:203], a[212:215], a[56:59], v[200:203]\n"
"v_cmp_gt_u32 vcc, 10, v45\n"
"v_mfma_f32_16x16x32_bf16 v[204:207], a[212:215], a[60:63], v[204:207]\n"
"s_nop 0\n"
"v_cndmask_b32 v58, 2.0, v46, vcc\n"
"v_mfma_f32_16x16x32_bf16 v[204:207], a[208:211], a[44:47], v[204:207]\n"
"buffer_load_dwordx4 v37, s[52:55], s25 offen offset:1024 lds\n"
"v_mfma_f32_16x16x32_bf16 v[208:211], a[216:219], a[32:35], v[208:211]\n"
"ds_read_b64_tr_b16 a[200:201], v21 offset:8\n"
"ds_read_b64_tr_b16 a[202:203], v21 offset:1544\n"
"v_mfma_f32_16x16x32_bf16 v[208:211], a[220:223], a[48:51], v[208:211]\n"
"ds_read_b64_tr_b16 a[204:205], v19 offset:12296\n"
"ds_read_b64_tr_b16 a[206:207], v19 offset:13832\n"
"v_mfma_f32_16x16x32_bf16 v[212:215], a[220:223], a[52:55], v[212:215]\n"
"v_mul_u32_u24 v45, 1280, v44\n"
"v_mfma_f32_16x16x32_bf16 v[212:215], a[216:219], a[36:39], v[212:215]\n"
"buffer_load_dwordx4 v38, s[52:55], s25 offen offset:2048 lds\n"
"v_mfma_f32_16x16x32_bf16 v[216:219], a[216:219], a[40:43], v[216:219]\n"
"v_lshl_add_u32 v59, v43, 3, v45\n"
"v_mfma_f32_16x16x32_bf16 v[216:219], a[220:223], a[56:59], v[216:219]\n"
"v_lshlrev_b32 v45, 7, v40\n"
"v_mfma_f32_16x16x32_bf16 v[220:223], a[220:223], a[60:63], v[220:223]\n"
"v_cmp_gt_u32 vcc, 30, v40\n"
"v_mfma_f32_16x16x32_bf16 v[220:223], a[216:219], a[44:47], v[220:223]\n"
"buffer_load_dwordx4 v39, s[52:55], s25 offen offset:3072 lds\n"
"v_mfma_f32_16x16x32_bf16 v[224:227], a[224:227], a[32:35], v[224:227]\n"
"ds_read_b64_tr_b16 a[208:209], v22 offset:0\n"
"ds_read_b64_tr_b16 a[210:211], v22 offset:1536\n"
"v_mfma_f32_16x16x32_bf16 v[224:227], a[228:231], a[48:51], v[224:227]\n"
"ds_read_b64_tr_b16 a[212:213], v20 offset:12288\n"
"ds_read_b64_tr_b16 a[214:215], v20 offset:13824\n"
"v_mfma_f32_16x16x32_bf16 v[228:231], a[228:231], a[52:55], v[228:231]\n"
"v_cndmask_b32 v26, 2.0, v45, vcc\n"
"v_mfma_f32_16x16x32_bf16 v[228:231], a[224:227], a[36:39], v[228:231]\n"
"s_mov_b32 s24, 3145728\n"
"s_add_u32 m0, s76, 98304\n"
"v_mfma_f32_16x16x32_bf16 v[232:235], a[224:227], a[40:43], v[232:235]\n"
"buffer_load_dwordx4 v23, s[48:51], s24 offen lds\n"
"v_mfma_f32_16x16x32_bf16 v[232:235], a[228:231], a[56:59], v[232:235]\n"
"v_sub_u32 v46, s78, v40\n"
"v_mfma_f32_16x16x32_bf16 v[236:239], a[228:231], a[60:63], v[236:239]\n"
"v_and_b32 v46, 7, v46\n"
"v_mfma_f32_16x16x32_bf16 v[236:239], a[224:227], a[44:47], v[236:239]\n"
"v_lshl_add_u32 v46, v46, 2, s79\n"
"v_mfma_f32_16x16x32_bf16 v[240:243], a[232:235], a[32:35], v[240:243]\n"
"ds_read_b64_tr_b16 a[216:217], v22 offset:8\n"
"ds_read_b64_tr_b16 a[218:219], v22 offset:1544\n"
"v_mfma_f32_16x16x32_bf16 v[240:243], a[236:239], a[48:51], v[240:243]\n"
"ds_read_b64_tr_b16 a[220:221], v20 offset:12296\n"
"ds_read_b64_tr_b16 a[222:223], v20 offset:13832\n"
"v_mfma_f32_16x16x32_bf16 v[244:247], a[236:239], a[52:55], v[244:247]\n"
"buffer_load_dwordx4 v24, s[48:51], s24 offen offset:1024 lds\n"
"v_mfma_f32_16x16x32_bf16 v[244:247], a[232:235], a[36:39], v[244:247]\n"
"v_lshrrev_b32 v45, 3, v40\n"
"v_mfma_f32_16x16x32_bf16 v[248:251], a[232:235], a[40:43], v[248:251]\n"
"v_lshlrev_b32 v45, 15, v45\n"
"v_mfma_f32_16x16x32_bf16 v[248:251], a[236:239], a[56:59], v[248:251]\n"
"v_lshl_add_u32 v46, v46, 7, v45\n"
"v_mfma_f32_16x16x32_bf16 v[252:255], a[236:239], a[60:63], v[252:255]\n"
"buffer_load_dwordx4 v25, s[48:51], s24 offen offset:2048 lds\n"
"v_mfma_f32_16x16x32_bf16 v[252:255], a[232:235], a[44:47], v[252:255]\n"
"s_waitcnt lgkmcnt(8)\n"
"v_mfma_f32_16x16x32_bf16 v[64:67], a[192:195], a[64:67], v[64:67]\n"
"ds_read_b64_tr_b16 a[224:225], v21 offset:128\n"
"ds_read_b64_tr_b16 a[226:227], v21 offset:1664\n"
"v_mfma_f32_16x16x32_bf16 v[64:67], a[196:199], a[80:83], v[64:67]\n"
"ds_read_b64_tr_b16 a[228:229], v19 offset:12416\n"
"ds_read_b64_tr_b16 a[230:231], v19 offset:13952\n"
"v_mfma_f32_16x16x32_bf16 v[68:71], a[196:199], a[84:87], v[68:71]\n"
"s_mov_b32 s24, 3538944\n"
"s_add_u32 m0, s76, 49152\n"
"v_mfma_f32_16x16x32_bf16 v[68:71], a[192:195], a[68:71], v[68:71]\n"
"buffer_load_dwordx4 v23, s[48:51], s24 offen lds\n"
"v_mfma_f32_16x16x32_bf16 v[72:75], a[192:195], a[72:75], v[72:75]\n"
"s_and_b32 s3, s94, 28\n"
"v_mfma_f32_16x16x32_bf16 v[72:75], a[196:199], a[88:91], v[72:75]\n"
"v_and_b32 v47, 3, v40\n"
"v_mfma_f32_16x16x32_bf16 v[76:79], a[196:199], a[92:95], v[76:79]\n"
"v_add_u32 v47, s3, v47\n"
"v_mfma_f32_16x16x32_bf16 v[76:79], a[192:195], a[76:79], v[76:79]\n"
"buffer_load_dwordx4 v24, s[48:51], s24 offen offset:1024 lds\n"
"v_mfma_f32_16x16x32_bf16 v[80:83], a[200:203], a[64:67], v[80:83]\n"
"ds_read_b64_tr_b16 a[232:233], v21 offset:136\n"
"ds_read_b64_tr_b16 a[234:235], v21 offset:1672\n"
"v_mfma_f32_16x16x32_bf16 v[80:83], a[204:207], a[80:83], v[80:83]\n"
"ds_read_b64_tr_b16 a[236:237], v19 offset:12424\n"
"ds_read_b64_tr_b16 a[238:239], v19 offset:13960\n"
"v_mfma_f32_16x16x32_bf16 v[84:87], a[204:207], a[84:87], v[84:87]\n"
"v_and_b32 v45, 12, v40\n"
"v_mfma_f32_16x16x32_bf16 v[84:87], a[200:203], a[68:71], v[84:87]\n"
"buffer_load_dwordx4 v25, s[48:51], s24 offen offset:2048 lds\n"
"v_mfma_f32_16x16x32_bf16 v[88:91], a[200:203], a[72:75], v[88:91]\n"
"v_lshlrev_b32 v45, 3, v45\n"
"v_mfma_f32_16x16x32_bf16 v[88:91], a[204:207], a[88:91], v[88:91]\n"
"v_lshl_add_u32 v47, v47, 7, v45\n"
"v_mfma_f32_16x16x32_bf16 v[92:95], a[204:207], a[92:95], v[92:95]\n"
"v_cmp_gt_u32 vcc, 16, v40\n"
"v_mfma_f32_16x16x32_bf16 v[92:95], a[200:203], a[76:79], v[92:95]\n"
"s_waitcnt lgkmcnt(8)\n"
"s_mov_b32 s24, 0\n"
"v_mfma_f32_16x16x32_bf16 v[96:99], a[208:211], a[64:67], v[96:99]\n"
"ds_read_b64_tr_b16 a[192:193], v22 offset:128\n"
"ds_read_b64_tr_b16 a[194:195], v22 offset:1664\n"
"v_mfma_f32_16x16x32_bf16 v[96:99], a[212:215], a[80:83], v[96:99]\n"
"ds_read_b64_tr_b16 a[196:197], v20 offset:12416\n"
"ds_read_b64_tr_b16 a[198:199], v20 offset:13952\n"
"v_mfma_f32_16x16x32_bf16 v[100:103], a[212:215], a[84:87], v[100:103]\n"
"s_waitcnt vmcnt(20)\n"
"ds_read_b128 a[96:99], v34 offset:0\n"
"v_mfma_f32_16x16x32_bf16 v[100:103], a[208:211], a[68:71], v[100:103]\n"
"ds_read_b128 a[112:115], v35 offset:0\n"
"ds_read_b128 a[100:103], v34 offset:2048\n"
"v_mfma_f32_16x16x32_bf16 v[104:107], a[208:211], a[72:75], v[104:107]\n"
"ds_read_b128 a[116:119], v35 offset:2048\n"
"ds_read_b128 a[104:107], v34 offset:4096\n"
"v_mfma_f32_16x16x32_bf16 v[104:107], a[212:215], a[88:91], v[104:107]\n"
"ds_read_b128 a[120:123], v35 offset:4096\n"
"ds_read_b128 a[108:111], v34 offset:6144\n"
"v_mfma_f32_16x16x32_bf16 v[108:111], a[212:215], a[92:95], v[108:111]\n"
"ds_read_b128 a[124:127], v35 offset:6144\n"
"s_add_u32 m0, s76, 0\n"
"v_mfma_f32_16x16x32_bf16 v[108:111], a[208:211], a[76:79], v[108:111]\n"
"buffer_load_dwordx4 v12, s[40:43], s24 offen nt lds\n"
"v_mfma_f32_16x16x32_bf16 v[112:115], a[216:219], a[64:67], v[112:115]\n"
"ds_read_b64_tr_b16 a[200:201], v22 offset:136\n"
"ds_read_b64_tr_b16 a[202:203], v22 offset:1672\n"
"v_mfma_f32_16x16x32_bf16 v[112:115], a[220:223], a[80:83], v[112:115]\n"
"ds_read_b64_tr_b16 a[204:205], v20 offset:12424\n"
"ds_read_b64_tr_b16 a[206:207], v20 offset:13960\n"
"v_mfma_f32_16x16x32_bf16 v[116:119], a[220:223], a[84:87], v[116:119]\n"
"v_cndmask_b32 v46, v46, v47, vcc\n"
"v_mfma_f32_16x16x32_bf16 v[116:119], a[216:219], a[68:71], v[116:119]\n"
"s_lshl_b32 s27, s93, 12\n"
"v_mfma_f32_16x16x32_bf16 v[120:123], a[216:219], a[72:75], v[120:123]\n"
"s_waitcnt lgkmcnt(4)\n"
"s_mov_b32 s25, 640\n"
"v_mfma_f32_16x16x32_bf16 v[120:123], a[220:223], a[88:91], v[120:123]\n"
"s_add_u32 m0, s39, 0\n"
"s_nop 0\n"
"buffer_load_dwordx4 v36, s[52:55], s25 offen lds\n"
"v_mfma_f32_16x16x32_bf16 v[124:127], a[220:223], a[92:95], v[124:127]\n"
"v_add_u32 v60, s27, v46\n"
"v_mfma_f32_16x16x32_bf16 v[124:127], a[216:219], a[76:79], v[124:127]\n"
"s_waitcnt lgkmcnt(15)\n"
"v_mfma_f32_16x16x32_bf16 v[128:131], a[224:227], a[64:67], v[128:131]\n"
"ds_read_b64_tr_b16 a[208:209], v21 offset:256\n"
"ds_read_b64_tr_b16 a[210:211], v21 offset:1792\n"
"v_mfma_f32_16x16x32_bf16 v[128:131], a[228:231], a[80:83], v[128:131]\n"
"ds_read_b64_tr_b16 a[212:213], v19 offset:12544\n"
"ds_read_b64_tr_b16 a[214:215], v19 offset:14080\n"
"v_mfma_f32_16x16x32_bf16 v[132:135], a[228:231], a[84:87], v[132:135]\n"
"buffer_load_dwordx4 v37, s[52:55], s25 offen offset:1024 lds\n"
"v_mfma_f32_16x16x32_bf16 v[132:135], a[224:227], a[68:71], v[132:135]\n"
"s_add_u32 s72, s20, 100663296\n"
"s_addc_u32 s73, s21, 0\n"
"v_mfma_f32_16x16x32_bf16 v[136:139], a[224:227], a[72:75], v[136:139]\n"
"s_mov_b32 s74, 98304\n"
"v_mfma_f32_16x16x32_bf16 v[136:139], a[228:231], a[88:91], v[136:139]\n"
"s_mov_b32 s75, 131072\n"
"v_mfma_f32_16x16x32_bf16 v[140:143], a[228:231], a[92:95], v[140:143]\n"
"buffer_load_dwordx4 v38, s[52:55], s25 offen offset:2048 lds\n"
"v_mfma_f32_16x16x32_bf16 v[140:143], a[224:227], a[76:79], v[140:143]\n"
"buffer_load_dword v61, v60, s[72:75], 0 offen sc1\n"
"v_mfma_f32_16x16x32_bf16 v[144:147], a[232:235], a[64:67], v[144:147]\n"
"ds_read_b64_tr_b16 a[216:217], v21 offset:264\n"
"ds_read_b64_tr_b16 a[218:219], v21 offset:1800\n"
"v_mfma_f32_16x16x32_bf16 v[144:147], a[236:239], a[80:83], v[144:147]\n"
"ds_read_b64_tr_b16 a[220:221], v19 offset:12552\n"
"ds_read_b64_tr_b16 a[222:223], v19 offset:14088\n"
"v_mfma_f32_16x16x32_bf16 v[148:151], a[236:239], a[84:87], v[148:151]\n"
"buffer_load_dwordx4 v39, s[52:55], s25 offen offset:3072 lds\n"
"v_mfma_f32_16x16x32_bf16 v[148:151], a[232:235], a[68:71], v[148:151]\n"
"s_add_u32 s3, s3, s33\n"
"v_mfma_f32_16x16x32_bf16 v[152:155], a[232:235], a[72:75], v[152:155]\n"
"s_mul_i32 s3, s3, 98304\n"
"v_mfma_f32_16x16x32_bf16 v[152:155], a[236:239], a[88:91], v[152:155]\n"
"s_mul_i32 s27, s77, 24576\n"
"v_mfma_f32_16x16x32_bf16 v[156:159], a[236:239], a[92:95], v[156:159]\n"
"s_mov_b32 s25, 41600\n"
"s_add_u32 m0, s39, 4096\n"
"v_mfma_f32_16x16x32_bf16 v[156:159], a[232:235], a[76:79], v[156:159]\n"
"s_waitcnt lgkmcnt(8)\n"
"buffer_load_dwordx4 v36, s[52:55], s25 offen lds\n"
"v_mfma_f32_16x16x32_bf16 v[160:163], a[192:195], a[64:67], v[160:163]\n"
"ds_read_b64_tr_b16 a[224:225], v22 offset:256\n"
"ds_read_b64_tr_b16 a[226:227], v22 offset:1792\n"
"v_mfma_f32_16x16x32_bf16 v[160:163], a[196:199], a[80:83], v[160:163]\n"
"ds_read_b64_tr_b16 a[228:229], v20 offset:12544\n"
"ds_read_b64_tr_b16 a[230:231], v20 offset:14080\n"
"v_mfma_f32_16x16x32_bf16 v[164:167], a[196:199], a[84:87], v[164:167]\n"
"s_add_u32 s3, s3, s27\n"
"v_mfma_f32_16x16x32_bf16 v[164:167], a[192:195], a[68:71], v[164:167]\n"
"buffer_load_dwordx4 v37, s[52:55], s25 offen offset:1024 lds\n"
"v_mfma_f32_16x16x32_bf16 v[168:171], a[192:195], a[72:75], v[168:171]\n"
"s_add_u32 s3, s3, 0\n"
"v_mfma_f32_16x16x32_bf16 v[168:171], a[196:199], a[88:91], v[168:171]\n"
"s_sub_u32 s27, 393216, s27\n"
"v_mfma_f32_16x16x32_bf16 v[172:175], a[196:199], a[92:95], v[172:175]\n"
"s_add_u32 s56, s20, s3\n"
"s_addc_u32 s57, s21, 0\n"
"v_mfma_f32_16x16x32_bf16 v[172:175], a[192:195], a[76:79], v[172:175]\n"
"buffer_load_dwordx4 v38, s[52:55], s25 offen offset:2048 lds\n"
"v_mfma_f32_16x16x32_bf16 v[176:179], a[200:203], a[64:67], v[176:179]\n"
"ds_read_b64_tr_b16 a[232:233], v22 offset:264\n"
"ds_read_b64_tr_b16 a[234:235], v22 offset:1800\n"
"v_mfma_f32_16x16x32_bf16 v[176:179], a[204:207], a[80:83], v[176:179]\n"
"ds_read_b64_tr_b16 a[236:237], v20 offset:12552\n"
"ds_read_b64_tr_b16 a[238:239], v20 offset:14088\n"
"v_mfma_f32_16x16x32_bf16 v[180:183], a[204:207], a[84:87], v[180:183]\n"
"s_mov_b32 s58, s27\n"
"v_mfma_f32_16x16x32_bf16 v[180:183], a[200:203], a[68:71], v[180:183]\n"
"buffer_load_dwordx4 v39, s[52:55], s25 offen offset:3072 lds\n"
"v_mfma_f32_16x16x32_bf16 v[184:187], a[200:203], a[72:75], v[184:187]\n"
"s_mov_b32 s59, 131072\n"
"v_mfma_f32_16x16x32_bf16 v[184:187], a[204:207], a[88:91], v[184:187]\n"
"s_mul_i32 s33, s79, 320\n"
"v_mfma_f32_16x16x32_bf16 v[188:191], a[204:207], a[92:95], v[188:191]\n"
"s_mul_i32 s27, s77, 10240\n"
"v_mfma_f32_16x16x32_bf16 v[188:191], a[200:203], a[76:79], v[188:191]\n"
"s_waitcnt vmcnt(24) lgkmcnt(0)\n"
"s_barrier\n"
"v_mfma_f32_16x16x32_bf16 v[192:195], a[208:211], a[64:67], v[192:195]\n"
"ds_read_b64_tr_b16 a[192:193], v21 offset:12288\n"
"ds_read_b64_tr_b16 a[194:195], v21 offset:13824\n"
"v_mfma_f32_16x16x32_bf16 v[192:195], a[212:215], a[80:83], v[192:195]\n"
"ds_read_b64_tr_b16 a[196:197], v21 offset:24576\n"
"ds_read_b64_tr_b16 a[198:199], v21 offset:26112\n"
"v_mfma_f32_16x16x32_bf16 v[196:199], a[212:215], a[84:87], v[196:199]\n"
"s_mov_b32 s24, 0\n"
"s_add_u32 m0, s76, 0\n"
"v_mfma_f32_16x16x32_bf16 v[196:199], a[208:211], a[68:71], v[196:199]\n"
"buffer_load_dwordx4 v13, s[40:43], s24 offen offset:1024 nt lds\n"
"v_mfma_f32_16x16x32_bf16 v[200:203], a[208:211], a[72:75], v[200:203]\n"
"s_add_u32 s27, s27, s33\n"
"v_mfma_f32_16x16x32_bf16 v[200:203], a[212:215], a[88:91], v[200:203]\n"
"s_mul_i32 s3, s78, 983040\n"
"v_mfma_f32_16x16x32_bf16 v[204:207], a[212:215], a[92:95], v[204:207]\n"
"s_add_u32 s3, s3, s27\n"
"v_mfma_f32_16x16x32_bf16 v[204:207], a[208:211], a[76:79], v[204:207]\n"
"s_mov_b32 s24, 3932160\n"
"s_add_u32 m0, s76, 110592\n"
"v_mfma_f32_16x16x32_bf16 v[208:211], a[216:219], a[64:67], v[208:211]\n"
"ds_read_b64_tr_b16 a[200:201], v21 offset:12296\n"
"ds_read_b64_tr_b16 a[202:203], v21 offset:13832\n"
"v_mfma_f32_16x16x32_bf16 v[208:211], a[220:223], a[80:83], v[208:211]\n"
"ds_read_b64_tr_b16 a[204:205], v21 offset:24584\n"
"ds_read_b64_tr_b16 a[206:207], v21 offset:26120\n"
"v_mfma_f32_16x16x32_bf16 v[212:215], a[220:223], a[84:87], v[212:215]\n"
"buffer_load_dwordx4 v23, s[48:51], s24 offen lds\n"
"v_mfma_f32_16x16x32_bf16 v[212:215], a[216:219], a[68:71], v[212:215]\n"
"s_sub_u32 s27, 983040, s27\n"
"v_mfma_f32_16x16x32_bf16 v[216:219], a[216:219], a[72:75], v[216:219]\n"
"s_add_u32 s60, s16, s3\n"
"s_addc_u32 s61, s17, 0\n"
"v_mfma_f32_16x16x32_bf16 v[216:219], a[220:223], a[88:91], v[216:219]\n"
"s_mov_b32 s62, s27\n"
"v_mfma_f32_16x16x32_bf16 v[220:223], a[220:223], a[92:95], v[220:223]\n"
"buffer_load_dwordx4 v24, s[48:51], s24 offen offset:1024 lds\n"
"v_mfma_f32_16x16x32_bf16 v[220:223], a[216:219], a[76:79], v[220:223]\n"
"s_mov_b32 s63, 131072\n"
"v_mfma_f32_16x16x32_bf16 v[224:227], a[224:227], a[64:67], v[224:227]\n"
"ds_read_b64_tr_b16 a[208:209], v22 offset:12288\n"
"ds_read_b64_tr_b16 a[210:211], v22 offset:13824\n"
"v_mfma_f32_16x16x32_bf16 v[224:227], a[228:231], a[80:83], v[224:227]\n"
"ds_read_b64_tr_b16 a[212:213], v22 offset:24576\n"
"ds_read_b64_tr_b16 a[214:215], v22 offset:26112\n"
"v_mfma_f32_16x16x32_bf16 v[228:231], a[228:231], a[84:87], v[228:231]\n"
"buffer_load_dwordx4 v25, s[48:51], s24 offen offset:2048 lds\n"
"v_mfma_f32_16x16x32_bf16 v[228:231], a[224:227], a[68:71], v[228:231]\n"
"s_add_u32 s3, s68, s33\n"
"v_mfma_f32_16x16x32_bf16 v[232:235], a[224:227], a[72:75], v[232:235]\n"
"s_sub_u32 s27, 81920, s33\n"
"v_mfma_f32_16x16x32_bf16 v[232:235], a[228:231], a[88:91], v[232:235]\n"
"s_add_u32 s68, s18, s3\n"
"s_addc_u32 s69, s19, 0\n"
"v_mfma_f32_16x16x32_bf16 v[236:239], a[228:231], a[92:95], v[236:239]\n"
"s_mov_b32 s24, 4325376\n"
"s_add_u32 m0, s76, 61440\n"
"v_mfma_f32_16x16x32_bf16 v[236:239], a[224:227], a[76:79], v[236:239]\n"
"buffer_load_dwordx4 v23, s[48:51], s24 offen lds\n"
"v_mfma_f32_16x16x32_bf16 v[240:243], a[232:235], a[64:67], v[240:243]\n"
"ds_read_b64_tr_b16 a[216:217], v22 offset:12296\n"
"ds_read_b64_tr_b16 a[218:219], v22 offset:13832\n"
"v_mfma_f32_16x16x32_bf16 v[240:243], a[236:239], a[80:83], v[240:243]\n"
"ds_read_b64_tr_b16 a[220:221], v22 offset:24584\n"
"ds_read_b64_tr_b16 a[222:223], v22 offset:26120\n"
"v_mfma_f32_16x16x32_bf16 v[244:247], a[236:239], a[84:87], v[244:247]\n"
"s_mov_b32 s70, s27\n"
"v_mfma_f32_16x16x32_bf16 v[244:247], a[232:235], a[68:71], v[244:247]\n"
"buffer_load_dwordx4 v24, s[48:51], s24 offen offset:1024 lds\n"
"v_mfma_f32_16x16x32_bf16 v[248:251], a[232:235], a[72:75], v[248:251]\n"
"s_mov_b32 s71, 131072\n"
"v_mfma_f32_16x16x32_bf16 v[248:251], a[236:239], a[88:91], v[248:251]\n"
"s_lshl_b32 s3, s93, 2\n"
"v_mfma_f32_16x16x32_bf16 v[252:255], a[236:239], a[92:95], v[252:255]\n"
"s_add_u32 s3, s3, s79\n"
"v_mfma_f32_16x16x32_bf16 v[252:255], a[232:235], a[76:79], v[252:255]\n"
"s_waitcnt lgkmcnt(8)\n"
"buffer_load_dwordx4 v25, s[48:51], s24 offen offset:2048 lds\n"
"v_mfma_f32_16x16x32_bf16 v[64:67], a[192:195], a[96:99], v[64:67]\n"
"ds_read_b64_tr_b16 a[224:225], v21 offset:12416\n"
"ds_read_b64_tr_b16 a[226:227], v21 offset:13952\n"
"v_mfma_f32_16x16x32_bf16 v[64:67], a[196:199], a[112:115], v[64:67]\n"
"ds_read_b64_tr_b16 a[228:229], v21 offset:24704\n"
"ds_read_b64_tr_b16 a[230:231], v21 offset:26240\n"
"v_mfma_f32_16x16x32_bf16 v[68:71], a[196:199], a[116:119], v[68:71]\n"
"s_waitcnt vmcnt(23)\n"
"ds_read_b128 a[128:131], v32 offset:0\n"
"v_mfma_f32_16x16x32_bf16 v[68:71], a[192:195], a[100:103], v[68:71]\n"
"ds_read_b128 a[144:147], v33 offset:0\n"
"ds_read_b128 a[132:135], v32 offset:2048\n"
"v_mfma_f32_16x16x32_bf16 v[72:75], a[192:195], a[104:107], v[72:75]\n"
"ds_read_b128 a[148:151], v33 offset:2048\n"
"ds_read_b128 a[136:139], v32 offset:4096\n"
"v_mfma_f32_16x16x32_bf16 v[72:75], a[196:199], a[120:123], v[72:75]\n"
"ds_read_b128 a[152:155], v33 offset:4096\n"
"ds_read_b128 a[140:143], v32 offset:6144\n"
"v_mfma_f32_16x16x32_bf16 v[76:79], a[196:199], a[124:127], v[76:79]\n"
"ds_read_b128 a[156:159], v33 offset:6144\n"
"v_mfma_f32_16x16x32_bf16 v[76:79], a[192:195], a[108:111], v[76:79]\n"
"s_lshl_b32 s3, s3, 2\n"
"v_mfma_f32_16x16x32_bf16 v[80:83], a[200:203], a[96:99], v[80:83]\n"
"ds_read_b64_tr_b16 a[232:233], v21 offset:12424\n"
"ds_read_b64_tr_b16 a[234:235], v21 offset:13960\n"
"v_mfma_f32_16x16x32_bf16 v[80:83], a[204:207], a[112:115], v[80:83]\n"
"ds_read_b64_tr_b16 a[236:237], v21 offset:24712\n"
"ds_read_b64_tr_b16 a[238:239], v21 offset:26248\n"
"v_mfma_f32_16x16x32_bf16 v[84:87], a[204:207], a[116:119], v[84:87]\n"
"s_add_u32 s3, s3, s77\n"
"v_mfma_f32_16x16x32_bf16 v[84:87], a[200:203], a[100:103], v[84:87]\n"
"s_lshl_b32 s3, s3, 18\n"
"v_mfma_f32_16x16x32_bf16 v[88:91], a[200:203], a[104:107], v[88:91]\n"
"s_waitcnt lgkmcnt(4)\n"
"s_mov_b32 s25, 768\n"
"v_mfma_f32_16x16x32_bf16 v[88:91], a[204:207], a[120:123], v[88:91]\n"
"s_add_u32 m0, s38, 0\n"
"s_nop 0\n"
"buffer_load_dwordx4 v36, s[52:55], s25 offen lds\n"
"v_mfma_f32_16x16x32_bf16 v[92:95], a[204:207], a[124:127], v[92:95]\n"
"s_add_u32 s3, s3, 33554432\n"
"v_mfma_f32_16x16x32_bf16 v[92:95], a[200:203], a[108:111], v[92:95]\n"
"s_waitcnt lgkmcnt(15)\n"
"v_mfma_f32_16x16x32_bf16 v[96:99], a[208:211], a[96:99], v[96:99]\n"
"ds_read_b64_tr_b16 a[192:193], v22 offset:12416\n"
"ds_read_b64_tr_b16 a[194:195], v22 offset:13952\n"
"v_mfma_f32_16x16x32_bf16 v[96:99], a[212:215], a[112:115], v[96:99]\n"
"ds_read_b64_tr_b16 a[196:197], v22 offset:24704\n"
"ds_read_b64_tr_b16 a[198:199], v22 offset:26240\n"
"v_mfma_f32_16x16x32_bf16 v[100:103], a[212:215], a[116:119], v[100:103]\n"
"buffer_load_dwordx4 v37, s[52:55], s25 offen offset:1024 lds\n"
"v_mfma_f32_16x16x32_bf16 v[100:103], a[208:211], a[100:103], v[100:103]\n"
"s_add_u32 s64, s20, s3\n"
"s_addc_u32 s65, s21, 0\n"
"v_mfma_f32_16x16x32_bf16 v[104:107], a[208:211], a[104:107], v[104:107]\n"
"s_mov_b32 s66, 262144\n"
"v_mfma_f32_16x16x32_bf16 v[104:107], a[212:215], a[120:123], v[104:107]\n"
"s_mov_b32 s67, 131072\n"
"v_mfma_f32_16x16x32_bf16 v[108:111], a[212:215], a[124:127], v[108:111]\n"
"buffer_load_dwordx4 v38, s[52:55], s25 offen offset:2048 lds\n"
"v_mfma_f32_16x16x32_bf16 v[108:111], a[208:211], a[108:111], v[108:111]\n"
"s_and_b32 s3, s93, 3\n"
"v_mfma_f32_16x16x32_bf16 v[112:115], a[216:219], a[96:99], v[112:115]\n"
"ds_read_b64_tr_b16 a[200:201], v22 offset:12424\n"
"ds_read_b64_tr_b16 a[202:203], v22 offset:13960\n"
"v_mfma_f32_16x16x32_bf16 v[112:115], a[220:223], a[112:115], v[112:115]\n"
"ds_read_b64_tr_b16 a[204:205], v22 offset:24712\n"
"ds_read_b64_tr_b16 a[206:207], v22 offset:26248\n"
"v_mfma_f32_16x16x32_bf16 v[116:119], a[220:223], a[116:119], v[116:119]\n"
"buffer_load_dwordx4 v39, s[52:55], s25 offen offset:3072 lds\n"
"v_mfma_f32_16x16x32_bf16 v[116:119], a[216:219], a[100:103], v[116:119]\n"
"s_lshl_b32 s33, s94, 2\n"
"v_mfma_f32_16x16x32_bf16 v[120:123], a[216:219], a[104:107], v[120:123]\n"
"s_add_u32 s3, s3, s33\n"
"v_mfma_f32_16x16x32_bf16 v[120:123], a[220:223], a[120:123], v[120:123]\n"
"s_lshl_b32 s3, s3, 2\n"
"v_mfma_f32_16x16x32_bf16 v[124:127], a[220:223], a[124:127], v[124:127]\n"
"s_mov_b32 s25, 41728\n"
"s_add_u32 m0, s38, 4096\n"
"v_mfma_f32_16x16x32_bf16 v[124:127], a[216:219], a[108:111], v[124:127]\n"
"s_waitcnt lgkmcnt(8)\n"
"buffer_load_dwordx4 v36, s[52:55], s25 offen lds\n"
"v_mfma_f32_16x16x32_bf16 v[128:131], a[224:227], a[96:99], v[128:131]\n"
"ds_read_b64_tr_b16 a[208:209], v21 offset:12544\n"
"ds_read_b64_tr_b16 a[210:211], v21 offset:14080\n"
"v_mfma_f32_16x16x32_bf16 v[128:131], a[228:231], a[112:115], v[128:131]\n"
"ds_read_b64_tr_b16 a[212:213], v21 offset:24832\n"
"ds_read_b64_tr_b16 a[214:215], v21 offset:26368\n"
"v_mfma_f32_16x16x32_bf16 v[132:135], a[228:231], a[116:119], v[132:135]\n"
"s_add_u32 s3, s3, s77\n"
"v_mfma_f32_16x16x32_bf16 v[132:135], a[224:227], a[100:103], v[132:135]\n"
"buffer_load_dwordx4 v37, s[52:55], s25 offen offset:1024 lds\n"
"v_mfma_f32_16x16x32_bf16 v[136:139], a[224:227], a[104:107], v[136:139]\n"
"s_mul_i32 s3, s3, 15360\n"
"v_mfma_f32_16x16x32_bf16 v[136:139], a[228:231], a[120:123], v[136:139]\n"
"s_add_u32 s88, s16, s3\n"
"s_addc_u32 s89, s17, 0\n"
"v_mfma_f32_16x16x32_bf16 v[140:143], a[228:231], a[124:127], v[140:143]\n"
"s_mov_b32 s90, 15360\n"
"v_mfma_f32_16x16x32_bf16 v[140:143], a[224:227], a[108:111], v[140:143]\n"
"buffer_load_dwordx4 v38, s[52:55], s25 offen offset:2048 lds\n"
"v_mfma_f32_16x16x32_bf16 v[144:147], a[232:235], a[96:99], v[144:147]\n"
"ds_read_b64_tr_b16 a[216:217], v21 offset:12552\n"
"ds_read_b64_tr_b16 a[218:219], v21 offset:14088\n"
"v_mfma_f32_16x16x32_bf16 v[144:147], a[236:239], a[112:115], v[144:147]\n"
"ds_read_b64_tr_b16 a[220:221], v21 offset:24840\n"
"ds_read_b64_tr_b16 a[222:223], v21 offset:26376\n"
"v_mfma_f32_16x16x32_bf16 v[148:151], a[236:239], a[116:119], v[148:151]\n"
"s_mov_b32 s91, 131072\n"
"v_mfma_f32_16x16x32_bf16 v[148:151], a[232:235], a[100:103], v[148:151]\n"
"buffer_load_dwordx4 v39, s[52:55], s25 offen offset:3072 lds\n"
"v_mfma_f32_16x16x32_bf16 v[152:155], a[232:235], a[104:107], v[152:155]\n"
"s_add_u32 s3, s79, 0\n"
"v_mfma_f32_16x16x32_bf16 v[152:155], a[236:239], a[120:123], v[152:155]\n"
"s_and_b32 s3, s3, 3\n"
"v_mfma_f32_16x16x32_bf16 v[156:159], a[236:239], a[124:127], v[156:159]\n"
"s_mul_i32 s80, s3, 98304\n"
"v_mfma_f32_16x16x32_bf16 v[156:159], a[232:235], a[108:111], v[156:159]\n"
"s_waitcnt lgkmcnt(8)\n"
"s_mov_b32 s24, 0\n"
"v_mfma_f32_16x16x32_bf16 v[160:163], a[192:195], a[96:99], v[160:163]\n"
"ds_read_b64_tr_b16 a[224:225], v22 offset:12544\n"
"ds_read_b64_tr_b16 a[226:227], v22 offset:14080\n"
"v_mfma_f32_16x16x32_bf16 v[160:163], a[196:199], a[112:115], v[160:163]\n"
"ds_read_b64_tr_b16 a[228:229], v22 offset:24832\n"
"ds_read_b64_tr_b16 a[230:231], v22 offset:26368\n"
"v_mfma_f32_16x16x32_bf16 v[164:167], a[196:199], a[116:119], v[164:167]\n"
"s_add_u32 m0, s76, 0\n"
"s_nop 0\n"
"buffer_load_dwordx4 v14, s[40:43], s24 offen offset:2048 nt lds\n"
"v_mfma_f32_16x16x32_bf16 v[164:167], a[192:195], a[100:103], v[164:167]\n"
"s_mul_i32 s84, s3, 245760\n"
"v_mfma_f32_16x16x32_bf16 v[168:171], a[192:195], a[104:107], v[168:171]\n"
"s_add_u32 s3, s79, 1\n"
"v_mfma_f32_16x16x32_bf16 v[168:171], a[196:199], a[120:123], v[168:171]\n"
"s_and_b32 s3, s3, 3\n"
"v_mfma_f32_16x16x32_bf16 v[172:175], a[196:199], a[124:127], v[172:175]\n"
"s_mul_i32 s81, s3, 98304\n"
"v_mfma_f32_16x16x32_bf16 v[172:175], a[192:195], a[108:111], v[172:175]\n"
"s_mul_i32 s85, s3, 245760\n"
"v_mfma_f32_16x16x32_bf16 v[176:179], a[200:203], a[96:99], v[176:179]\n"
"ds_read_b64_tr_b16 a[232:233], v22 offset:12552\n"
"ds_read_b64_tr_b16 a[234:235], v22 offset:14088\n"
"v_mfma_f32_16x16x32_bf16 v[176:179], a[204:207], a[112:115], v[176:179]\n"
"ds_read_b64_tr_b16 a[236:237], v22 offset:24840\n"
"ds_read_b64_tr_b16 a[238:239], v22 offset:26376\n"
"v_mfma_f32_16x16x32_bf16 v[180:183], a[204:207], a[116:119], v[180:183]\n"
"s_add_u32 s3, s79, 2\n"
"v_mfma_f32_16x16x32_bf16 v[180:183], a[200:203], a[100:103], v[180:183]\n"
"s_and_b32 s3, s3, 3\n"
"v_mfma_f32_16x16x32_bf16 v[184:187], a[200:203], a[104:107], v[184:187]\n"
"s_mul_i32 s82, s3, 98304\n"
"v_mfma_f32_16x16x32_bf16 v[184:187], a[204:207], a[120:123], v[184:187]\n"
"s_mul_i32 s86, s3, 245760\n"
"v_mfma_f32_16x16x32_bf16 v[188:191], a[204:207], a[124:127], v[188:191]\n"
"s_add_u32 s3, s79, 3\n"
"v_mfma_f32_16x16x32_bf16 v[188:191], a[200:203], a[108:111], v[188:191]\n"
"s_waitcnt vmcnt(26) lgkmcnt(0)\n"
"s_barrier\n"
"v_mfma_f32_16x16x32_bf16 v[192:195], a[208:211], a[96:99], v[192:195]\n"
"ds_read_b64_tr_b16 a[192:193], v19 offset:49152\n"
"ds_read_b64_tr_b16 a[194:195], v19 offset:50688\n"
"v_mfma_f32_16x16x32_bf16 v[192:195], a[212:215], a[112:115], v[192:195]\n"
"ds_read_b64_tr_b16 a[196:197], v19 offset:0\n"
"ds_read_b64_tr_b16 a[198:199], v19 offset:1536\n"
"v_mfma_f32_16x16x32_bf16 v[196:199], a[212:215], a[116:119], v[196:199]\n"
"s_mov_b32 s24, 4718592\n"
"s_add_u32 m0, s76, 122880\n"
"v_mfma_f32_16x16x32_bf16 v[196:199], a[208:211], a[100:103], v[196:199]\n"
"buffer_load_dwordx4 v23, s[48:51], s24 offen lds\n"
"v_mfma_f32_16x16x32_bf16 v[200:203], a[208:211], a[104:107], v[200:203]\n"
"s_and_b32 s3, s3, 3\n"
"v_mfma_f32_16x16x32_bf16 v[200:203], a[212:215], a[120:123], v[200:203]\n"
"s_mul_i32 s83, s3, 98304\n"
"v_mfma_f32_16x16x32_bf16 v[204:207], a[212:215], a[124:127], v[204:207]\n"
"s_mul_i32 s87, s3, 245760\n"
"v_mfma_f32_16x16x32_bf16 v[204:207], a[208:211], a[108:111], v[204:207]\n"
"buffer_load_dwordx4 v24, s[48:51], s24 offen offset:1024 lds\n"
"v_mfma_f32_16x16x32_bf16 v[208:211], a[216:219], a[96:99], v[208:211]\n"
"ds_read_b64_tr_b16 a[200:201], v19 offset:49160\n"
"ds_read_b64_tr_b16 a[202:203], v19 offset:50696\n"
"v_mfma_f32_16x16x32_bf16 v[208:211], a[220:223], a[112:115], v[208:211]\n"
"ds_read_b64_tr_b16 a[204:205], v19 offset:8\n"
"ds_read_b64_tr_b16 a[206:207], v19 offset:1544\n"
"v_mfma_f32_16x16x32_bf16 v[212:215], a[220:223], a[116:119], v[212:215]\n"
"s_lshl_b32 s92, 273, s79\n"
"v_mfma_f32_16x16x32_bf16 v[212:215], a[216:219], a[100:103], v[212:215]\n"
"buffer_load_dwordx4 v25, s[48:51], s24 offen offset:2048 lds\n"
"v_mfma_f32_16x16x32_bf16 v[216:219], a[216:219], a[104:107], v[216:219]\n"
"s_or_b32 s92, s92, 65536\n"
"v_mfma_f32_16x16x32_bf16 v[216:219], a[220:223], a[120:123], v[216:219]\n"
"s_cmp_eq_u32 s77, 3\n"
"s_cselect_b32 s93, s92, 0\n"
"v_mfma_f32_16x16x32_bf16 v[220:223], a[220:223], a[124:127], v[220:223]\n"
"s_mov_b32 s26, 0x3dd53b95\n"
"v_mfma_f32_16x16x32_bf16 v[220:223], a[216:219], a[108:111], v[220:223]\n"
"s_mov_b32 s24, 5111808\n"
"s_add_u32 m0, s76, 135168\n"
"v_mfma_f32_16x16x32_bf16 v[224:227], a[224:227], a[96:99], v[224:227]\n"
"ds_read_b64_tr_b16 a[208:209], v20 offset:49152\n"
"ds_read_b64_tr_b16 a[210:211], v20 offset:50688\n"
"v_mfma_f32_16x16x32_bf16 v[224:227], a[228:231], a[112:115], v[224:227]\n"
"ds_read_b64_tr_b16 a[212:213], v20 offset:0\n"
"ds_read_b64_tr_b16 a[214:215], v20 offset:1536\n"
"v_mfma_f32_16x16x32_bf16 v[228:231], a[228:231], a[116:119], v[228:231]\n"
"s_waitcnt vmcnt(19)\n"
"ds_read_b128 a[160:163], v34 offset:0\n"
"v_mfma_f32_16x16x32_bf16 v[228:231], a[224:227], a[100:103], v[228:231]\n"
"ds_read_b128 a[176:179], v35 offset:0\n"
"ds_read_b128 a[164:167], v34 offset:2048\n"
"v_mfma_f32_16x16x32_bf16 v[232:235], a[224:227], a[104:107], v[232:235]\n"
"ds_read_b128 a[180:183], v35 offset:2048\n"
"ds_read_b128 a[168:171], v34 offset:4096\n"
"v_mfma_f32_16x16x32_bf16 v[232:235], a[228:231], a[120:123], v[232:235]\n"
"ds_read_b128 a[184:187], v35 offset:4096\n"
"ds_read_b128 a[172:175], v34 offset:6144\n"
"v_mfma_f32_16x16x32_bf16 v[236:239], a[228:231], a[124:127], v[236:239]\n"
"ds_read_b128 a[188:191], v35 offset:6144\n"
"buffer_load_dwordx4 v23, s[48:51], s24 offen lds\n"
"v_mfma_f32_16x16x32_bf16 v[236:239], a[224:227], a[108:111], v[236:239]\n"
"s_mov_b32 s27, 0x44800000\n"
"v_mfma_f32_16x16x32_bf16 v[240:243], a[232:235], a[96:99], v[240:243]\n"
"ds_read_b64_tr_b16 a[216:217], v20 offset:49160\n"
"ds_read_b64_tr_b16 a[218:219], v20 offset:50696\n"
"v_mfma_f32_16x16x32_bf16 v[240:243], a[236:239], a[112:115], v[240:243]\n"
"ds_read_b64_tr_b16 a[220:221], v20 offset:8\n"
"ds_read_b64_tr_b16 a[222:223], v20 offset:1544\n"
"v_mfma_f32_16x16x32_bf16 v[244:247], a[236:239], a[116:119], v[244:247]\n"
"buffer_load_dwordx4 v24, s[48:51], s24 offen offset:1024 lds\n"
"v_mfma_f32_16x16x32_bf16 v[244:247], a[232:235], a[100:103], v[244:247]\n"
"s_mov_b32 s31, 0\n"
"v_mfma_f32_16x16x32_bf16 v[248:251], a[232:235], a[104:107], v[248:251]\n"
"s_mov_b32 s34, 0\n"
"v_mfma_f32_16x16x32_bf16 v[248:251], a[236:239], a[120:123], v[248:251]\n"
"s_mov_b32 s8, 4294955008\n"
"v_mfma_f32_16x16x32_bf16 v[252:255], a[236:239], a[124:127], v[252:255]\n"
"buffer_load_dwordx4 v25, s[48:51], s24 offen offset:2048 lds\n"
"v_mfma_f32_16x16x32_bf16 v[252:255], a[232:235], a[108:111], v[252:255]\n"
"s_waitcnt lgkmcnt(15)\n"
"v_mfma_f32_16x16x32_bf16 v[64:67], a[192:195], a[128:131], v[64:67]\n"
"ds_read_b64_tr_b16 a[224:225], v19 offset:49280\n"
"ds_read_b64_tr_b16 a[226:227], v19 offset:50816\n"
"v_mfma_f32_16x16x32_bf16 v[64:67], a[196:199], a[144:147], v[64:67]\n"
"ds_read_b64_tr_b16 a[228:229], v19 offset:128\n"
"ds_read_b64_tr_b16 a[230:231], v19 offset:1664\n"
"v_mfma_f32_16x16x32_bf16 v[68:71], a[196:199], a[148:151], v[68:71]\n"
"s_waitcnt lgkmcnt(8)\n"
"s_mov_b32 s25, 896\n"
"v_mfma_f32_16x16x32_bf16 v[68:71], a[192:195], a[132:135], v[68:71]\n"
"s_add_u32 m0, s39, 0\n"
"s_nop 0\n"
"buffer_load_dwordx4 v36, s[52:55], s25 offen lds\n"
"v_mfma_f32_16x16x32_bf16 v[72:75], a[192:195], a[136:139], v[72:75]\n"
"s_mov_b32 s9, 0\n"
"v_mfma_f32_16x16x32_bf16 v[72:75], a[196:199], a[152:155], v[72:75]\n"
"s_mov_b32 s10, 12288\n"
"v_mfma_f32_16x16x32_bf16 v[76:79], a[196:199], a[156:159], v[76:79]\n"
"s_mov_b32 s11, 24576\n"
"v_mfma_f32_16x16x32_bf16 v[76:79], a[192:195], a[140:143], v[76:79]\n"
"buffer_load_dwordx4 v37, s[52:55], s25 offen offset:1024 lds\n"
"v_mfma_f32_16x16x32_bf16 v[80:83], a[200:203], a[128:131], v[80:83]\n"
"ds_read_b64_tr_b16 a[232:233], v19 offset:49288\n"
"ds_read_b64_tr_b16 a[234:235], v19 offset:50824\n"
"v_mfma_f32_16x16x32_bf16 v[80:83], a[204:207], a[144:147], v[80:83]\n"
"ds_read_b64_tr_b16 a[236:237], v19 offset:136\n"
"ds_read_b64_tr_b16 a[238:239], v19 offset:1672\n"
"v_mfma_f32_16x16x32_bf16 v[84:87], a[204:207], a[148:151], v[84:87]\n"
"s_mov_b32 s12, 36864\n"
"v_mfma_f32_16x16x32_bf16 v[84:87], a[200:203], a[132:135], v[84:87]\n"
"buffer_load_dwordx4 v38, s[52:55], s25 offen offset:2048 lds\n"
"v_mfma_f32_16x16x32_bf16 v[88:91], a[200:203], a[136:139], v[88:91]\n"
"s_mov_b32 s13, 49152\n"
"v_mfma_f32_16x16x32_bf16 v[88:91], a[204:207], a[152:155], v[88:91]\n"
"s_mov_b32 s14, 4294942720\n"
"v_mfma_f32_16x16x32_bf16 v[92:95], a[204:207], a[156:159], v[92:95]\n"
"s_mov_b32 s15, 4294955008\n"
"v_mfma_f32_16x16x32_bf16 v[92:95], a[200:203], a[140:143], v[92:95]\n"
"s_waitcnt lgkmcnt(8)\n"
"buffer_load_dwordx4 v39, s[52:55], s25 offen offset:3072 lds\n"
"v_mfma_f32_16x16x32_bf16 v[96:99], a[208:211], a[128:131], v[96:99]\n"
"ds_read_b64_tr_b16 a[192:193], v20 offset:49280\n"
"ds_read_b64_tr_b16 a[194:195], v20 offset:50816\n"
"v_mfma_f32_16x16x32_bf16 v[96:99], a[212:215], a[144:147], v[96:99]\n"
"ds_read_b64_tr_b16 a[196:197], v20 offset:128\n"
"ds_read_b64_tr_b16 a[198:199], v20 offset:1664\n"
"v_mfma_f32_16x16x32_bf16 v[100:103], a[212:215], a[148:151], v[100:103]\n"
"s_mov_b32 s16, 0\n"
"v_mfma_f32_16x16x32_bf16 v[100:103], a[208:211], a[132:135], v[100:103]\n"
"s_mov_b32 s25, 41856\n"
"s_add_u32 m0, s39, 4096\n"
"v_mfma_f32_16x16x32_bf16 v[104:107], a[208:211], a[136:139], v[104:107]\n"
"buffer_load_dwordx4 v36, s[52:55], s25 offen lds\n"
"v_mfma_f32_16x16x32_bf16 v[104:107], a[212:215], a[152:155], v[104:107]\n"
"s_mov_b32 s17, 12288\n"
"v_mfma_f32_16x16x32_bf16 v[108:111], a[212:215], a[156:159], v[108:111]\n"
"s_mov_b32 s18, 24576\n"
"v_mfma_f32_16x16x32_bf16 v[108:111], a[208:211], a[140:143], v[108:111]\n"
"s_mov_b32 s19, 36864\n"
"v_mfma_f32_16x16x32_bf16 v[112:115], a[216:219], a[128:131], v[112:115]\n"
"ds_read_b64_tr_b16 a[200:201], v20 offset:49288\n"
"ds_read_b64_tr_b16 a[202:203], v20 offset:50824\n"
"v_mfma_f32_16x16x32_bf16 v[112:115], a[220:223], a[144:147], v[112:115]\n"
"ds_read_b64_tr_b16 a[204:205], v20 offset:136\n"
"ds_read_b64_tr_b16 a[206:207], v20 offset:1672\n"
"v_mfma_f32_16x16x32_bf16 v[116:119], a[220:223], a[148:151], v[116:119]\n"
"buffer_load_dwordx4 v37, s[52:55], s25 offen offset:1024 lds\n"
"v_mfma_f32_16x16x32_bf16 v[116:119], a[216:219], a[132:135], v[116:119]\n"
"s_getpc_b64 s[20:21]\n"
".Lmha_pc2:\n"
"v_mfma_f32_16x16x32_bf16 v[120:123], a[216:219], a[136:139], v[120:123]\n"
"s_add_u32 s20, s20, .Lmha_q7-.Lmha_pc2\n"
"s_addc_u32 s21, s21, 0\n"
"v_mfma_f32_16x16x32_bf16 v[120:123], a[220:223], a[152:155], v[120:123]\n"
"s_mov_b32 s22, .Lmha_cend-.Lmha_q7\n"
"v_mfma_f32_16x16x32_bf16 v[124:127], a[220:223], a[156:159], v[124:127]\n"
"buffer_load_dwordx4 v38, s[52:55], s25 offen offset:2048 lds\n"
"v_mfma_f32_16x16x32_bf16 v[124:127], a[216:219], a[140:143], v[124:127]\n"
"s_waitcnt lgkmcnt(8)\n"
"v_mfma_f32_16x16x32_bf16 v[128:131], a[224:227], a[128:131], v[128:131]\n"
"ds_read_b64_tr_b16 a[208:209], v19 offset:49408\n"
"ds_read_b64_tr_b16 a[210:211], v19 offset:50944\n"
"v_mfma_f32_16x16x32_bf16 v[128:131], a[228:231], a[144:147], v[128:131]\n"
"ds_read_b64_tr_b16 a[212:213], v19 offset:256\n"
"ds_read_b64_tr_b16 a[214:215], v19 offset:1792\n"
"v_mfma_f32_16x16x32_bf16 v[132:135], a[228:231], a[148:151], v[132:135]\n"
"buffer_load_dwordx4 v39, s[52:55], s25 offen offset:3072 lds\n"
"v_mfma_f32_16x16x32_bf16 v[132:135], a[224:227], a[132:135], v[132:135]\n"
"s_mov_b32 s23, 131072\n"
"v_mfma_f32_16x16x32_bf16 v[136:139], a[224:227], a[136:139], v[136:139]\n"
"s_bfe_u32 s3, s6, 131075\n"
"v_mfma_f32_16x16x32_bf16 v[136:139], a[228:231], a[152:155], v[136:139]\n"
"s_lshl_b32 s3, s3, 2\n"
"v_mfma_f32_16x16x32_bf16 v[140:143], a[228:231], a[156:159], v[140:143]\n"
"s_mov_b32 s24, 12288\n"
"s_add_u32 m0, s76, 12288\n"
"v_mfma_f32_16x16x32_bf16 v[140:143], a[224:227], a[140:143], v[140:143]\n"
"buffer_load_dwordx4 v12, s[40:43], s24 offen nt lds\n"
"v_mfma_f32_16x16x32_bf16 v[144:147], a[232:235], a[128:131], v[144:147]\n"
"ds_read_b64_tr_b16 a[216:217], v19 offset:49416\n"
"ds_read_b64_tr_b16 a[218:219], v19 offset:50952\n"
"v_mfma_f32_16x16x32_bf16 v[144:147], a[236:239], a[144:147], v[144:147]\n"
"ds_read_b64_tr_b16 a[220:221], v19 offset:264\n"
"ds_read_b64_tr_b16 a[222:223], v19 offset:1800\n"
"v_mfma_f32_16x16x32_bf16 v[148:151], a[236:239], a[148:151], v[148:151]\n"
"s_add_u32 s3, s3, s77\n"
"v_mfma_f32_16x16x32_bf16 v[148:151], a[232:235], a[132:135], v[148:151]\n"
"s_lshl_b32 s3, s3, 11\n"
"v_mfma_f32_16x16x32_bf16 v[152:155], a[232:235], a[136:139], v[152:155]\n"
"v_lshl_add_u32 v27, v44, 7, s3\n"
"v_mfma_f32_16x16x32_bf16 v[152:155], a[236:239], a[152:155], v[152:155]\n"
"v_cmp_gt_u32 vcc, 16, v40\n"
"v_mfma_f32_16x16x32_bf16 v[156:159], a[236:239], a[156:159], v[156:159]\n"
"s_nop 0\n"
"v_cndmask_b32 v27, 2.0, v27, vcc\n"
"v_mfma_f32_16x16x32_bf16 v[156:159], a[232:235], a[140:143], v[156:159]\n"
"s_waitcnt lgkmcnt(8)\n"
"v_mfma_f32_16x16x32_bf16 v[160:163], a[192:195], a[128:131], v[160:163]\n"
"ds_read_b64_tr_b16 a[224:225], v20 offset:49408\n"
"ds_read_b64_tr_b16 a[226:227], v20 offset:50944\n"
"v_mfma_f32_16x16x32_bf16 v[160:163], a[196:199], a[144:147], v[160:163]\n"
"ds_read_b64_tr_b16 a[228:229], v20 offset:256\n"
"ds_read_b64_tr_b16 a[230:231], v20 offset:1792\n"
"v_mfma_f32_16x16x32_bf16 v[164:167], a[196:199], a[148:151], v[164:167]\n"
"v_mov_b32 v40, 0\n"
"v_mfma_f32_16x16x32_bf16 v[164:167], a[192:195], a[132:135], v[164:167]\n"
"v_mov_b32 v41, 0\n"
"v_mfma_f32_16x16x32_bf16 v[168:171], a[192:195], a[136:139], v[168:171]\n"
"v_mov_b32 v42, 0\n"
"v_mfma_f32_16x16x32_bf16 v[168:171], a[196:199], a[152:155], v[168:171]\n"
"v_mov_b32 v43, 0\n"
"v_mfma_f32_16x16x32_bf16 v[172:175], a[196:199], a[156:159], v[172:175]\n"
"v_mov_b32 v44, 0\n"
"v_mfma_f32_16x16x32_bf16 v[172:175], a[192:195], a[140:143], v[172:175]\n"
"v_mov_b32 v45, 0\n"
"v_mfma_f32_16x16x32_bf16 v[176:179], a[200:203], a[128:131], v[176:179]\n"
"ds_read_b64_tr_b16 a[232:233], v20 offset:49416\n"
"ds_read_b64_tr_b16 a[234:235], v20 offset:50952\n"
"v_mfma_f32_16x16x32_bf16 v[176:179], a[204:207], a[144:147], v[176:179]\n"
"ds_read_b64_tr_b16 a[236:237], v20 offset:264\n"
"ds_read_b64_tr_b16 a[238:239], v20 offset:1800\n"
"v_mfma_f32_16x16x32_bf16 v[180:183], a[204:207], a[148:151], v[180:183]\n"
"v_mov_b32 v46, 0\n"
"v_mfma_f32_16x16x32_bf16 v[180:183], a[200:203], a[132:135], v[180:183]\n"
"v_mov_b32 v47, 0\n"
"v_mfma_f32_16x16x32_bf16 v[184:187], a[200:203], a[136:139], v[184:187]\n"
"v_mov_b32 v48, 0\n"
"v_mfma_f32_16x16x32_bf16 v[184:187], a[204:207], a[152:155], v[184:187]\n"
"v_mov_b32 v49, 0\n"
"v_mfma_f32_16x16x32_bf16 v[188:191], a[204:207], a[156:159], v[188:191]\n"
"v_mov_b32 v50, 0\n"
"v_mfma_f32_16x16x32_bf16 v[188:191], a[200:203], a[140:143], v[188:191]\n"
"s_waitcnt vmcnt(24) lgkmcnt(0)\n"
"s_barrier\n"
"v_mfma_f32_16x16x32_bf16 v[192:195], a[208:211], a[128:131], v[192:195]\n"
"ds_read_b64_tr_b16 a[192:193], v21 offset:0\n"
"ds_read_b64_tr_b16 a[194:195], v21 offset:1536\n"
"v_mfma_f32_16x16x32_bf16 v[192:195], a[212:215], a[144:147], v[192:195]\n"
"ds_read_b64_tr_b16 a[196:197], v19 offset:12288\n"
"ds_read_b64_tr_b16 a[198:199], v19 offset:13824\n"
"v_mfma_f32_16x16x32_bf16 v[196:199], a[212:215], a[148:151], v[196:199]\n"
"s_waitcnt vmcnt(16)\n"
"ds_read_b128 a[0:3], v32 offset:0\n"
"v_mfma_f32_16x16x32_bf16 v[196:199], a[208:211], a[132:135], v[196:199]\n"
"ds_read_b128 a[16:19], v33 offset:0\n"
"ds_read_b128 a[4:7], v32 offset:2048\n"
"v_mfma_f32_16x16x32_bf16 v[200:203], a[208:211], a[136:139], v[200:203]\n"
"ds_read_b128 a[20:23], v33 offset:2048\n"
"ds_read_b128 a[8:11], v32 offset:4096\n"
"v_mfma_f32_16x16x32_bf16 v[200:203], a[212:215], a[152:155], v[200:203]\n"
"ds_read_b128 a[24:27], v33 offset:4096\n"
"ds_read_b128 a[12:15], v32 offset:6144\n"
"v_mfma_f32_16x16x32_bf16 v[204:207], a[212:215], a[156:159], v[204:207]\n"
"ds_read_b128 a[28:31], v33 offset:6144\n"
"s_mov_b32 s24, 5505024\n"
"v_mfma_f32_16x16x32_bf16 v[204:207], a[208:211], a[140:143], v[204:207]\n"
"s_add_u32 m0, s76, 98304\n"
"s_nop 0\n"
"buffer_load_dwordx4 v23, s[48:51], s24 offen lds\n"
"v_mfma_f32_16x16x32_bf16 v[208:211], a[216:219], a[128:131], v[208:211]\n"
"ds_read_b64_tr_b16 a[200:201], v21 offset:8\n"
"ds_read_b64_tr_b16 a[202:203], v21 offset:1544\n"
"v_mfma_f32_16x16x32_bf16 v[208:211], a[220:223], a[144:147], v[208:211]\n"
"ds_read_b64_tr_b16 a[204:205], v19 offset:12296\n"
"ds_read_b64_tr_b16 a[206:207], v19 offset:13832\n"
"v_mfma_f32_16x16x32_bf16 v[212:215], a[220:223], a[148:151], v[212:215]\n"
"v_mov_b32 v51, 0\n"
"v_mfma_f32_16x16x32_bf16 v[212:215], a[216:219], a[132:135], v[212:215]\n"
"buffer_load_dwordx4 v24, s[48:51], s24 offen offset:1024 lds\n"
"v_mfma_f32_16x16x32_bf16 v[216:219], a[216:219], a[136:139], v[216:219]\n"
"v_mov_b32 v52, 0\n"
"v_mfma_f32_16x16x32_bf16 v[216:219], a[220:223], a[152:155], v[216:219]\n"
"v_mov_b32 v53, 0\n"
"v_mfma_f32_16x16x32_bf16 v[220:223], a[220:223], a[156:159], v[220:223]\n"
"v_mov_b32 v54, 0\n"
"v_mfma_f32_16x16x32_bf16 v[220:223], a[216:219], a[140:143], v[220:223]\n"
"buffer_load_dwordx4 v25, s[48:51], s24 offen offset:2048 lds\n"
"v_mfma_f32_16x16x32_bf16 v[224:227], a[224:227], a[128:131], v[224:227]\n"
"ds_read_b64_tr_b16 a[208:209], v22 offset:0\n"
"ds_read_b64_tr_b16 a[210:211], v22 offset:1536\n"
"v_mfma_f32_16x16x32_bf16 v[224:227], a[228:231], a[144:147], v[224:227]\n"
"ds_read_b64_tr_b16 a[212:213], v20 offset:12288\n"
"ds_read_b64_tr_b16 a[214:215], v20 offset:13824\n"
"v_mfma_f32_16x16x32_bf16 v[228:231], a[228:231], a[148:151], v[228:231]\n"
"v_mov_b32 v55, 0\n"
"v_mfma_f32_16x16x32_bf16 v[228:231], a[224:227], a[132:135], v[228:231]\n"
"s_waitcnt lgkmcnt(8)\n"
"s_mov_b32 s25, 1024\n"
"v_mfma_f32_16x16x32_bf16 v[232:235], a[224:227], a[136:139], v[232:235]\n"
"s_add_u32 m0, s38, 0\n"
"s_nop 0\n"
"buffer_load_dwordx4 v36, s[52:55], s25 offen lds\n"
"v_mfma_f32_16x16x32_bf16 v[232:235], a[228:231], a[152:155], v[232:235]\n"
"v_mfma_f32_16x16x32_bf16 v[236:239], a[228:231], a[156:159], v[236:239]\n"
"v_mfma_f32_16x16x32_bf16 v[236:239], a[224:227], a[140:143], v[236:239]\n"
"v_mfma_f32_16x16x32_bf16 v[240:243], a[232:235], a[128:131], v[240:243]\n"
"ds_read_b64_tr_b16 a[216:217], v22 offset:8\n"
"ds_read_b64_tr_b16 a[218:219], v22 offset:1544\n"
"v_mfma_f32_16x16x32_bf16 v[240:243], a[236:239], a[144:147], v[240:243]\n"
"ds_read_b64_tr_b16 a[220:221], v20 offset:12296\n"
"ds_read_b64_tr_b16 a[222:223], v20 offset:13832\n"
"v_mfma_f32_16x16x32_bf16 v[244:247], a[236:239], a[148:151], v[244:247]\n"
"buffer_load_dwordx4 v37, s[52:55], s25 offen offset:1024 lds\n"
"v_mfma_f32_16x16x32_bf16 v[244:247], a[232:235], a[132:135], v[244:247]\n"
"v_mfma_f32_16x16x32_bf16 v[248:251], a[232:235], a[136:139], v[248:251]\n"
"v_mfma_f32_16x16x32_bf16 v[248:251], a[236:239], a[152:155], v[248:251]\n"
"v_mfma_f32_16x16x32_bf16 v[252:255], a[236:239], a[156:159], v[252:255]\n"
"buffer_load_dwordx4 v38, s[52:55], s25 offen offset:2048 lds\n"
"v_mfma_f32_16x16x32_bf16 v[252:255], a[232:235], a[140:143], v[252:255]\n"
"s_waitcnt lgkmcnt(8)\n"
"v_mfma_f32_16x16x32_bf16 v[64:67], a[192:195], a[160:163], v[64:67]\n"
"ds_read_b64_tr_b16 a[224:225], v21 offset:128\n"
"ds_read_b64_tr_b16 a[226:227], v21 offset:1664\n"
"v_mfma_f32_16x16x32_bf16 v[64:67], a[196:199], a[176:179], v[64:67]\n"
"ds_read_b64_tr_b16 a[228:229], v19 offset:12416\n"
"ds_read_b64_tr_b16 a[230:231], v19 offset:13952\n"
"v_mfma_f32_16x16x32_bf16 v[68:71], a[196:199], a[180:183], v[68:71]\n"
"buffer_load_dwordx4 v39, s[52:55], s25 offen offset:3072 lds\n"
"v_mfma_f32_16x16x32_bf16 v[68:71], a[192:195], a[164:167], v[68:71]\n"
"v_mfma_f32_16x16x32_bf16 v[72:75], a[192:195], a[168:171], v[72:75]\n"
"v_mfma_f32_16x16x32_bf16 v[72:75], a[196:199], a[184:187], v[72:75]\n"
"v_mfma_f32_16x16x32_bf16 v[76:79], a[196:199], a[188:191], v[76:79]\n"
"s_mov_b32 s25, 41984\n"
"s_add_u32 m0, s38, 4096\n"
"v_mfma_f32_16x16x32_bf16 v[76:79], a[192:195], a[172:175], v[76:79]\n"
"buffer_load_dwordx4 v36, s[52:55], s25 offen lds\n"
"v_mfma_f32_16x16x32_bf16 v[80:83], a[200:203], a[160:163], v[80:83]\n"
"ds_read_b64_tr_b16 a[232:233], v21 offset:136\n"
"ds_read_b64_tr_b16 a[234:235], v21 offset:1672\n"
"v_mfma_f32_16x16x32_bf16 v[80:83], a[204:207], a[176:179], v[80:83]\n"
"ds_read_b64_tr_b16 a[236:237], v19 offset:12424\n"
"ds_read_b64_tr_b16 a[238:239], v19 offset:13960\n"
"v_mfma_f32_16x16x32_bf16 v[84:87], a[204:207], a[180:183], v[84:87]\n"
"v_mfma_f32_16x16x32_bf16 v[84:87], a[200:203], a[164:167], v[84:87]\n"
"buffer_load_dwordx4 v37, s[52:55], s25 offen offset:1024 lds\n"
"v_mfma_f32_16x16x32_bf16 v[88:91], a[200:203], a[168:171], v[88:91]\n"
"v_mfma_f32_16x16x32_bf16 v[88:91], a[204:207], a[184:187], v[88:91]\n"
"v_mfma_f32_16x16x32_bf16 v[92:95], a[204:207], a[188:191], v[92:95]\n"
"v_mfma_f32_16x16x32_bf16 v[92:95], a[200:203], a[172:175], v[92:95]\n"
"s_waitcnt lgkmcnt(8)\n"
"buffer_load_dwordx4 v38, s[52:55], s25 offen offset:2048 lds\n"
"v_mfma_f32_16x16x32_bf16 v[96:99], a[208:211], a[160:163], v[96:99]\n"
"ds_read_b64_tr_b16 a[192:193], v22 offset:128\n"
"ds_read_b64_tr_b16 a[194:195], v22 offset:1664\n"
"v_mfma_f32_16x16x32_bf16 v[96:99], a[212:215], a[176:179], v[96:99]\n"
"ds_read_b64_tr_b16 a[196:197], v20 offset:12416\n"
"ds_read_b64_tr_b16 a[198:199], v20 offset:13952\n"
"v_mfma_f32_16x16x32_bf16 v[100:103], a[212:215], a[180:183], v[100:103]\n"
"v_mfma_f32_16x16x32_bf16 v[100:103], a[208:211], a[164:167], v[100:103]\n"
"buffer_load_dwordx4 v39, s[52:55], s25 offen offset:3072 lds\n"
"v_mfma_f32_16x16x32_bf16 v[104:107], a[208:211], a[168:171], v[104:107]\n"
"v_mfma_f32_16x16x32_bf16 v[104:107], a[212:215], a[184:187], v[104:107]\n"
"v_mfma_f32_16x16x32_bf16 v[108:111], a[212:215], a[188:191], v[108:111]\n"
"v_mfma_f32_16x16x32_bf16 v[108:111], a[208:211], a[172:175], v[108:111]\n"
"s_mov_b32 s24, 5898240\n"
"s_add_u32 m0, s76, 49152\n"
"v_mfma_f32_16x16x32_bf16 v[112:115], a[216:219], a[160:163], v[112:115]\n"
"ds_read_b64_tr_b16 a[200:201], v22 offset:136\n"
"ds_read_b64_tr_b16 a[202:203], v22 offset:1672\n"
"v_mfma_f32_16x16x32_bf16 v[112:115], a[220:223], a[176:179], v[112:115]\n"
"ds_read_b64_tr_b16 a[204:205], v20 offset:12424\n"
"ds_read_b64_tr_b16 a[206:207], v20 offset:13960\n"
"v_mfma_f32_16x16x32_bf16 v[116:119], a[220:223], a[180:183], v[116:119]\n"
"buffer_load_dwordx4 v23, s[48:51], s24 offen lds\n"
"v_mfma_f32_16x16x32_bf16 v[116:119], a[216:219], a[164:167], v[116:119]\n"
"v_mfma_f32_16x16x32_bf16 v[120:123], a[216:219], a[168:171], v[120:123]\n"
"v_mfma_f32_16x16x32_bf16 v[120:123], a[220:223], a[184:187], v[120:123]\n"
"v_mfma_f32_16x16x32_bf16 v[124:127], a[220:223], a[188:191], v[124:127]\n"
"buffer_load_dwordx4 v24, s[48:51], s24 offen offset:1024 lds\n"
"v_mfma_f32_16x16x32_bf16 v[124:127], a[216:219], a[172:175], v[124:127]\n"
"s_waitcnt lgkmcnt(8)\n"
"v_mfma_f32_16x16x32_bf16 v[128:131], a[224:227], a[160:163], v[128:131]\n"
"ds_read_b64_tr_b16 a[208:209], v21 offset:256\n"
"ds_read_b64_tr_b16 a[210:211], v21 offset:1792\n"
"v_mfma_f32_16x16x32_bf16 v[128:131], a[228:231], a[176:179], v[128:131]\n"
"ds_read_b64_tr_b16 a[212:213], v19 offset:12544\n"
"ds_read_b64_tr_b16 a[214:215], v19 offset:14080\n"
"v_mfma_f32_16x16x32_bf16 v[132:135], a[228:231], a[180:183], v[132:135]\n"
"buffer_load_dwordx4 v25, s[48:51], s24 offen offset:2048 lds\n"
"v_mfma_f32_16x16x32_bf16 v[132:135], a[224:227], a[164:167], v[132:135]\n"
"v_mfma_f32_16x16x32_bf16 v[136:139], a[224:227], a[168:171], v[136:139]\n"
"v_mfma_f32_16x16x32_bf16 v[136:139], a[228:231], a[184:187], v[136:139]\n"
"v_mfma_f32_16x16x32_bf16 v[140:143], a[228:231], a[188:191], v[140:143]\n"
"s_mov_b32 s24, 12288\n"
"s_add_u32 m0, s76, 12288\n"
"v_mfma_f32_16x16x32_bf16 v[140:143], a[224:227], a[172:175], v[140:143]\n"
"buffer_load_dwordx4 v13, s[40:43], s24 offen offset:1024 nt lds\n"
"v_mfma_f32_16x16x32_bf16 v[144:147], a[232:235], a[160:163], v[144:147]\n"
"ds_read_b64_tr_b16 a[216:217], v21 offset:264\n"
"ds_read_b64_tr_b16 a[218:219], v21 offset:1800\n"
"v_mfma_f32_16x16x32_bf16 v[144:147], a[236:239], a[176:179], v[144:147]\n"
"ds_read_b64_tr_b16 a[220:221], v19 offset:12552\n"
"ds_read_b64_tr_b16 a[222:223], v19 offset:14088\n"
"v_mfma_f32_16x16x32_bf16 v[148:151], a[236:239], a[180:183], v[148:151]\n"
"v_mfma_f32_16x16x32_bf16 v[148:151], a[232:235], a[164:167], v[148:151]\n"
"v_mfma_f32_16x16x32_bf16 v[152:155], a[232:235], a[168:171], v[152:155]\n"
"v_mfma_f32_16x16x32_bf16 v[152:155], a[236:239], a[184:187], v[152:155]\n"
"v_mfma_f32_16x16x32_bf16 v[156:159], a[236:239], a[188:191], v[156:159]\n"
"v_mfma_f32_16x16x32_bf16 v[156:159], a[232:235], a[172:175], v[156:159]\n"
"s_waitcnt lgkmcnt(8)\n"
"v_mfma_f32_16x16x32_bf16 v[160:163], a[192:195], a[160:163], v[160:163]\n"
"ds_read_b64_tr_b16 a[224:225], v22 offset:256\n"
"ds_read_b64_tr_b16 a[226:227], v22 offset:1792\n"
"v_mfma_f32_16x16x32_bf16 v[160:163], a[196:199], a[176:179], v[160:163]\n"
"ds_read_b64_tr_b16 a[228:229], v20 offset:12544\n"
"ds_read_b64_tr_b16 a[230:231], v20 offset:14080\n"
"v_mfma_f32_16x16x32_bf16 v[164:167], a[196:199], a[180:183], v[164:167]\n"
"s_waitcnt vmcnt(16)\n"
"ds_read_b128 a[32:35], v34 offset:0\n"
"v_mfma_f32_16x16x32_bf16 v[164:167], a[192:195], a[164:167], v[164:167]\n"
"ds_read_b128 a[48:51], v35 offset:0\n"
"ds_read_b128 a[36:39], v34 offset:2048\n"
"v_mfma_f32_16x16x32_bf16 v[168:171], a[192:195], a[168:171], v[168:171]\n"
"ds_read_b128 a[52:55], v35 offset:2048\n"
"ds_read_b128 a[40:43], v34 offset:4096\n"
"v_mfma_f32_16x16x32_bf16 v[168:171], a[196:199], a[184:187], v[168:171]\n"
"ds_read_b128 a[56:59], v35 offset:4096\n"
"ds_read_b128 a[44:47], v34 offset:6144\n"
"v_mfma_f32_16x16x32_bf16 v[172:175], a[196:199], a[188:191], v[172:175]\n"
"ds_read_b128 a[60:63], v35 offset:6144\n"
"v_mfma_f32_16x16x32_bf16 v[172:175], a[192:195], a[172:175], v[172:175]\n"
"v_mfma_f32_16x16x32_bf16 v[176:179], a[200:203], a[160:163], v[176:179]\n"
"ds_read_b64_tr_b16 a[232:233], v22 offset:264\n"
"ds_read_b64_tr_b16 a[234:235], v22 offset:1800\n"
"v_mfma_f32_16x16x32_bf16 v[176:179], a[204:207], a[176:179], v[176:179]\n"
"ds_read_b64_tr_b16 a[236:237], v20 offset:12552\n"
"ds_read_b64_tr_b16 a[238:239], v20 offset:14088\n"
"v_mfma_f32_16x16x32_bf16 v[180:183], a[204:207], a[180:183], v[180:183]\n"
"v_mfma_f32_16x16x32_bf16 v[180:183], a[200:203], a[164:167], v[180:183]\n"
"v_mfma_f32_16x16x32_bf16 v[184:187], a[200:203], a[168:171], v[184:187]\n"
"s_waitcnt lgkmcnt(4)\n"
"s_mov_b32 s25, 1152\n"
"v_mfma_f32_16x16x32_bf16 v[184:187], a[204:207], a[184:187], v[184:187]\n"
"s_add_u32 m0, s39, 0\n"
"s_nop 0\n"
"buffer_load_dwordx4 v36, s[52:55], s25 offen lds\n"
"v_mfma_f32_16x16x32_bf16 v[188:191], a[204:207], a[188:191], v[188:191]\n"
"v_mfma_f32_16x16x32_bf16 v[188:191], a[200:203], a[172:175], v[188:191]\n"
"s_waitcnt vmcnt(63) lgkmcnt(0)\n"
"s_barrier\n"
"v_mfma_f32_16x16x32_bf16 v[192:195], a[208:211], a[160:163], v[192:195]\n"
"ds_read_b64_tr_b16 a[192:193], v21 offset:12288\n"
"ds_read_b64_tr_b16 a[194:195], v21 offset:13824\n"
"v_mfma_f32_16x16x32_bf16 v[192:195], a[212:215], a[176:179], v[192:195]\n"
"ds_read_b64_tr_b16 a[196:197], v21 offset:24576\n"
"ds_read_b64_tr_b16 a[198:199], v21 offset:26112\n"
"v_mfma_f32_16x16x32_bf16 v[196:199], a[212:215], a[180:183], v[196:199]\n"
"buffer_load_dwordx4 v37, s[52:55], s25 offen offset:1024 lds\n"
"v_mfma_f32_16x16x32_bf16 v[196:199], a[208:211], a[164:167], v[196:199]\n"
"v_mfma_f32_16x16x32_bf16 v[200:203], a[208:211], a[168:171], v[200:203]\n"
"v_mfma_f32_16x16x32_bf16 v[200:203], a[212:215], a[184:187], v[200:203]\n"
"v_mfma_f32_16x16x32_bf16 v[204:207], a[212:215], a[188:191], v[204:207]\n"
"buffer_load_dwordx4 v38, s[52:55], s25 offen offset:2048 lds\n"
"v_mfma_f32_16x16x32_bf16 v[204:207], a[208:211], a[172:175], v[204:207]\n"
"v_mfma_f32_16x16x32_bf16 v[208:211], a[216:219], a[160:163], v[208:211]\n"
"ds_read_b64_tr_b16 a[200:201], v21 offset:12296\n"
"ds_read_b64_tr_b16 a[202:203], v21 offset:13832\n"
"v_mfma_f32_16x16x32_bf16 v[208:211], a[220:223], a[176:179], v[208:211]\n"
"ds_read_b64_tr_b16 a[204:205], v21 offset:24584\n"
"ds_read_b64_tr_b16 a[206:207], v21 offset:26120\n"
"v_mfma_f32_16x16x32_bf16 v[212:215], a[220:223], a[180:183], v[212:215]\n"
"buffer_load_dwordx4 v39, s[52:55], s25 offen offset:3072 lds\n"
"v_mfma_f32_16x16x32_bf16 v[212:215], a[216:219], a[164:167], v[212:215]\n"
"v_mfma_f32_16x16x32_bf16 v[216:219], a[216:219], a[168:171], v[216:219]\n"
"v_mfma_f32_16x16x32_bf16 v[216:219], a[220:223], a[184:187], v[216:219]\n"
"v_mfma_f32_16x16x32_bf16 v[220:223], a[220:223], a[188:191], v[220:223]\n"
"s_mov_b32 s25, 42112\n"
"s_add_u32 m0, s39, 4096\n"
"v_mfma_f32_16x16x32_bf16 v[220:223], a[216:219], a[172:175], v[220:223]\n"
"buffer_load_dwordx4 v36, s[52:55], s25 offen lds\n"
"v_mfma_f32_16x16x32_bf16 v[224:227], a[224:227], a[160:163], v[224:227]\n"
"ds_read_b64_tr_b16 a[208:209], v22 offset:12288\n"
"ds_read_b64_tr_b16 a[210:211], v22 offset:13824\n"
"v_mfma_f32_16x16x32_bf16 v[224:227], a[228:231], a[176:179], v[224:227]\n"
"ds_read_b64_tr_b16 a[212:213], v22 offset:24576\n"
"ds_read_b64_tr_b16 a[214:215], v22 offset:26112\n"
"v_mfma_f32_16x16x32_bf16 v[228:231], a[228:231], a[180:183], v[228:231]\n"
"v_mfma_f32_16x16x32_bf16 v[228:231], a[224:227], a[164:167], v[228:231]\n"
"buffer_load_dwordx4 v37, s[52:55], s25 offen offset:1024 lds\n"
"v_mfma_f32_16x16x32_bf16 v[232:235], a[224:227], a[168:171], v[232:235]\n"
"v_mfma_f32_16x16x32_bf16 v[232:235], a[228:231], a[184:187], v[232:235]\n"
"v_mfma_f32_16x16x32_bf16 v[236:239], a[228:231], a[188:191], v[236:239]\n"
"v_mfma_f32_16x16x32_bf16 v[236:239], a[224:227], a[172:175], v[236:239]\n"
"buffer_load_dwordx4 v38, s[52:55], s25 offen offset:2048 lds\n"
"v_mfma_f32_16x16x32_bf16 v[240:243], a[232:235], a[160:163], v[240:243]\n"
"ds_read_b64_tr_b16 a[216:217], v22 offset:12296\n"
"ds_read_b64_tr_b16 a[218:219], v22 offset:13832\n"
"v_mfma_f32_16x16x32_bf16 v[240:243], a[236:239], a[176:179], v[240:243]\n"
"ds_read_b64_tr_b16 a[220:221], v22 offset:24584\n"
"ds_read_b64_tr_b16 a[222:223], v22 offset:26120\n"
"v_mfma_f32_16x16x32_bf16 v[244:247], a[236:239], a[180:183], v[244:247]\n"
"v_mfma_f32_16x16x32_bf16 v[244:247], a[232:235], a[164:167], v[244:247]\n"
"buffer_load_dwordx4 v39, s[52:55], s25 offen offset:3072 lds\n"
"v_mfma_f32_16x16x32_bf16 v[248:251], a[232:235], a[168:171], v[248:251]\n"
"v_mfma_f32_16x16x32_bf16 v[248:251], a[236:239], a[184:187], v[248:251]\n"
"v_mfma_f32_16x16x32_bf16 v[252:255], a[236:239], a[188:191], v[252:255]\n"
"v_mfma_f32_16x16x32_bf16 v[252:255], a[232:235], a[172:175], v[252:255]\n"
"s_waitcnt lgkmcnt(8)\n"
"s_mov_b32 s24, 6291456\n"
"v_mfma_f32_16x16x32_bf16 v[64:67], a[192:195], a[0:3], v[64:67]\n"
"ds_read_b64_tr_b16 a[224:225], v21 offset:12416\n"
"ds_read_b64_tr_b16 a[226:227], v21 offset:13952\n"
"v_mfma_f32_16x16x32_bf16 v[64:67], a[196:199], a[16:19], v[64:67]\n"
"ds_read_b64_tr_b16 a[228:229], v21 offset:24704\n"
"ds_read_b64_tr_b16 a[230:231], v21 offset:26240\n"
"v_mfma_f32_16x16x32_bf16 v[68:71], a[196:199], a[20:23], v[68:71]\n"
"s_add_u32 m0, s76, 110592\n"
"s_nop 0\n"
"buffer_load_dwordx4 v23, s[48:51], s24 offen lds\n"
"v_mfma_f32_16x16x32_bf16 v[68:71], a[192:195], a[4:7], v[68:71]\n"
"v_mfma_f32_16x16x32_bf16 v[72:75], a[192:195], a[8:11], v[72:75]\n"
"v_mfma_f32_16x16x32_bf16 v[72:75], a[196:199], a[24:27], v[72:75]\n"
"v_mfma_f32_16x16x32_bf16 v[76:79], a[196:199], a[28:31], v[76:79]\n"
"buffer_load_dwordx4 v24, s[48:51], s24 offen offset:1024 lds\n"
"v_mfma_f32_16x16x32_bf16 v[76:79], a[192:195], a[12:15], v[76:79]\n"
"v_mfma_f32_16x16x32_bf16 v[80:83], a[200:203], a[0:3], v[80:83]\n"
"ds_read_b64_tr_b16 a[232:233], v21 offset:12424\n"
"ds_read_b64_tr_b16 a[234:235], v21 offset:13960\n"
"v_mfma_f32_16x16x32_bf16 v[80:83], a[204:207], a[16:19], v[80:83]\n"
"ds_read_b64_tr_b16 a[236:237], v21 offset:24712\n"
"ds_read_b64_tr_b16 a[238:239], v21 offset:26248\n"
"v_mfma_f32_16x16x32_bf16 v[84:87], a[204:207], a[20:23], v[84:87]\n"
"buffer_load_dwordx4 v25, s[48:51], s24 offen offset:2048 lds\n"
"v_mfma_f32_16x16x32_bf16 v[84:87], a[200:203], a[4:7], v[84:87]\n"
"v_mfma_f32_16x16x32_bf16 v[88:91], a[200:203], a[8:11], v[88:91]\n"
"v_mfma_f32_16x16x32_bf16 v[88:91], a[204:207], a[24:27], v[88:91]\n"
"v_mfma_f32_16x16x32_bf16 v[92:95], a[204:207], a[28:31], v[92:95]\n"
"s_mov_b32 s24, 6684672\n"
"s_add_u32 m0, s76, 61440\n"
"v_mfma_f32_16x16x32_bf16 v[92:95], a[200:203], a[12:15], v[92:95]\n"
"s_waitcnt lgkmcnt(8)\n"
"buffer_load_dwordx4 v23, s[48:51], s24 offen lds\n"
"v_mfma_f32_16x16x32_bf16 v[96:99], a[208:211], a[0:3], v[96:99]\n"
"ds_read_b64_tr_b16 a[192:193], v22 offset:12416\n"
"ds_read_b64_tr_b16 a[194:195], v22 offset:13952\n"
"v_mfma_f32_16x16x32_bf16 v[96:99], a[212:215], a[16:19], v[96:99]\n"
"ds_read_b64_tr_b16 a[196:197], v22 offset:24704\n"
"ds_read_b64_tr_b16 a[198:199], v22 offset:26240\n"
"v_mfma_f32_16x16x32_bf16 v[100:103], a[212:215], a[20:23], v[100:103]\n"
"v_mfma_f32_16x16x32_bf16 v[100:103], a[208:211], a[4:7], v[100:103]\n"
"buffer_load_dwordx4 v24, s[48:51], s24 offen offset:1024 lds\n"
"v_mfma_f32_16x16x32_bf16 v[104:107], a[208:211], a[8:11], v[104:107]\n"
"v_mfma_f32_16x16x32_bf16 v[104:107], a[212:215], a[24:27], v[104:107]\n"
"v_mfma_f32_16x16x32_bf16 v[108:111], a[212:215], a[28:31], v[108:111]\n"
"v_mfma_f32_16x16x32_bf16 v[108:111], a[208:211], a[12:15], v[108:111]\n"
"buffer_load_dwordx4 v25, s[48:51], s24 offen offset:2048 lds\n"
"v_mfma_f32_16x16x32_bf16 v[112:115], a[216:219], a[0:3], v[112:115]\n"
"ds_read_b64_tr_b16 a[200:201], v22 offset:12424\n"
"ds_read_b64_tr_b16 a[202:203], v22 offset:13960\n"
"v_mfma_f32_16x16x32_bf16 v[112:115], a[220:223], a[16:19], v[112:115]\n"
"ds_read_b64_tr_b16 a[204:205], v22 offset:24712\n"
"ds_read_b64_tr_b16 a[206:207], v22 offset:26248\n"
"v_mfma_f32_16x16x32_bf16 v[116:119], a[220:223], a[20:23], v[116:119]\n"
"v_mfma_f32_16x16x32_bf16 v[116:119], a[216:219], a[4:7], v[116:119]\n"
"v_mfma_f32_16x16x32_bf16 v[120:123], a[216:219], a[8:11], v[120:123]\n"
"v_mfma_f32_16x16x32_bf16 v[120:123], a[220:223], a[24:27], v[120:123]\n"
"v_mfma_f32_16x16x32_bf16 v[124:127], a[220:223], a[28:31], v[124:127]\n"
"v_mfma_f32_16x16x32_bf16 v[124:127], a[216:219], a[12:15], v[124:127]\n"
"s_waitcnt lgkmcnt(8)\n"
"v_mfma_f32_16x16x32_bf16 v[128:131], a[224:227], a[0:3], v[128:131]\n"
"ds_read_b64_tr_b16 a[208:209], v21 offset:12544\n"
"ds_read_b64_tr_b16 a[210:211], v21 offset:14080\n"
"v_mfma_f32_16x16x32_bf16 v[128:131], a[228:231], a[16:19], v[128:131]\n"
"ds_read_b64_tr_b16 a[212:213], v21 offset:24832\n"
"ds_read_b64_tr_b16 a[214:215], v21 offset:26368\n"
"v_mfma_f32_16x16x32_bf16 v[132:135], a[228:231], a[20:23], v[132:135]\n"
"s_waitcnt vmcnt(18)\n"
"ds_read_b128 a[64:67], v32 offset:0\n"
"v_mfma_f32_16x16x32_bf16 v[132:135], a[224:227], a[4:7], v[132:135]\n"
"ds_read_b128 a[80:83], v33 offset:0\n"
"ds_read_b128 a[68:71], v32 offset:2048\n"
"v_mfma_f32_16x16x32_bf16 v[136:139], a[224:227], a[8:11], v[136:139]\n"
"ds_read_b128 a[84:87], v33 offset:2048\n"
"ds_read_b128 a[72:75], v32 offset:4096\n"
"v_mfma_f32_16x16x32_bf16 v[136:139], a[228:231], a[24:27], v[136:139]\n"
"ds_read_b128 a[88:91], v33 offset:4096\n"
"ds_read_b128 a[76:79], v32 offset:6144\n"
"v_mfma_f32_16x16x32_bf16 v[140:143], a[228:231], a[28:31], v[140:143]\n"
"ds_read_b128 a[92:95], v33 offset:6144\n"
"s_mov_b32 s24, 12288\n"
"v_mfma_f32_16x16x32_bf16 v[140:143], a[224:227], a[12:15], v[140:143]\n"
"s_add_u32 m0, s76, 12288\n"
"s_nop 0\n"
"buffer_load_dwordx4 v14, s[40:43], s24 offen offset:2048 nt lds\n"
"v_mfma_f32_16x16x32_bf16 v[144:147], a[232:235], a[0:3], v[144:147]\n"
"ds_read_b64_tr_b16 a[216:217], v21 offset:12552\n"
"ds_read_b64_tr_b16 a[218:219], v21 offset:14088\n"
"v_mfma_f32_16x16x32_bf16 v[144:147], a[236:239], a[16:19], v[144:147]\n"
"ds_read_b64_tr_b16 a[220:221], v21 offset:24840\n"
"ds_read_b64_tr_b16 a[222:223], v21 offset:26376\n"
"v_mfma_f32_16x16x32_bf16 v[148:151], a[236:239], a[20:23], v[148:151]\n"
"v_mfma_f32_16x16x32_bf16 v[148:151], a[232:235], a[4:7], v[148:151]\n"
"v_mfma_f32_16x16x32_bf16 v[152:155], a[232:235], a[8:11], v[152:155]\n"
"buffer_load_dword v62, v27, s[20:23], 0 offen\n"
"v_mfma_f32_16x16x32_bf16 v[152:155], a[236:239], a[24:27], v[152:155]\n"
"v_mfma_f32_16x16x32_bf16 v[156:159], a[236:239], a[28:31], v[156:159]\n"
"v_mfma_f32_16x16x32_bf16 v[156:159], a[232:235], a[12:15], v[156:159]\n"
"s_waitcnt lgkmcnt(15)\n"
"v_mfma_f32_16x16x32_bf16 v[160:163], a[192:195], a[0:3], v[160:163]\n"
"ds_read_b64_tr_b16 a[224:225], v22 offset:12544\n"
"ds_read_b64_tr_b16 a[226:227], v22 offset:14080\n"
"v_mfma_f32_16x16x32_bf16 v[160:163], a[196:199], a[16:19], v[160:163]\n"
"ds_read_b64_tr_b16 a[228:229], v22 offset:24832\n"
"ds_read_b64_tr_b16 a[230:231], v22 offset:26368\n"
"v_mfma_f32_16x16x32_bf16 v[164:167], a[196:199], a[20:23], v[164:167]\n"
"v_mfma_f32_16x16x32_bf16 v[164:167], a[192:195], a[4:7], v[164:167]\n"
"v_mfma_f32_16x16x32_bf16 v[168:171], a[192:195], a[8:11], v[168:171]\n"
"v_mfma_f32_16x16x32_bf16 v[168:171], a[196:199], a[24:27], v[168:171]\n"
"v_mfma_f32_16x16x32_bf16 v[172:175], a[196:199], a[28:31], v[172:175]\n"
"v_mfma_f32_16x16x32_bf16 v[172:175], a[192:195], a[12:15], v[172:175]\n"
"v_mfma_f32_16x16x32_bf16 v[176:179], a[200:203], a[0:3], v[176:179]\n"
"ds_read_b64_tr_b16 a[232:233], v22 offset:12552\n"
"ds_read_b64_tr_b16 a[234:235], v22 offset:14088\n"
"v_mfma_f32_16x16x32_bf16 v[176:179], a[204:207], a[16:19], v[176:179]\n"
"ds_read_b64_tr_b16 a[236:237], v22 offset:24840\n"
"ds_read_b64_tr_b16 a[238:239], v22 offset:26376\n"
"v_mfma_f32_16x16x32_bf16 v[180:183], a[204:207], a[20:23], v[180:183]\n"
"v_mfma_f32_16x16x32_bf16 v[180:183], a[200:203], a[4:7], v[180:183]\n"
"v_mfma_f32_16x16x32_bf16 v[184:187], a[200:203], a[8:11], v[184:187]\n"
"v_mfma_f32_16x16x32_bf16 v[184:187], a[204:207], a[24:27], v[184:187]\n"
"v_mfma_f32_16x16x32_bf16 v[188:191], a[204:207], a[28:31], v[188:191]\n"
"v_mfma_f32_16x16x32_bf16 v[188:191], a[200:203], a[12:15], v[188:191]\n"
"s_waitcnt vmcnt(17) lgkmcnt(0)\n"
"s_barrier\n"
"v_mfma_f32_16x16x32_bf16 v[192:195], a[208:211], a[0:3], v[192:195]\n"
"ds_read_b64_tr_b16 a[192:193], v19 offset:49152\n"
"ds_read_b64_tr_b16 a[194:195], v19 offset:50688\n"
"v_mfma_f32_16x16x32_bf16 v[192:195], a[212:215], a[16:19], v[192:195]\n"
"ds_read_b64_tr_b16 a[196:197], v19 offset:0\n"
"ds_read_b64_tr_b16 a[198:199], v19 offset:1536\n"
"v_mfma_f32_16x16x32_bf16 v[196:199], a[212:215], a[20:23], v[196:199]\n"
"s_mov_b32 s24, 7077888\n"
"s_add_u32 m0, s76, 122880\n"
"v_mfma_f32_16x16x32_bf16 v[196:199], a[208:211], a[4:7], v[196:199]\n"
"buffer_load_dwordx4 v23, s[48:51], s24 offen lds\n"
"v_mfma_f32_16x16x32_bf16 v[200:203], a[208:211], a[8:11], v[200:203]\n"
"v_mfma_f32_16x16x32_bf16 v[200:203], a[212:215], a[24:27], v[200:203]\n"
"v_mfma_f32_16x16x32_bf16 v[204:207], a[212:215], a[28:31], v[204:207]\n"
"v_mfma_f32_16x16x32_bf16 v[204:207], a[208:211], a[12:15], v[204:207]\n"
"buffer_load_dwordx4 v24, s[48:51], s24 offen offset:1024 lds\n"
"v_mfma_f32_16x16x32_bf16 v[208:211], a[216:219], a[0:3], v[208:211]\n"
"ds_read_b64_tr_b16 a[200:201], v19 offset:49160\n"
"ds_read_b64_tr_b16 a[202:203], v19 offset:50696\n"
"v_mfma_f32_16x16x32_bf16 v[208:211], a[220:223], a[16:19], v[208:211]\n"
"ds_read_b64_tr_b16 a[204:205], v19 offset:8\n"
"ds_read_b64_tr_b16 a[206:207], v19 offset:1544\n"
"v_mfma_f32_16x16x32_bf16 v[212:215], a[220:223], a[20:23], v[212:215]\n"
"v_mfma_f32_16x16x32_bf16 v[212:215], a[216:219], a[4:7], v[212:215]\n"
"buffer_load_dwordx4 v25, s[48:51], s24 offen offset:2048 lds\n"
"v_mfma_f32_16x16x32_bf16 v[216:219], a[216:219], a[8:11], v[216:219]\n"
"v_mfma_f32_16x16x32_bf16 v[216:219], a[220:223], a[24:27], v[216:219]\n"
"v_mfma_f32_16x16x32_bf16 v[220:223], a[220:223], a[28:31], v[220:223]\n"
"v_mfma_f32_16x16x32_bf16 v[220:223], a[216:219], a[12:15], v[220:223]\n"
"s_mov_b32 s24, 7471104\n"
"s_add_u32 m0, s76, 135168\n"
"v_mfma_f32_16x16x32_bf16 v[224:227], a[224:227], a[0:3], v[224:227]\n"
"ds_read_b64_tr_b16 a[208:209], v20 offset:49152\n"
"ds_read_b64_tr_b16 a[210:211], v20 offset:50688\n"
"v_mfma_f32_16x16x32_bf16 v[224:227], a[228:231], a[16:19], v[224:227]\n"
"ds_read_b64_tr_b16 a[212:213], v20 offset:0\n"
"ds_read_b64_tr_b16 a[214:215], v20 offset:1536\n"
"v_mfma_f32_16x16x32_bf16 v[228:231], a[228:231], a[20:23], v[228:231]\n"
"buffer_load_dwordx4 v23, s[48:51], s24 offen lds\n"
"v_mfma_f32_16x16x32_bf16 v[228:231], a[224:227], a[4:7], v[228:231]\n"
"v_mfma_f32_16x16x32_bf16 v[232:235], a[224:227], a[8:11], v[232:235]\n"
"v_mfma_f32_16x16x32_bf16 v[232:235], a[228:231], a[24:27], v[232:235]\n"
"v_mfma_f32_16x16x32_bf16 v[236:239], a[228:231], a[28:31], v[236:239]\n"
"buffer_load_dwordx4 v24, s[48:51], s24 offen offset:1024 lds\n"
"v_mfma_f32_16x16x32_bf16 v[236:239], a[224:227], a[12:15], v[236:239]\n"
"v_mfma_f32_16x16x32_bf16 v[240:243], a[232:235], a[0:3], v[240:243]\n"
"ds_read_b64_tr_b16 a[216:217], v20 offset:49160\n"
"ds_read_b64_tr_b16 a[218:219], v20 offset:50696\n"
"v_mfma_f32_16x16x32_bf16 v[240:243], a[236:239], a[16:19], v[240:243]\n"
"ds_read_b64_tr_b16 a[220:221], v20 offset:8\n"
"ds_read_b64_tr_b16 a[222:223], v20 offset:1544\n"
"v_mfma_f32_16x16x32_bf16 v[244:247], a[236:239], a[20:23], v[244:247]\n"
"buffer_load_dwordx4 v25, s[48:51], s24 offen offset:2048 lds\n"
"v_mfma_f32_16x16x32_bf16 v[244:247], a[232:235], a[4:7], v[244:247]\n"
"v_mfma_f32_16x16x32_bf16 v[248:251], a[232:235], a[8:11], v[248:251]\n"
"v_mfma_f32_16x16x32_bf16 v[248:251], a[236:239], a[24:27], v[248:251]\n"
"v_mfma_f32_16x16x32_bf16 v[252:255], a[236:239], a[28:31], v[252:255]\n"
"s_mov_b32 s24, 24576\n"
"s_add_u32 m0, s76, 24576\n"
"v_mfma_f32_16x16x32_bf16 v[252:255], a[232:235], a[12:15], v[252:255]\n"
"s_waitcnt lgkmcnt(8)\n"
"buffer_load_dwordx4 v12, s[40:43], s24 offen nt lds\n"
"v_mfma_f32_16x16x32_bf16 v[64:67], a[192:195], a[32:35], v[64:67]\n"
"ds_read_b64_tr_b16 a[224:225], v19 offset:49280\n"
"ds_read_b64_tr_b16 a[226:227], v19 offset:50816\n"
"v_mfma_f32_16x16x32_bf16 v[64:67], a[196:199], a[48:51], v[64:67]\n"
"ds_read_b64_tr_b16 a[228:229], v19 offset:128\n"
"ds_read_b64_tr_b16 a[230:231], v19 offset:1664\n"
"v_mfma_f32_16x16x32_bf16 v[68:71], a[196:199], a[52:55], v[68:71]\n"
"v_mfma_f32_16x16x32_bf16 v[68:71], a[192:195], a[36:39], v[68:71]\n"
"buffer_load_dwordx4 v13, s[40:43], s24 offen offset:1024 nt lds\n"
"v_mfma_f32_16x16x32_bf16 v[72:75], a[192:195], a[40:43], v[72:75]\n"
"v_mfma_f32_16x16x32_bf16 v[72:75], a[196:199], a[56:59], v[72:75]\n"
"v_mfma_f32_16x16x32_bf16 v[76:79], a[196:199], a[60:63], v[76:79]\n"
"v_mfma_f32_16x16x32_bf16 v[76:79], a[192:195], a[44:47], v[76:79]\n"
"buffer_load_dwordx4 v14, s[40:43], s24 offen offset:2048 nt lds\n"
"v_mfma_f32_16x16x32_bf16 v[80:83], a[200:203], a[32:35], v[80:83]\n"
"ds_read_b64_tr_b16 a[232:233], v19 offset:49288\n"
"ds_read_b64_tr_b16 a[234:235], v19 offset:50824\n"
"v_mfma_f32_16x16x32_bf16 v[80:83], a[204:207], a[48:51], v[80:83]\n"
"ds_read_b64_tr_b16 a[236:237], v19 offset:136\n"
"ds_read_b64_tr_b16 a[238:239], v19 offset:1672\n"
"v_mfma_f32_16x16x32_bf16 v[84:87], a[204:207], a[52:55], v[84:87]\n"
"v_mfma_f32_16x16x32_bf16 v[84:87], a[200:203], a[36:39], v[84:87]\n"
"v_mfma_f32_16x16x32_bf16 v[88:91], a[200:203], a[40:43], v[88:91]\n"
"v_mfma_f32_16x16x32_bf16 v[88:91], a[204:207], a[56:59], v[88:91]\n"
"v_mfma_f32_16x16x32_bf16 v[92:95], a[204:207], a[60:63], v[92:95]\n"
"v_mfma_f32_16x16x32_bf16 v[92:95], a[200:203], a[44:47], v[92:95]\n"
"s_waitcnt lgkmcnt(8)\n"
"v_mfma_f32_16x16x32_bf16 v[96:99], a[208:211], a[32:35], v[96:99]\n"
"ds_read_b64_tr_b16 a[192:193], v20 offset:49280\n"
"ds_read_b64_tr_b16 a[194:195], v20 offset:50816\n"
"v_mfma_f32_16x16x32_bf16 v[96:99], a[212:215], a[48:51], v[96:99]\n"
"ds_read_b64_tr_b16 a[196:197], v20 offset:128\n"
"ds_read_b64_tr_b16 a[198:199], v20 offset:1664\n"
"v_mfma_f32_16x16x32_bf16 v[100:103], a[212:215], a[52:55], v[100:103]\n"
"s_waitcnt vmcnt(17)\n"
"ds_read_b128 a[96:99], v34 offset:0\n"
"v_mfma_f32_16x16x32_bf16 v[100:103], a[208:211], a[36:39], v[100:103]\n"
"ds_read_b128 a[112:115], v35 offset:0\n"
"ds_read_b128 a[100:103], v34 offset:2048\n"
"v_mfma_f32_16x16x32_bf16 v[104:107], a[208:211], a[40:43], v[104:107]\n"
"ds_read_b128 a[116:119], v35 offset:2048\n"
"ds_read_b128 a[104:107], v34 offset:4096\n"
"v_mfma_f32_16x16x32_bf16 v[104:107], a[212:215], a[56:59], v[104:107]\n"
"ds_read_b128 a[120:123], v35 offset:4096\n"
"ds_read_b128 a[108:111], v34 offset:6144\n"
"v_mfma_f32_16x16x32_bf16 v[108:111], a[212:215], a[60:63], v[108:111]\n"
"ds_read_b128 a[124:127], v35 offset:6144\n"
"v_mfma_f32_16x16x32_bf16 v[108:111], a[208:211], a[44:47], v[108:111]\n"
"v_mfma_f32_16x16x32_bf16 v[112:115], a[216:219], a[32:35], v[112:115]\n"
"ds_read_b64_tr_b16 a[200:201], v20 offset:49288\n"
"ds_read_b64_tr_b16 a[202:203], v20 offset:50824\n"
"v_mfma_f32_16x16x32_bf16 v[112:115], a[220:223], a[48:51], v[112:115]\n"
"ds_read_b64_tr_b16 a[204:205], v20 offset:136\n"
"ds_read_b64_tr_b16 a[206:207], v20 offset:1672\n"
"v_mfma_f32_16x16x32_bf16 v[116:119], a[220:223], a[52:55], v[116:119]\n"
"v_mfma_f32_16x16x32_bf16 v[116:119], a[216:219], a[36:39], v[116:119]\n"
".Lmha_q7:\n"
"v_mfma_f32_16x16x32_bf16 v[120:123], a[216:219], a[40:43], v[120:123]\n"
"v_mfma_f32_16x16x32_bf16 v[120:123], a[220:223], a[56:59], v[120:123]\n"
"v_mfma_f32_16x16x32_bf16 v[124:127], a[220:223], a[60:63], v[124:127]\n"
"v_mfma_f32_16x16x32_bf16 v[124:127], a[216:219], a[44:47], v[124:127]\n"
"s_waitcnt lgkmcnt(15)\n"
"v_mfma_f32_16x16x32_bf16 v[128:131], a[224:227], a[32:35], v[128:131]\n"
"ds_read_b64_tr_b16 a[208:209], v19 offset:49408\n"
"ds_read_b64_tr_b16 a[210:211], v19 offset:50944\n"
"v_mfma_f32_16x16x32_bf16 v[128:131], a[228:231], a[48:51], v[128:131]\n"
"ds_read_b64_tr_b16 a[212:213], v19 offset:256\n"
"ds_read_b64_tr_b16 a[214:215], v19 offset:1792\n"
"v_mfma_f32_16x16x32_bf16 v[132:135], a[228:231], a[52:55], v[132:135]\n"
"v_mfma_f32_16x16x32_bf16 v[132:135], a[224:227], a[36:39], v[132:135]\n"
"v_mfma_f32_16x16x32_bf16 v[136:139], a[224:227], a[40:43], v[136:139]\n"
"v_mfma_f32_16x16x32_bf16 v[136:139], a[228:231], a[56:59], v[136:139]\n"
"v_mfma_f32_16x16x32_bf16 v[140:143], a[228:231], a[60:63], v[140:143]\n"
"v_mfma_f32_16x16x32_bf16 v[140:143], a[224:227], a[44:47], v[140:143]\n"
"v_mfma_f32_16x16x32_bf16 v[144:147], a[232:235], a[32:35], v[144:147]\n"
"ds_read_b64_tr_b16 a[216:217], v19 offset:49416\n"
"ds_read_b64_tr_b16 a[218:219], v19 offset:50952\n"
"v_mfma_f32_16x16x32_bf16 v[144:147], a[236:239], a[48:51], v[144:147]\n"
"ds_read_b64_tr_b16 a[220:221], v19 offset:264\n"
"ds_read_b64_tr_b16 a[222:223], v19 offset:1800\n"
"v_mfma_f32_16x16x32_bf16 v[148:151], a[236:239], a[52:55], v[148:151]\n"
"v_mfma_f32_16x16x32_bf16 v[148:151], a[232:235], a[36:39], v[148:151]\n"
"v_mfma_f32_16x16x32_bf16 v[152:155], a[232:235], a[40:43], v[152:155]\n"
"v_mfma_f32_16x16x32_bf16 v[152:155], a[236:239], a[56:59], v[152:155]\n"
"v_mfma_f32_16x16x32_bf16 v[156:159], a[236:239], a[60:63], v[156:159]\n"
"v_mfma_f32_16x16x32_bf16 v[156:159], a[232:235], a[44:47], v[156:159]\n"
"s_waitcnt lgkmcnt(8)\n"
"v_mfma_f32_16x16x32_bf16 v[160:163], a[192:195], a[32:35], v[160:163]\n"
"ds_read_b64_tr_b16 a[224:225], v20 offset:49408\n"
"ds_read_b64_tr_b16 a[226:227], v20 offset:50944\n"
"v_mfma_f32_16x16x32_bf16 v[160:163], a[196:199], a[48:51], v[160:163]\n"
"ds_read_b64_tr_b16 a[228:229], v20 offset:256\n"
"ds_read_b64_tr_b16 a[230:231], v20 offset:1792\n"
"v_mfma_f32_16x16x32_bf16 v[164:167], a[196:199], a[52:55], v[164:167]\n"
"v_mfma_f32_16x16x32_bf16 v[164:167], a[192:195], a[36:39], v[164:167]\n"
"v_mfma_f32_16x16x32_bf16 v[168:171], a[192:195], a[40:43], v[168:171]\n"
"v_mfma_f32_16x16x32_bf16 v[168:171], a[196:199], a[56:59], v[168:171]\n"
"v_mfma_f32_16x16x32_bf16 v[172:175], a[196:199], a[60:63], v[172:175]\n"
"v_mfma_f32_16x16x32_bf16 v[172:175], a[192:195], a[44:47], v[172:175]\n"
"v_mfma_f32_16x16x32_bf16 v[176:179], a[200:203], a[32:35], v[176:179]\n"
"ds_read_b64_tr_b16 a[232:233], v20 offset:49416\n"
"ds_read_b64_tr_b16 a[234:235], v20 offset:50952\n"
"v_mfma_f32_16x16x32_bf16 v[176:179], a[204:207], a[48:51], v[176:179]\n"
"ds_read_b64_tr_b16 a[236:237], v20 offset:264\n"
"ds_read_b64_tr_b16 a[238:239], v20 offset:1800\n"
"v_mfma_f32_16x16x32_bf16 v[180:183], a[204:207], a[52:55], v[180:183]\n"
"v_mfma_f32_16x16x32_bf16 v[180:183], a[200:203], a[36:39], v[180:183]\n"
"v_mfma_f32_16x16x32_bf16 v[184:187], a[200:203], a[40:43], v[184:187]\n"
"v_mfma_f32_16x16x32_bf16 v[184:187], a[204:207], a[56:59], v[184:187]\n"
"v_mfma_f32_16x16x32_bf16 v[188:191], a[204:207], a[60:63], v[188:191]\n"
"v_mfma_f32_16x16x32_bf16 v[188:191], a[200:203], a[44:47], v[188:191]\n"
"s_waitcnt vmcnt(3) lgkmcnt(0)\n"
"s_barrier\n"
"v_mfma_f32_16x16x32_bf16 v[192:195], a[208:211], a[32:35], v[192:195]\n"
"ds_read_b64_tr_b16 a[192:193], v21 offset:0\n"
"ds_read_b64_tr_b16 a[194:195], v21 offset:1536\n"
"v_mfma_f32_16x16x32_bf16 v[192:195], a[212:215], a[48:51], v[192:195]\n"
"ds_read_b64_tr_b16 a[196:197], v19 offset:12288\n"
"ds_read_b64_tr_b16 a[198:199], v19 offset:13824\n"
"v_mfma_f32_16x16x32_bf16 v[196:199], a[212:215], a[52:55], v[196:199]\n"
"ds_read_b64_tr_b16 a[200:201], v21 offset:12288\n"
"ds_read_b64_tr_b16 a[202:203], v21 offset:13824\n"
"v_mfma_f32_16x16x32_bf16 v[196:199], a[208:211], a[36:39], v[196:199]\n"
"ds_read_b64_tr_b16 a[204:205], v21 offset:24576\n"
"ds_read_b64_tr_b16 a[206:207], v21 offset:26112\n"
"v_mfma_f32_16x16x32_bf16 v[200:203], a[208:211], a[40:43], v[200:203]\n"
"s_mov_b32 s24, 0\n"
"s_add_u32 m0, s76, 73728\n"
"v_mfma_f32_16x16x32_bf16 v[200:203], a[212:215], a[56:59], v[200:203]\n"
"buffer_load_dwordx4 v15, s[44:47], s24 offen nt lds\n"
"v_mfma_f32_16x16x32_bf16 v[204:207], a[212:215], a[60:63], v[204:207]\n"
"v_mfma_f32_16x16x32_bf16 v[204:207], a[208:211], a[44:47], v[204:207]\n"
"v_mfma_f32_16x16x32_bf16 v[208:211], a[216:219], a[32:35], v[208:211]\n"
"v_mfma_f32_16x16x32_bf16 v[208:211], a[220:223], a[48:51], v[208:211]\n"
"buffer_load_dwordx4 v16, s[44:47], s24 offen offset:1024 nt lds\n"
"v_mfma_f32_16x16x32_bf16 v[212:215], a[220:223], a[52:55], v[212:215]\n"
"v_mfma_f32_16x16x32_bf16 v[212:215], a[216:219], a[36:39], v[212:215]\n"
"v_mfma_f32_16x16x32_bf16 v[216:219], a[216:219], a[40:43], v[216:219]\n"
"v_mfma_f32_16x16x32_bf16 v[216:219], a[220:223], a[56:59], v[216:219]\n"
"buffer_load_dwordx4 v17, s[44:47], s24 offen offset:2048 nt lds\n"
"v_mfma_f32_16x16x32_bf16 v[220:223], a[220:223], a[60:63], v[220:223]\n"
"v_mfma_f32_16x16x32_bf16 v[220:223], a[216:219], a[44:47], v[220:223]\n"
"v_mfma_f32_16x16x32_bf16 v[224:227], a[224:227], a[32:35], v[224:227]\n"
"ds_read_b64_tr_b16 a[208:209], v21 offset:8\n"
"ds_read_b64_tr_b16 a[210:211], v21 offset:1544\n"
"v_mfma_f32_16x16x32_bf16 v[224:227], a[228:231], a[48:51], v[224:227]\n"
"ds_read_b64_tr_b16 a[212:213], v19 offset:12296\n"
"ds_read_b64_tr_b16 a[214:215], v19 offset:13832\n"
"v_mfma_f32_16x16x32_bf16 v[228:231], a[228:231], a[52:55], v[228:231]\n"
"ds_read_b64_tr_b16 a[216:217], v21 offset:12296\n"
"ds_read_b64_tr_b16 a[218:219], v21 offset:13832\n"
"v_mfma_f32_16x16x32_bf16 v[228:231], a[224:227], a[36:39], v[228:231]\n"
"ds_read_b64_tr_b16 a[220:221], v21 offset:24584\n"
"ds_read_b64_tr_b16 a[222:223], v21 offset:26120\n"
"v_mfma_f32_16x16x32_bf16 v[232:235], a[224:227], a[40:43], v[232:235]\n"
"s_mov_b32 s24, 12288\n"
"s_add_u32 m0, s76, 86016\n"
"v_mfma_f32_16x16x32_bf16 v[232:235], a[228:231], a[56:59], v[232:235]\n"
"buffer_load_dwordx4 v15, s[44:47], s24 offen nt lds\n"
"v_mfma_f32_16x16x32_bf16 v[236:239], a[228:231], a[60:63], v[236:239]\n"
"v_mfma_f32_16x16x32_bf16 v[236:239], a[224:227], a[44:47], v[236:239]\n"
"v_mfma_f32_16x16x32_bf16 v[240:243], a[232:235], a[32:35], v[240:243]\n"
"v_mfma_f32_16x16x32_bf16 v[240:243], a[236:239], a[48:51], v[240:243]\n"
"buffer_load_dwordx4 v16, s[44:47], s24 offen offset:1024 nt lds\n"
"v_mfma_f32_16x16x32_bf16 v[244:247], a[236:239], a[52:55], v[244:247]\n"
"v_mfma_f32_16x16x32_bf16 v[244:247], a[232:235], a[36:39], v[244:247]\n"
"v_mfma_f32_16x16x32_bf16 v[248:251], a[232:235], a[40:43], v[248:251]\n"
"v_mfma_f32_16x16x32_bf16 v[248:251], a[236:239], a[56:59], v[248:251]\n"
"buffer_load_dwordx4 v17, s[44:47], s24 offen offset:2048 nt lds\n"
"v_mfma_f32_16x16x32_bf16 v[252:255], a[236:239], a[60:63], v[252:255]\n"
"v_mfma_f32_16x16x32_bf16 v[252:255], a[232:235], a[44:47], v[252:255]\n"
"s_waitcnt lgkmcnt(8)\n"
"v_mfma_f32_16x16x32_bf16 v[64:67], a[192:195], a[64:67], v[64:67]\n"
"ds_read_b64_tr_b16 a[224:225], v22 offset:0\n"
"ds_read_b64_tr_b16 a[226:227], v22 offset:1536\n"
"v_mfma_f32_16x16x32_bf16 v[64:67], a[196:199], a[80:83], v[64:67]\n"
"ds_read_b64_tr_b16 a[228:229], v20 offset:12288\n"
"ds_read_b64_tr_b16 a[230:231], v20 offset:13824\n"
"s_mov_b32 s24, 0\n"
"v_mfma_f32_16x16x32_bf16 v[64:67], a[200:203], a[96:99], v[64:67]\n"
"ds_read_b64_tr_b16 a[232:233], v22 offset:12288\n"
"ds_read_b64_tr_b16 a[234:235], v22 offset:13824\n"
"buffer_load_dword v62, v26, s[88:91], s24 offen\n"
"v_mfma_f32_16x16x32_bf16 v[64:67], a[204:207], a[112:115], v[64:67]\n"
"ds_read_b64_tr_b16 a[236:237], v22 offset:24576\n"
"ds_read_b64_tr_b16 a[238:239], v22 offset:26112\n"
"v_mfma_f32_16x16x32_bf16 v[68:71], a[204:207], a[116:119], v[68:71]\n"
"v_mfma_f32_16x16x32_bf16 v[68:71], a[200:203], a[100:103], v[68:71]\n"
"v_mfma_f32_16x16x32_bf16 v[68:71], a[196:199], a[84:87], v[68:71]\n"
"v_mfma_f32_16x16x32_bf16 v[68:71], a[192:195], a[68:71], v[68:71]\n"
"s_nop 1\n"
"v_mul_f32 v64, s26, v64\n"
"v_mul_f32 v65, s26, v65\n"
"v_mfma_f32_16x16x32_bf16 v[72:75], a[192:195], a[72:75], v[72:75]\n"
"v_mul_f32 v66, s26, v66\n"
"v_mul_f32 v67, s26, v67\n"
"v_mfma_f32_16x16x32_bf16 v[72:75], a[196:199], a[88:91], v[72:75]\n"
"v_cvt_pk_bf16_f32 v64, v64, v65\n"
"v_cvt_pk_bf16_f32 v65, v66, v67\n"
"v_mfma_f32_16x16x32_bf16 v[72:75], a[200:203], a[104:107], v[72:75]\n"
"v_mfma_f32_16x16x32_bf16 v[72:75], a[204:207], a[120:123], v[72:75]\n"
"v_mul_f32 v68, s26, v68\n"
"v_mul_f32 v69, s26, v69\n"
"v_mfma_f32_16x16x32_bf16 v[76:79], a[204:207], a[124:127], v[76:79]\n"
"v_mul_f32 v70, s26, v70\n"
"v_mul_f32 v71, s26, v71\n"
"v_mfma_f32_16x16x32_bf16 v[76:79], a[200:203], a[108:111], v[76:79]\n"
"v_cvt_pk_bf16_f32 v68, v68, v69\n"
"v_cvt_pk_bf16_f32 v69, v70, v71\n"
"v_mfma_f32_16x16x32_bf16 v[76:79], a[196:199], a[92:95], v[76:79]\n"
"v_mfma_f32_16x16x32_bf16 v[76:79], a[192:195], a[76:79], v[76:79]\n"
"s_waitcnt lgkmcnt(8)\n"
"v_mul_f32 v72, s26, v72\n"
"v_mfma_f32_16x16x32_bf16 v[80:83], a[208:211], a[64:67], v[80:83]\n"
"ds_read_b64_tr_b16 a[192:193], v22 offset:8\n"
"ds_read_b64_tr_b16 a[194:195], v22 offset:1544\n"
"v_mfma_f32_16x16x32_bf16 v[80:83], a[212:215], a[80:83], v[80:83]\n"
"ds_read_b64_tr_b16 a[196:197], v20 offset:12296\n"
"ds_read_b64_tr_b16 a[198:199], v20 offset:13832\n"
"v_mfma_f32_16x16x32_bf16 v[80:83], a[216:219], a[96:99], v[80:83]\n"
"ds_read_b64_tr_b16 a[200:201], v22 offset:12296\n"
"ds_read_b64_tr_b16 a[202:203], v22 offset:13832\n"
"v_mfma_f32_16x16x32_bf16 v[80:83], a[220:223], a[112:115], v[80:83]\n"
"ds_read_b64_tr_b16 a[204:205], v22 offset:24584\n"
"ds_read_b64_tr_b16 a[206:207], v22 offset:26120\n"
"v_mfma_f32_16x16x32_bf16 v[84:87], a[220:223], a[116:119], v[84:87]\n"
"v_mul_f32 v73, s26, v73\n"
"v_mul_f32 v74, s26, v74\n"
"v_mfma_f32_16x16x32_bf16 v[84:87], a[216:219], a[100:103], v[84:87]\n"
"v_mul_f32 v75, s26, v75\n"
"v_cvt_pk_bf16_f32 v72, v72, v73\n"
"v_mfma_f32_16x16x32_bf16 v[84:87], a[212:215], a[84:87], v[84:87]\n"
"v_cvt_pk_bf16_f32 v73, v74, v75\n"
"v_mul_f32 v76, s26, v76\n"
"v_mfma_f32_16x16x32_bf16 v[84:87], a[208:211], a[68:71], v[84:87]\n"
"v_mul_f32 v77, s26, v77\n"
"v_mul_f32 v78, s26, v78\n"
"v_mfma_f32_16x16x32_bf16 v[88:91], a[208:211], a[72:75], v[88:91]\n"
"v_mul_f32 v79, s26, v79\n"
"v_cvt_pk_bf16_f32 v76, v76, v77\n"
"v_mfma_f32_16x16x32_bf16 v[88:91], a[212:215], a[88:91], v[88:91]\n"
"v_cvt_pk_bf16_f32 v77, v78, v79\n"
"v_mul_f32 v80, s26, v80\n"
"v_mfma_f32_16x16x32_bf16 v[88:91], a[216:219], a[104:107], v[88:91]\n"
"v_mul_f32 v81, s26, v81\n"
"v_mul_f32 v82, s26, v82\n"
"v_mfma_f32_16x16x32_bf16 v[88:91], a[220:223], a[120:123], v[88:91]\n"
"v_mul_f32 v83, s26, v83\n"
"v_cvt_pk_bf16_f32 v66, v80, v81\n"
"v_mfma_f32_16x16x32_bf16 v[92:95], a[220:223], a[124:127], v[92:95]\n"
"v_cvt_pk_bf16_f32 v67, v82, v83\n"
"v_mul_f32 v84, s26, v84\n"
"v_mfma_f32_16x16x32_bf16 v[92:95], a[216:219], a[108:111], v[92:95]\n"
"v_mul_f32 v85, s26, v85\n"
"v_mul_f32 v86, s26, v86\n"
"v_mfma_f32_16x16x32_bf16 v[92:95], a[212:215], a[92:95], v[92:95]\n"
"v_mul_f32 v87, s26, v87\n"
"v_cvt_pk_bf16_f32 v70, v84, v85\n"
"v_mfma_f32_16x16x32_bf16 v[92:95], a[208:211], a[76:79], v[92:95]\n"
"s_waitcnt lgkmcnt(8)\n"
"v_cvt_pk_bf16_f32 v71, v86, v87\n"
"v_mfma_f32_16x16x32_bf16 v[96:99], a[224:227], a[64:67], v[96:99]\n"
"ds_read_b64_tr_b16 a[208:209], v21 offset:128\n"
"ds_read_b64_tr_b16 a[210:211], v21 offset:1664\n"
"v_mfma_f32_16x16x32_bf16 v[96:99], a[228:231], a[80:83], v[96:99]\n"
"ds_read_b64_tr_b16 a[212:213], v19 offset:12416\n"
"ds_read_b64_tr_b16 a[214:215], v19 offset:13952\n"
"v_mfma_f32_16x16x32_bf16 v[96:99], a[232:235], a[96:99], v[96:99]\n"
"ds_read_b64_tr_b16 a[216:217], v21 offset:12416\n"
"ds_read_b64_tr_b16 a[218:219], v21 offset:13952\n"
"v_mfma_f32_16x16x32_bf16 v[96:99], a[236:239], a[112:115], v[96:99]\n"
"ds_read_b64_tr_b16 a[220:221], v21 offset:24704\n"
"ds_read_b64_tr_b16 a[222:223], v21 offset:26240\n"
"v_mfma_f32_16x16x32_bf16 v[100:103], a[236:239], a[116:119], v[100:103]\n"
"v_mul_f32 v88, s26, v88\n"
"v_mul_f32 v89, s26, v89\n"
"v_mfma_f32_16x16x32_bf16 v[100:103], a[232:235], a[100:103], v[100:103]\n"
"v_mul_f32 v90, s26, v90\n"
"v_mul_f32 v91, s26, v91\n"
"v_mfma_f32_16x16x32_bf16 v[100:103], a[228:231], a[84:87], v[100:103]\n"
"v_cvt_pk_bf16_f32 v74, v88, v89\n"
"v_cvt_pk_bf16_f32 v75, v90, v91\n"
"v_mfma_f32_16x16x32_bf16 v[100:103], a[224:227], a[68:71], v[100:103]\n"
"v_mul_f32 v92, s26, v92\n"
"v_mul_f32 v93, s26, v93\n"
"v_mfma_f32_16x16x32_bf16 v[104:107], a[224:227], a[72:75], v[104:107]\n"
"v_mul_f32 v94, s26, v94\n"
"v_mul_f32 v95, s26, v95\n"
"v_mfma_f32_16x16x32_bf16 v[104:107], a[228:231], a[88:91], v[104:107]\n"
"v_cvt_pk_bf16_f32 v78, v92, v93\n"
"v_cvt_pk_bf16_f32 v79, v94, v95\n"
"v_mfma_f32_16x16x32_bf16 v[104:107], a[232:235], a[104:107], v[104:107]\n"
"v_mul_f32 v96, s26, v96\n"
"v_mul_f32 v97, s26, v97\n"
"s_mov_b32 s24, 3840\n"
"v_mfma_f32_16x16x32_bf16 v[104:107], a[236:239], a[120:123], v[104:107]\n"
"v_mul_f32 v98, s26, v98\n"
"v_mul_f32 v99, s26, v99\n"
"buffer_load_dword v62, v26, s[88:91], s24 offen\n"
"v_mfma_f32_16x16x32_bf16 v[108:111], a[236:239], a[124:127], v[108:111]\n"
"v_cvt_pk_bf16_f32 v96, v96, v97\n"
"v_cvt_pk_bf16_f32 v97, v98, v99\n"
"v_mfma_f32_16x16x32_bf16 v[108:111], a[232:235], a[108:111], v[108:111]\n"
"v_mul_f32 v100, s26, v100\n"
"v_mul_f32 v101, s26, v101\n"
"v_mfma_f32_16x16x32_bf16 v[108:111], a[228:231], a[92:95], v[108:111]\n"
"v_mul_f32 v102, s26, v102\n"
"v_mul_f32 v103, s26, v103\n"
"v_mfma_f32_16x16x32_bf16 v[108:111], a[224:227], a[76:79], v[108:111]\n"
"s_waitcnt lgkmcnt(8)\n"
"v_cvt_pk_bf16_f32 v100, v100, v101\n"
"v_mfma_f32_16x16x32_bf16 v[112:115], a[192:195], a[64:67], v[112:115]\n"
"ds_read_b64_tr_b16 a[224:225], v21 offset:136\n"
"ds_read_b64_tr_b16 a[226:227], v21 offset:1672\n"
"v_mfma_f32_16x16x32_bf16 v[112:115], a[196:199], a[80:83], v[112:115]\n"
"ds_read_b64_tr_b16 a[228:229], v19 offset:12424\n"
"ds_read_b64_tr_b16 a[230:231], v19 offset:13960\n"
"v_mfma_f32_16x16x32_bf16 v[112:115], a[200:203], a[96:99], v[112:115]\n"
"ds_read_b64_tr_b16 a[232:233], v21 offset:12424\n"
"ds_read_b64_tr_b16 a[234:235], v21 offset:13960\n"
"v_mfma_f32_16x16x32_bf16 v[112:115], a[204:207], a[112:115], v[112:115]\n"
"ds_read_b64_tr_b16 a[236:237], v21 offset:24712\n"
"ds_read_b64_tr_b16 a[238:239], v21 offset:26248\n"
"v_mfma_f32_16x16x32_bf16 v[116:119], a[204:207], a[116:119], v[116:119]\n"
"v_cvt_pk_bf16_f32 v101, v102, v103\n"
"v_mul_f32 v104, s26, v104\n"
"v_mfma_f32_16x16x32_bf16 v[116:119], a[200:203], a[100:103], v[116:119]\n"
"v_mul_f32 v105, s26, v105\n"
"v_mul_f32 v106, s26, v106\n"
"v_mfma_f32_16x16x32_bf16 v[116:119], a[196:199], a[84:87], v[116:119]\n"
"v_mul_f32 v107, s26, v107\n"
"v_cvt_pk_bf16_f32 v104, v104, v105\n"
"v_mfma_f32_16x16x32_bf16 v[116:119], a[192:195], a[68:71], v[116:119]\n"
"v_cvt_pk_bf16_f32 v105, v106, v107\n"
"v_mul_f32 v108, s26, v108\n"
"v_mfma_f32_16x16x32_bf16 v[120:123], a[192:195], a[72:75], v[120:123]\n"
"v_mul_f32 v109, s26, v109\n"
"v_mul_f32 v110, s26, v110\n"
"v_mfma_f32_16x16x32_bf16 v[120:123], a[196:199], a[88:91], v[120:123]\n"
"v_mul_f32 v111, s26, v111\n"
"v_cvt_pk_bf16_f32 v108, v108, v109\n"
"v_mfma_f32_16x16x32_bf16 v[120:123], a[200:203], a[104:107], v[120:123]\n"
"v_cvt_pk_bf16_f32 v109, v110, v111\n"
"v_mul_f32 v112, s26, v112\n"
"v_mfma_f32_16x16x32_bf16 v[120:123], a[204:207], a[120:123], v[120:123]\n"
"v_mul_f32 v113, s26, v113\n"
"v_mul_f32 v114, s26, v114\n"
"v_mfma_f32_16x16x32_bf16 v[124:127], a[204:207], a[124:127], v[124:127]\n"
"v_mul_f32 v115, s26, v115\n"
"v_cvt_pk_bf16_f32 v98, v112, v113\n"
"v_mfma_f32_16x16x32_bf16 v[124:127], a[200:203], a[108:111], v[124:127]\n"
"v_cvt_pk_bf16_f32 v99, v114, v115\n"
"v_mul_f32 v116, s26, v116\n"
"v_mfma_f32_16x16x32_bf16 v[124:127], a[196:199], a[92:95], v[124:127]\n"
"v_mul_f32 v117, s26, v117\n"
"v_mul_f32 v118, s26, v118\n"
"v_mfma_f32_16x16x32_bf16 v[124:127], a[192:195], a[76:79], v[124:127]\n"
"s_waitcnt lgkmcnt(8)\n"
"v_mul_f32 v119, s26, v119\n"
"v_mfma_f32_16x16x32_bf16 v[128:131], a[208:211], a[64:67], v[128:131]\n"
"ds_read_b64_tr_b16 a[192:193], v22 offset:128\n"
"ds_read_b64_tr_b16 a[194:195], v22 offset:1664\n"
"v_mfma_f32_16x16x32_bf16 v[128:131], a[212:215], a[80:83], v[128:131]\n"
"ds_read_b64_tr_b16 a[196:197], v20 offset:12416\n"
"ds_read_b64_tr_b16 a[198:199], v20 offset:13952\n"
"v_mfma_f32_16x16x32_bf16 v[128:131], a[216:219], a[96:99], v[128:131]\n"
"ds_read_b64_tr_b16 a[200:201], v22 offset:12416\n"
"ds_read_b64_tr_b16 a[202:203], v22 offset:13952\n"
"v_mfma_f32_16x16x32_bf16 v[128:131], a[220:223], a[112:115], v[128:131]\n"
"ds_read_b64_tr_b16 a[204:205], v22 offset:24704\n"
"ds_read_b64_tr_b16 a[206:207], v22 offset:26240\n"
"v_mfma_f32_16x16x32_bf16 v[132:135], a[220:223], a[116:119], v[132:135]\n"
"v_cvt_pk_bf16_f32 v102, v116, v117\n"
"v_cvt_pk_bf16_f32 v103, v118, v119\n"
"v_mfma_f32_16x16x32_bf16 v[132:135], a[216:219], a[100:103], v[132:135]\n"
"v_mul_f32 v120, s26, v120\n"
"v_mul_f32 v121, s26, v121\n"
"v_mfma_f32_16x16x32_bf16 v[132:135], a[212:215], a[84:87], v[132:135]\n"
"v_mul_f32 v122, s26, v122\n"
"v_mul_f32 v123, s26, v123\n"
"v_mfma_f32_16x16x32_bf16 v[132:135], a[208:211], a[68:71], v[132:135]\n"
"v_cvt_pk_bf16_f32 v106, v120, v121\n"
"v_cvt_pk_bf16_f32 v107, v122, v123\n"
"v_mfma_f32_16x16x32_bf16 v[136:139], a[208:211], a[72:75], v[136:139]\n"
"v_mul_f32 v124, s26, v124\n"
"v_mul_f32 v125, s26, v125\n"
"v_mfma_f32_16x16x32_bf16 v[136:139], a[212:215], a[88:91], v[136:139]\n"
"v_mul_f32 v126, s26, v126\n"
"v_mul_f32 v127, s26, v127\n"
"v_mfma_f32_16x16x32_bf16 v[136:139], a[216:219], a[104:107], v[136:139]\n"
"v_cvt_pk_bf16_f32 v110, v124, v125\n"
"v_cvt_pk_bf16_f32 v111, v126, v127\n"
"v_mfma_f32_16x16x32_bf16 v[136:139], a[220:223], a[120:123], v[136:139]\n"
"v_mul_f32 v128, s26, v128\n"
"v_mul_f32 v129, s26, v129\n"
"v_mfma_f32_16x16x32_bf16 v[140:143], a[220:223], a[124:127], v[140:143]\n"
"v_mul_f32 v130, s26, v130\n"
"v_mul_f32 v131, s26, v131\n"
"v_mfma_f32_16x16x32_bf16 v[140:143], a[216:219], a[108:111], v[140:143]\n"
"v_cvt_pk_bf16_f32 v128, v128, v129\n"
"v_cvt_pk_bf16_f32 v129, v130, v131\n"
"v_mfma_f32_16x16x32_bf16 v[140:143], a[212:215], a[92:95], v[140:143]\n"
"v_mul_f32 v132, s26, v132\n"
"v_mul_f32 v133, s26, v133\n"
"v_mfma_f32_16x16x32_bf16 v[140:143], a[208:211], a[76:79], v[140:143]\n"
"s_waitcnt lgkmcnt(8)\n"
"v_mul_f32 v134, s26, v134\n"
"v_mfma_f32_16x16x32_bf16 v[144:147], a[224:227], a[64:67], v[144:147]\n"
"ds_read_b64_tr_b16 a[208:209], v22 offset:136\n"
"ds_read_b64_tr_b16 a[210:211], v22 offset:1672\n"
"v_mfma_f32_16x16x32_bf16 v[144:147], a[228:231], a[80:83], v[144:147]\n"
"ds_read_b64_tr_b16 a[212:213], v20 offset:12424\n"
"ds_read_b64_tr_b16 a[214:215], v20 offset:13960\n"
"v_mfma_f32_16x16x32_bf16 v[144:147], a[232:235], a[96:99], v[144:147]\n"
"ds_read_b64_tr_b16 a[216:217], v22 offset:12424\n"
"ds_read_b64_tr_b16 a[218:219], v22 offset:13960\n"
"v_mfma_f32_16x16x32_bf16 v[144:147], a[236:239], a[112:115], v[144:147]\n"
"ds_read_b64_tr_b16 a[220:221], v22 offset:24712\n"
"ds_read_b64_tr_b16 a[222:223], v22 offset:26248\n"
"v_mfma_f32_16x16x32_bf16 v[148:151], a[236:239], a[116:119], v[148:151]\n"
"v_mul_f32 v135, s26, v135\n"
"v_cvt_pk_bf16_f32 v132, v132, v133\n"
"v_mfma_f32_16x16x32_bf16 v[148:151], a[232:235], a[100:103], v[148:151]\n"
"v_cvt_pk_bf16_f32 v133, v134, v135\n"
"v_mul_f32 v136, s26, v136\n"
"v_mfma_f32_16x16x32_bf16 v[148:151], a[228:231], a[84:87], v[148:151]\n"
"v_mul_f32 v137, s26, v137\n"
"v_mul_f32 v138, s26, v138\n"
"v_mfma_f32_16x16x32_bf16 v[148:151], a[224:227], a[68:71], v[148:151]\n"
"v_mul_f32 v139, s26, v139\n"
"v_cvt_pk_bf16_f32 v136, v136, v137\n"
"v_mfma_f32_16x16x32_bf16 v[152:155], a[224:227], a[72:75], v[152:155]\n"
"v_cvt_pk_bf16_f32 v137, v138, v139\n"
"v_mul_f32 v140, s26, v140\n"
"v_mfma_f32_16x16x32_bf16 v[152:155], a[228:231], a[88:91], v[152:155]\n"
"v_mul_f32 v141, s26, v141\n"
"v_mul_f32 v142, s26, v142\n"
"v_mfma_f32_16x16x32_bf16 v[152:155], a[232:235], a[104:107], v[152:155]\n"
"v_mul_f32 v143, s26, v143\n"
"v_cvt_pk_bf16_f32 v140, v140, v141\n"
"v_mfma_f32_16x16x32_bf16 v[152:155], a[236:239], a[120:123], v[152:155]\n"
"v_cvt_pk_bf16_f32 v141, v142, v143\n"
"v_mul_f32 v144, s26, v144\n"
"v_mfma_f32_16x16x32_bf16 v[156:159], a[236:239], a[124:127], v[156:159]\n"
"v_mul_f32 v145, s26, v145\n"
"v_mul_f32 v146, s26, v146\n"
"s_mov_b32 s24, 7680\n"
"v_mfma_f32_16x16x32_bf16 v[156:159], a[232:235], a[108:111], v[156:159]\n"
"v_mul_f32 v147, s26, v147\n"
"v_cvt_pk_bf16_f32 v130, v144, v145\n"
"buffer_load_dword v62, v26, s[88:91], s24 offen\n"
"v_mfma_f32_16x16x32_bf16 v[156:159], a[228:231], a[92:95], v[156:159]\n"
"v_cvt_pk_bf16_f32 v131, v146, v147\n"
"v_mul_f32 v148, s26, v148\n"
"v_mfma_f32_16x16x32_bf16 v[156:159], a[224:227], a[76:79], v[156:159]\n"
"s_waitcnt lgkmcnt(8)\n"
"v_mul_f32 v149, s26, v149\n"
"v_mfma_f32_16x16x32_bf16 v[160:163], a[192:195], a[64:67], v[160:163]\n"
"ds_read_b64_tr_b16 a[224:225], v21 offset:256\n"
"ds_read_b64_tr_b16 a[226:227], v21 offset:1792\n"
"v_mfma_f32_16x16x32_bf16 v[160:163], a[196:199], a[80:83], v[160:163]\n"
"ds_read_b64_tr_b16 a[228:229], v19 offset:12544\n"
"ds_read_b64_tr_b16 a[230:231], v19 offset:14080\n"
"v_mfma_f32_16x16x32_bf16 v[160:163], a[200:203], a[96:99], v[160:163]\n"
"ds_read_b64_tr_b16 a[232:233], v21 offset:12544\n"
"ds_read_b64_tr_b16 a[234:235], v21 offset:14080\n"
"v_mfma_f32_16x16x32_bf16 v[160:163], a[204:207], a[112:115], v[160:163]\n"
"ds_read_b64_tr_b16 a[236:237], v21 offset:24832\n"
"ds_read_b64_tr_b16 a[238:239], v21 offset:26368\n"
"v_mfma_f32_16x16x32_bf16 v[164:167], a[204:207], a[116:119], v[164:167]\n"
"v_mul_f32 v150, s26, v150\n"
"v_mul_f32 v151, s26, v151\n"
"v_mfma_f32_16x16x32_bf16 v[164:167], a[200:203], a[100:103], v[164:167]\n"
"v_cvt_pk_bf16_f32 v134, v148, v149\n"
"v_cvt_pk_bf16_f32 v135, v150, v151\n"
"v_mfma_f32_16x16x32_bf16 v[164:167], a[196:199], a[84:87], v[164:167]\n"
"v_mul_f32 v152, s26, v152\n"
"v_mul_f32 v153, s26, v153\n"
"v_mfma_f32_16x16x32_bf16 v[164:167], a[192:195], a[68:71], v[164:167]\n"
"v_mul_f32 v154, s26, v154\n"
"v_mul_f32 v155, s26, v155\n"
"v_mfma_f32_16x16x32_bf16 v[168:171], a[192:195], a[72:75], v[168:171]\n"
"v_cvt_pk_bf16_f32 v138, v152, v153\n"
"v_cvt_pk_bf16_f32 v139, v154, v155\n"
"v_mfma_f32_16x16x32_bf16 v[168:171], a[196:199], a[88:91], v[168:171]\n"
"v_mul_f32 v156, s26, v156\n"
"v_mul_f32 v157, s26, v157\n"
"v_mfma_f32_16x16x32_bf16 v[168:171], a[200:203], a[104:107], v[168:171]\n"
"v_mul_f32 v158, s26, v158\n"
"v_mul_f32 v159, s26, v159\n"
"v_mfma_f32_16x16x32_bf16 v[168:171], a[204:207], a[120:123], v[168:171]\n"
"v_cvt_pk_bf16_f32 v142, v156, v157\n"
"v_cvt_pk_bf16_f32 v143, v158, v159\n"
"v_mfma_f32_16x16x32_bf16 v[172:175], a[204:207], a[124:127], v[172:175]\n"
"v_mul_f32 v160, s26, v160\n"
"v_mul_f32 v161, s26, v161\n"
"v_mfma_f32_16x16x32_bf16 v[172:175], a[200:203], a[108:111], v[172:175]\n"
"v_mul_f32 v162, s26, v162\n"
"v_mul_f32 v163, s26, v163\n"
"v_mfma_f32_16x16x32_bf16 v[172:175], a[196:199], a[92:95], v[172:175]\n"
"v_cvt_pk_bf16_f32 v160, v160, v161\n"
"v_cvt_pk_bf16_f32 v161, v162, v163\n"
"v_mfma_f32_16x16x32_bf16 v[172:175], a[192:195], a[76:79], v[172:175]\n"
"s_waitcnt lgkmcnt(8)\n"
"v_mul_f32 v164, s26, v164\n"
"v_mfma_f32_16x16x32_bf16 v[176:179], a[208:211], a[64:67], v[176:179]\n"
"ds_read_b64_tr_b16 a[192:193], v21 offset:264\n"
"ds_read_b64_tr_b16 a[194:195], v21 offset:1800\n"
"v_mfma_f32_16x16x32_bf16 v[176:179], a[212:215], a[80:83], v[176:179]\n"
"ds_read_b64_tr_b16 a[196:197], v19 offset:12552\n"
"ds_read_b64_tr_b16 a[198:199], v19 offset:14088\n"
"v_mfma_f32_16x16x32_bf16 v[176:179], a[216:219], a[96:99], v[176:179]\n"
"ds_read_b64_tr_b16 a[200:201], v21 offset:12552\n"
"ds_read_b64_tr_b16 a[202:203], v21 offset:14088\n"
"v_mfma_f32_16x16x32_bf16 v[176:179], a[220:223], a[112:115], v[176:179]\n"
"ds_read_b64_tr_b16 a[204:205], v21 offset:24840\n"
"ds_read_b64_tr_b16 a[206:207], v21 offset:26376\n"
"v_mfma_f32_16x16x32_bf16 v[180:183], a[220:223], a[116:119], v[180:183]\n"
"v_mul_f32 v165, s26, v165\n"
"v_mul_f32 v166, s26, v166\n"
"v_mfma_f32_16x16x32_bf16 v[180:183], a[216:219], a[100:103], v[180:183]\n"
"v_mul_f32 v167, s26, v167\n"
"v_cvt_pk_bf16_f32 v164, v164, v165\n"
"v_mfma_f32_16x16x32_bf16 v[180:183], a[212:215], a[84:87], v[180:183]\n"
"v_cvt_pk_bf16_f32 v165, v166, v167\n"
"v_mul_f32 v168, s26, v168\n"
"v_mfma_f32_16x16x32_bf16 v[180:183], a[208:211], a[68:71], v[180:183]\n"
"v_mul_f32 v169, s26, v169\n"
"v_mul_f32 v170, s26, v170\n"
"v_mfma_f32_16x16x32_bf16 v[184:187], a[208:211], a[72:75], v[184:187]\n"
"v_mul_f32 v171, s26, v171\n"
"v_cvt_pk_bf16_f32 v168, v168, v169\n"
"v_mfma_f32_16x16x32_bf16 v[184:187], a[212:215], a[88:91], v[184:187]\n"
"v_cvt_pk_bf16_f32 v169, v170, v171\n"
"v_mul_f32 v172, s26, v172\n"
"v_mfma_f32_16x16x32_bf16 v[184:187], a[216:219], a[104:107], v[184:187]\n"
"v_mul_f32 v173, s26, v173\n"
"v_mul_f32 v174, s26, v174\n"
"v_mfma_f32_16x16x32_bf16 v[184:187], a[220:223], a[120:123], v[184:187]\n"
"v_mul_f32 v175, s26, v175\n"
"v_cvt_pk_bf16_f32 v172, v172, v173\n"
"v_mfma_f32_16x16x32_bf16 v[188:191], a[220:223], a[124:127], v[188:191]\n"
"v_cvt_pk_bf16_f32 v173, v174, v175\n"
"v_mul_f32 v176, s26, v176\n"
"v_mfma_f32_16x16x32_bf16 v[188:191], a[216:219], a[108:111], v[188:191]\n"
"v_mul_f32 v177, s26, v177\n"
"v_mul_f32 v178, s26, v178\n"
"v_mfma_f32_16x16x32_bf16 v[188:191], a[212:215], a[92:95], v[188:191]\n"
"v_mul_f32 v179, s26, v179\n"
"v_cvt_pk_bf16_f32 v162, v176, v177\n"
"v_mfma_f32_16x16x32_bf16 v[188:191], a[208:211], a[76:79], v[188:191]\n"
"s_waitcnt lgkmcnt(8)\n"
"v_cvt_pk_bf16_f32 v163, v178, v179\n"
"v_mfma_f32_16x16x32_bf16 v[192:195], a[224:227], a[64:67], v[192:195]\n"
"ds_read_b64_tr_b16 a[208:209], v22 offset:256\n"
"ds_read_b64_tr_b16 a[210:211], v22 offset:1792\n"
"v_mfma_f32_16x16x32_bf16 v[192:195], a[228:231], a[80:83], v[192:195]\n"
"ds_read_b64_tr_b16 a[212:213], v20 offset:12544\n"
"ds_read_b64_tr_b16 a[214:215], v20 offset:14080\n"
"v_mfma_f32_16x16x32_bf16 v[192:195], a[232:235], a[96:99], v[192:195]\n"
"ds_read_b64_tr_b16 a[216:217], v22 offset:12544\n"
"ds_read_b64_tr_b16 a[218:219], v22 offset:14080\n"
"v_mfma_f32_16x16x32_bf16 v[192:195], a[236:239], a[112:115], v[192:195]\n"
"ds_read_b64_tr_b16 a[220:221], v22 offset:24832\n"
"ds_read_b64_tr_b16 a[222:223], v22 offset:26368\n"
"v_mfma_f32_16x16x32_bf16 v[196:199], a[236:239], a[116:119], v[196:199]\n"
"v_mul_f32 v180, s26, v180\n"
"v_mul_f32 v181, s26, v181\n"
"v_mfma_f32_16x16x32_bf16 v[196:199], a[232:235], a[100:103], v[196:199]\n"
"v_mul_f32 v182, s26, v182\n"
"v_mul_f32 v183, s26, v183\n"
"v_mfma_f32_16x16x32_bf16 v[196:199], a[228:231], a[84:87], v[196:199]\n"
"v_cvt_pk_bf16_f32 v166, v180, v181\n"
"v_cvt_pk_bf16_f32 v167, v182, v183\n"
"v_mfma_f32_16x16x32_bf16 v[196:199], a[224:227], a[68:71], v[196:199]\n"
"v_mul_f32 v184, s26, v184\n"
"v_mul_f32 v185, s26, v185\n"
"v_mfma_f32_16x16x32_bf16 v[200:203], a[224:227], a[72:75], v[200:203]\n"
"v_mul_f32 v186, s26, v186\n"
"v_mul_f32 v187, s26, v187\n"
"v_mfma_f32_16x16x32_bf16 v[200:203], a[228:231], a[88:91], v[200:203]\n"
"v_cvt_pk_bf16_f32 v170, v184, v185\n"
"v_cvt_pk_bf16_f32 v171, v186, v187\n"
"v_mfma_f32_16x16x32_bf16 v[200:203], a[232:235], a[104:107], v[200:203]\n"
"v_mul_f32 v188, s26, v188\n"
"v_mul_f32 v189, s26, v189\n"
"v_mfma_f32_16x16x32_bf16 v[200:203], a[236:239], a[120:123], v[200:203]\n"
"v_mul_f32 v190, s26, v190\n"
"v_mul_f32 v191, s26, v191\n"
"v_mfma_f32_16x16x32_bf16 v[204:207], a[236:239], a[124:127], v[204:207]\n"
"v_cvt_pk_bf16_f32 v174, v188, v189\n"
"v_cvt_pk_bf16_f32 v175, v190, v191\n"
"v_mfma_f32_16x16x32_bf16 v[204:207], a[232:235], a[108:111], v[204:207]\n"
"v_mul_f32 v192, s26, v192\n"
"v_mul_f32 v193, s26, v193\n"
"v_mfma_f32_16x16x32_bf16 v[204:207], a[228:231], a[92:95], v[204:207]\n"
"v_mul_f32 v194, s26, v194\n"
"v_mul_f32 v195, s26, v195\n"
"s_mov_b32 s24, 11520\n"
"v_mfma_f32_16x16x32_bf16 v[204:207], a[224:227], a[76:79], v[204:207]\n"
"s_waitcnt lgkmcnt(8)\n"
"v_cvt_pk_bf16_f32 v192, v192, v193\n"
"buffer_load_dword v62, v26, s[88:91], s24 offen\n"
"v_mfma_f32_16x16x32_bf16 v[208:211], a[192:195], a[64:67], v[208:211]\n"
"ds_read_b64_tr_b16 a[224:225], v22 offset:264\n"
"ds_read_b64_tr_b16 a[226:227], v22 offset:1800\n"
"v_mfma_f32_16x16x32_bf16 v[208:211], a[196:199], a[80:83], v[208:211]\n"
"ds_read_b64_tr_b16 a[228:229], v20 offset:12552\n"
"ds_read_b64_tr_b16 a[230:231], v20 offset:14088\n"
"v_mfma_f32_16x16x32_bf16 v[208:211], a[200:203], a[96:99], v[208:211]\n"
"ds_read_b64_tr_b16 a[232:233], v22 offset:12552\n"
"ds_read_b64_tr_b16 a[234:235], v22 offset:14088\n"
"v_mfma_f32_16x16x32_bf16 v[208:211], a[204:207], a[112:115], v[208:211]\n"
"ds_read_b64_tr_b16 a[236:237], v22 offset:24840\n"
"ds_read_b64_tr_b16 a[238:239], v22 offset:26376\n"
"v_mfma_f32_16x16x32_bf16 v[212:215], a[204:207], a[116:119], v[212:215]\n"
"v_cvt_pk_bf16_f32 v193, v194, v195\n"
"v_mul_f32 v196, s26, v196\n"
"v_mfma_f32_16x16x32_bf16 v[212:215], a[200:203], a[100:103], v[212:215]\n"
"v_mul_f32 v197, s26, v197\n"
"v_mul_f32 v198, s26, v198\n"
"v_mfma_f32_16x16x32_bf16 v[212:215], a[196:199], a[84:87], v[212:215]\n"
"v_mul_f32 v199, s26, v199\n"
"v_cvt_pk_bf16_f32 v196, v196, v197\n"
"v_mfma_f32_16x16x32_bf16 v[212:215], a[192:195], a[68:71], v[212:215]\n"
"v_cvt_pk_bf16_f32 v197, v198, v199\n"
"v_mul_f32 v200, s26, v200\n"
"v_mfma_f32_16x16x32_bf16 v[216:219], a[192:195], a[72:75], v[216:219]\n"
"v_mul_f32 v201, s26, v201\n"
"v_mul_f32 v202, s26, v202\n"
"v_mfma_f32_16x16x32_bf16 v[216:219], a[196:199], a[88:91], v[216:219]\n"
"v_mul_f32 v203, s26, v203\n"
"v_cvt_pk_bf16_f32 v200, v200, v201\n"
"v_mfma_f32_16x16x32_bf16 v[216:219], a[200:203], a[104:107], v[216:219]\n"
"v_cvt_pk_bf16_f32 v201, v202, v203\n"
"v_mul_f32 v204, s26, v204\n"
"v_mfma_f32_16x16x32_bf16 v[216:219], a[204:207], a[120:123], v[216:219]\n"
"v_mul_f32 v205, s26, v205\n"
"v_mul_f32 v206, s26, v206\n"
"v_mfma_f32_16x16x32_bf16 v[220:223], a[204:207], a[124:127], v[220:223]\n"
"v_mul_f32 v207, s26, v207\n"
"v_cvt_pk_bf16_f32 v204, v204, v205\n"
"v_mfma_f32_16x16x32_bf16 v[220:223], a[200:203], a[108:111], v[220:223]\n"
"v_cvt_pk_bf16_f32 v205, v206, v207\n"
"v_mul_f32 v208, s26, v208\n"
"v_mfma_f32_16x16x32_bf16 v[220:223], a[196:199], a[92:95], v[220:223]\n"
"v_mul_f32 v209, s26, v209\n"
"v_mul_f32 v210, s26, v210\n"
"v_mfma_f32_16x16x32_bf16 v[220:223], a[192:195], a[76:79], v[220:223]\n"
"s_waitcnt vmcnt(63) lgkmcnt(0)\n"
"s_barrier\n"
"v_mfma_f32_16x16x32_bf16 v[224:227], a[208:211], a[64:67], v[224:227]\n"
"ds_read_b128 a[192:195], v0 offset:0\n"
"ds_read_b128 a[196:199], v0 offset:6144\n"
"v_mfma_f32_16x16x32_bf16 v[224:227], a[212:215], a[80:83], v[224:227]\n"
"ds_read_b128 a[200:203], v1 offset:0\n"
"ds_read_b128 a[204:207], v1 offset:6144\n"
"v_mfma_f32_16x16x32_bf16 v[224:227], a[216:219], a[96:99], v[224:227]\n"
"v_mul_f32 v211, s26, v211\n"
"v_cvt_pk_bf16_f32 v194, v208, v209\n"
"v_mfma_f32_16x16x32_bf16 v[224:227], a[220:223], a[112:115], v[224:227]\n"
"v_cvt_pk_bf16_f32 v195, v210, v211\n"
"v_mul_f32 v212, s26, v212\n"
"v_mfma_f32_16x16x32_bf16 v[228:231], a[220:223], a[116:119], v[228:231]\n"
"v_mul_f32 v213, s26, v213\n"
"v_mul_f32 v214, s26, v214\n"
"v_mfma_f32_16x16x32_bf16 v[228:231], a[216:219], a[100:103], v[228:231]\n"
"v_mul_f32 v215, s26, v215\n"
"v_cvt_pk_bf16_f32 v198, v212, v213\n"
"v_mfma_f32_16x16x32_bf16 v[228:231], a[212:215], a[84:87], v[228:231]\n"
"v_cvt_pk_bf16_f32 v199, v214, v215\n"
"v_mul_f32 v216, s26, v216\n"
"v_mfma_f32_16x16x32_bf16 v[228:231], a[208:211], a[68:71], v[228:231]\n"
"v_mul_f32 v217, s26, v217\n"
"v_mul_f32 v218, s26, v218\n"
"v_mfma_f32_16x16x32_bf16 v[232:235], a[208:211], a[72:75], v[232:235]\n"
"v_mul_f32 v219, s26, v219\n"
"v_cvt_pk_bf16_f32 v202, v216, v217\n"
"v_mfma_f32_16x16x32_bf16 v[232:235], a[212:215], a[88:91], v[232:235]\n"
"v_cvt_pk_bf16_f32 v203, v218, v219\n"
"v_mul_f32 v220, s26, v220\n"
"v_mfma_f32_16x16x32_bf16 v[232:235], a[216:219], a[104:107], v[232:235]\n"
"v_mul_f32 v221, s26, v221\n"
"v_mul_f32 v222, s26, v222\n"
"v_mfma_f32_16x16x32_bf16 v[232:235], a[220:223], a[120:123], v[232:235]\n"
"v_mul_f32 v223, s26, v223\n"
"v_cvt_pk_bf16_f32 v206, v220, v221\n"
"v_mfma_f32_16x16x32_bf16 v[236:239], a[220:223], a[124:127], v[236:239]\n"
"v_cvt_pk_bf16_f32 v207, v222, v223\n"
"v_mul_f32 v224, s26, v224\n"
"v_mfma_f32_16x16x32_bf16 v[236:239], a[216:219], a[108:111], v[236:239]\n"
"v_mul_f32 v225, s26, v225\n"
"v_mul_f32 v226, s26, v226\n"
"v_mfma_f32_16x16x32_bf16 v[236:239], a[212:215], a[92:95], v[236:239]\n"
"v_mul_f32 v227, s26, v227\n"
"v_cvt_pk_bf16_f32 v224, v224, v225\n"
"v_mfma_f32_16x16x32_bf16 v[236:239], a[208:211], a[76:79], v[236:239]\n"
"v_cvt_pk_bf16_f32 v225, v226, v227\n"
"v_mul_f32 v228, s26, v228\n"
"v_mfma_f32_16x16x32_bf16 v[240:243], a[224:227], a[64:67], v[240:243]\n"
"ds_read_b128 a[208:211], v0 offset:128\n"
"ds_read_b128 a[212:215], v0 offset:6272\n"
"v_mfma_f32_16x16x32_bf16 v[240:243], a[228:231], a[80:83], v[240:243]\n"
"ds_read_b128 a[216:219], v1 offset:128\n"
"ds_read_b128 a[220:223], v1 offset:6272\n"
"v_mfma_f32_16x16x32_bf16 v[240:243], a[232:235], a[96:99], v[240:243]\n"
"v_mul_f32 v229, s26, v229\n"
"v_mul_f32 v230, s26, v230\n"
"v_mfma_f32_16x16x32_bf16 v[240:243], a[236:239], a[112:115], v[240:243]\n"
"v_mul_f32 v231, s26, v231\n"
"v_cvt_pk_bf16_f32 v228, v228, v229\n"
"v_mfma_f32_16x16x32_bf16 v[244:247], a[236:239], a[116:119], v[244:247]\n"
"v_cvt_pk_bf16_f32 v229, v230, v231\n"
"v_mul_f32 v232, s26, v232\n"
"v_mfma_f32_16x16x32_bf16 v[244:247], a[232:235], a[100:103], v[244:247]\n"
"v_mul_f32 v233, s26, v233\n"
"v_mul_f32 v234, s26, v234\n"
"v_mfma_f32_16x16x32_bf16 v[244:247], a[228:231], a[84:87], v[244:247]\n"
"v_mul_f32 v235, s26, v235\n"
"v_cvt_pk_bf16_f32 v232, v232, v233\n"
"v_mfma_f32_16x16x32_bf16 v[244:247], a[224:227], a[68:71], v[244:247]\n"
"v_cvt_pk_bf16_f32 v233, v234, v235\n"
"v_mul_f32 v236, s26, v236\n"
"v_mfma_f32_16x16x32_bf16 v[248:251], a[224:227], a[72:75], v[248:251]\n"
"v_mul_f32 v237, s26, v237\n"
"v_mul_f32 v238, s26, v238\n"
"v_mfma_f32_16x16x32_bf16 v[248:251], a[228:231], a[88:91], v[248:251]\n"
"v_mul_f32 v239, s26, v239\n"
"v_cvt_pk_bf16_f32 v236, v236, v237\n"
"v_mfma_f32_16x16x32_bf16 v[248:251], a[232:235], a[104:107], v[248:251]\n"
"v_cvt_pk_bf16_f32 v237, v238, v239\n"
"v_mul_f32 v240, s26, v240\n"
"v_mfma_f32_16x16x32_bf16 v[248:251], a[236:239], a[120:123], v[248:251]\n"
"v_mul_f32 v241, s26, v241\n"
"v_mul_f32 v242, s26, v242\n"
"v_mfma_f32_16x16x32_bf16 v[252:255], a[236:239], a[124:127], v[252:255]\n"
"v_mul_f32 v243, s26, v243\n"
"v_cvt_pk_bf16_f32 v226, v240, v241\n"
"v_mfma_f32_16x16x32_bf16 v[252:255], a[232:235], a[108:111], v[252:255]\n"
"v_cvt_pk_bf16_f32 v227, v242, v243\n"
"v_mul_f32 v244, s26, v244\n"
"v_mfma_f32_16x16x32_bf16 v[252:255], a[228:231], a[92:95], v[252:255]\n"
"v_mul_f32 v245, s26, v245\n"
"v_mul_f32 v246, s26, v246\n"
"v_mfma_f32_16x16x32_bf16 v[252:255], a[224:227], a[76:79], v[252:255]\n"
"s_waitcnt lgkmcnt(4)\n"
"v_mul_f32 v247, s26, v247\n"
".Lmha_lp:\n"
"v_mfma_f32_16x16x32_bf16 v[80:83], a[192:195], v[64:67], 0\n"
"ds_read_b128 a[224:227], v0 offset:256\n"
"ds_read_b128 a[228:231], v0 offset:6400\n"
"v_mfma_f32_16x16x32_bf16 v[84:87], a[192:195], v[68:71], 0\n"
"ds_read_b128 a[232:235], v1 offset:256\n"
"ds_read_b128 a[236:239], v1 offset:6400\n"
"v_mfma_f32_16x16x32_bf16 v[88:91], a[192:195], v[72:75], 0\n"
"v_cvt_pk_bf16_f32 v230, v244, v245\n"
"v_cvt_pk_bf16_f32 v231, v246, v247\n"
"v_mfma_f32_16x16x32_bf16 v[92:95], a[192:195], v[76:79], 0\n"
"v_mul_f32 v248, s26, v248\n"
"v_mul_f32 v249, s26, v249\n"
"v_mfma_f32_16x16x32_bf16 v[124:127], a[196:199], v[76:79], 0\n"
"v_mul_f32 v250, s26, v250\n"
"v_mul_f32 v251, s26, v251\n"
"v_mfma_f32_16x16x32_bf16 v[120:123], a[196:199], v[72:75], 0\n"
"v_cvt_pk_bf16_f32 v234, v248, v249\n"
"v_cvt_pk_bf16_f32 v235, v250, v251\n"
"v_mfma_f32_16x16x32_bf16 v[116:119], a[196:199], v[68:71], 0\n"
"v_mul_f32 v252, s26, v252\n"
"v_mul_f32 v253, s26, v253\n"
"v_mfma_f32_16x16x32_bf16 v[112:115], a[196:199], v[64:67], 0\n"
"v_mul_f32 v254, s26, v254\n"
"v_mul_f32 v255, s26, v255\n"
"v_mfma_f32_16x16x32_bf16 v[80:83], a[200:203], v[96:99], v[80:83]\n"
"v_cvt_pk_bf16_f32 v238, v252, v253\n"
"v_cvt_pk_bf16_f32 v239, v254, v255\n"
"v_mfma_f32_16x16x32_bf16 v[84:87], a[200:203], v[100:103], v[84:87]\n"
"v_mfma_f32_16x16x32_bf16 v[88:91], a[200:203], v[104:107], v[88:91]\n"
"v_mfma_f32_16x16x32_bf16 v[92:95], a[200:203], v[108:111], v[92:95]\n"
"v_mfma_f32_16x16x32_bf16 v[124:127], a[204:207], v[108:111], v[124:127]\n"
"v_mfma_f32_16x16x32_bf16 v[120:123], a[204:207], v[104:107], v[120:123]\n"
"v_mfma_f32_16x16x32_bf16 v[116:119], a[204:207], v[100:103], v[116:119]\n"
"v_mfma_f32_16x16x32_bf16 v[112:115], a[204:207], v[96:99], v[112:115]\n"
"s_waitcnt lgkmcnt(4)\n"
"v_mfma_f32_16x16x32_bf16 v[80:83], a[208:211], v[128:131], v[80:83]\n"
"v_mfma_f32_16x16x32_bf16 v[84:87], a[208:211], v[132:135], v[84:87]\n"
"v_mfma_f32_16x16x32_bf16 v[88:91], a[208:211], v[136:139], v[88:91]\n"
"v_mfma_f32_16x16x32_bf16 v[92:95], a[208:211], v[140:143], v[92:95]\n"
"v_mfma_f32_16x16x32_bf16 v[124:127], a[212:215], v[140:143], v[124:127]\n"
"v_mfma_f32_16x16x32_bf16 v[120:123], a[212:215], v[136:139], v[120:123]\n"
"v_mfma_f32_16x16x32_bf16 v[116:119], a[212:215], v[132:135], v[116:119]\n"
"v_mfma_f32_16x16x32_bf16 v[112:115], a[212:215], v[128:131], v[112:115]\n"
"v_mfma_f32_16x16x32_bf16 v[80:83], a[216:219], v[160:163], v[80:83]\n"
"v_mfma_f32_16x16x32_bf16 v[84:87], a[216:219], v[164:167], v[84:87]\n"
"v_mfma_f32_16x16x32_bf16 v[88:91], a[216:219], v[168:171], v[88:91]\n"
"v_mfma_f32_16x16x32_bf16 v[92:95], a[216:219], v[172:175], v[92:95]\n"
"v_mfma_f32_16x16x32_bf16 v[124:127], a[220:223], v[172:175], v[124:127]\n"
"v_mfma_f32_16x16x32_bf16 v[120:123], a[220:223], v[168:171], v[120:123]\n"
"v_mfma_f32_16x16x32_bf16 v[116:119], a[220:223], v[164:167], v[116:119]\n"
"v_mfma_f32_16x16x32_bf16 v[112:115], a[220:223], v[160:163], v[112:115]\n"
"s_waitcnt lgkmcnt(0)\n"
"v_mfma_f32_16x16x32_bf16 v[80:83], a[224:227], v[192:195], v[80:83]\n"
"v_mfma_f32_16x16x32_bf16 v[84:87], a[224:227], v[196:199], v[84:87]\n"
"v_mfma_f32_16x16x32_bf16 v[88:91], a[224:227], v[200:203], v[88:91]\n"
"v_mfma_f32_16x16x32_bf16 v[92:95], a[224:227], v[204:207], v[92:95]\n"
"v_mfma_f32_16x16x32_bf16 v[124:127], a[228:231], v[204:207], v[124:127]\n"
"v_mfma_f32_16x16x32_bf16 v[120:123], a[228:231], v[200:203], v[120:123]\n"
"v_mfma_f32_16x16x32_bf16 v[116:119], a[228:231], v[196:199], v[116:119]\n"
"v_mfma_f32_16x16x32_bf16 v[112:115], a[228:231], v[192:195], v[112:115]\n"
"v_mfma_f32_16x16x32_bf16 v[80:83], a[232:235], v[224:227], v[80:83]\n"
"v_mfma_f32_16x16x32_bf16 v[84:87], a[232:235], v[228:231], v[84:87]\n"
"v_mfma_f32_16x16x32_bf16 v[88:91], a[232:235], v[232:235], v[88:91]\n"
"v_mfma_f32_16x16x32_bf16 v[92:95], a[232:235], v[236:239], v[92:95]\n"
"v_mfma_f32_16x16x32_bf16 v[124:127], a[236:239], v[236:239], v[124:127]\n"
"v_mfma_f32_16x16x32_bf16 v[120:123], a[236:239], v[232:235], v[120:123]\n"
"v_mfma_f32_16x16x32_bf16 v[116:119], a[236:239], v[228:231], v[116:119]\n"
"v_mfma_f32_16x16x32_bf16 v[112:115], a[236:239], v[224:227], v[112:115]\n"
"v_mov_b32 v220, v15\n"
"v_mov_b32 v221, v16\n"
"v_mov_b32 v222, v17\n"
"s_mov_b64 s[52:53], s[44:45]\n"
"s_mov_b64 s[54:55], s[46:47]\n"
"v_mov_b32 v252, v12\n"
"v_mov_b32 v253, v13\n"
"v_mov_b32 v254, v14\n"
"s_mov_b64 s[48:49], s[40:41]\n"
"s_mov_b64 s[50:51], s[42:43]\n"
"v_mov_b32 v255, v27\n"
"s_add_u32 s20, s20, .Lmha_lp-.Lmha_q7\n"
"s_addc_u32 s21, s21, 0\n"
"s_mov_b32 s22, .Lmha_cend-.Lmha_lp\n"
"s_lshr_b32 s7, s6, 6\n"
"s_lshl_b32 s7, s7, 3\n"
"s_lshr_b32 s7, 0x65655a5a, s7\n"
"s_and_b32 s7, s7, 0xff\n"
"s_mov_b32 s6, 0\n"
"v_mov_b32 v30, 0x7f800000\n"
"v_exp_f32 v32, v80\n"
"v_exp_f32 v33, v81\n"
"v_exp_f32 v34, v82\n"
"v_exp_f32 v35, v83\n"
"v_add_f32 v24, v32, v33\n"
"v_cvt_pk_bf16_f32 v144, v32, v33\n"
"v_exp_f32 v36, v84\n"
"v_add_f32 v24, v24, v34\n"
"v_exp_f32 v37, v85\n"
"v_add_f32 v24, v24, v35\n"
"v_cvt_pk_bf16_f32 v145, v34, v35\n"
"v_exp_f32 v38, v86\n"
"v_exp_f32 v39, v87\n"
"v_add_f32 v25, v36, v37\n"
"v_cvt_pk_bf16_f32 v148, v36, v37\n"
"v_exp_f32 v32, v88\n"
"v_add_f32 v25, v25, v38\n"
"v_exp_f32 v33, v89\n"
"v_add_f32 v25, v25, v39\n"
"v_cvt_pk_bf16_f32 v149, v38, v39\n"
"v_exp_f32 v34, v90\n"
"v_exp_f32 v35, v91\n"
"v_add_f32 v26, v32, v33\n"
"v_cvt_pk_bf16_f32 v152, v32, v33\n"
"v_exp_f32 v36, v92\n"
"v_add_f32 v26, v26, v34\n"
"v_exp_f32 v37, v93\n"
"v_add_f32 v26, v26, v35\n"
"v_cvt_pk_bf16_f32 v153, v34, v35\n"
"s_waitcnt vmcnt(6) lgkmcnt(0)\n"
"s_barrier\n"
"v_exp_f32 v38, v94\n"
"v_exp_f32 v39, v95\n"
"s_mov_b32 s24, 36864\n"
"s_add_u32 m0, s76, 36864\n"
"v_add_f32 v27, v36, v37\n"
"v_cvt_pk_bf16_f32 v156, v36, v37\n"
"buffer_load_dwordx4 v12, s[40:43], s24 offen nt lds\n"
"v_exp_f32 v32, v112\n"
"v_add_f32 v27, v27, v38\n"
"v_exp_f32 v33, v113\n"
"v_add_f32 v27, v27, v39\n"
"v_cvt_pk_bf16_f32 v157, v38, v39\n"
"v_exp_f32 v34, v114\n"
"v_add_f32 v24, v24, v32\n"
"v_exp_f32 v35, v115\n"
"v_add_f32 v24, v24, v33\n"
"ds_read_b128 a[192:195], v0 offset:12288\n"
"buffer_load_dwordx4 v13, s[40:43], s24 offen offset:1024 nt lds\n"
"v_cvt_pk_bf16_f32 v146, v32, v33\n"
"v_exp_f32 v36, v116\n"
"v_add_f32 v24, v24, v34\n"
"v_exp_f32 v37, v117\n"
"v_add_f32 v24, v24, v35\n"
"v_cvt_pk_bf16_f32 v147, v34, v35\n"
"v_exp_f32 v38, v118\n"
"v_add_f32 v25, v25, v36\n"
"ds_read_b128 a[196:199], v1 offset:12288\n"
"buffer_load_dwordx4 v14, s[40:43], s24 offen offset:2048 nt lds\n"
"v_exp_f32 v39, v119\n"
"v_add_f32 v25, v25, v37\n"
"v_cvt_pk_bf16_f32 v150, v36, v37\n"
"v_exp_f32 v32, v120\n"
"v_add_f32 v25, v25, v38\n"
"v_exp_f32 v33, v121\n"
"v_add_f32 v25, v25, v39\n"
"ds_read_b128 a[200:203], v0 offset:12416\n"
"s_mov_b32 s24, 24576\n"
"s_add_u32 m0, s76, 98304\n"
"v_cvt_pk_bf16_f32 v151, v38, v39\n"
"v_exp_f32 v34, v122\n"
"v_add_f32 v26, v26, v32\n"
"v_exp_f32 v35, v123\n"
"v_add_f32 v26, v26, v33\n"
"v_cvt_pk_bf16_f32 v154, v32, v33\n"
"v_exp_f32 v36, v124\n"
"v_add_f32 v26, v26, v34\n"
"ds_read_b128 a[204:207], v1 offset:12416\n"
"buffer_load_dwordx4 v15, s[44:47], s24 offen nt lds\n"
"v_exp_f32 v37, v125\n"
"v_add_f32 v26, v26, v35\n"
"v_cvt_pk_bf16_f32 v155, v34, v35\n"
"v_exp_f32 v38, v126\n"
"v_add_f32 v27, v27, v36\n"
"v_exp_f32 v39, v127\n"
"v_add_f32 v27, v27, v37\n"
"ds_read_b128 a[208:211], v0 offset:12544\n"
"buffer_load_dwordx4 v16, s[44:47], s24 offen offset:1024 nt lds\n"
"v_cvt_pk_bf16_f32 v158, v36, v37\n"
"v_add_f32 v27, v27, v38\n"
"ds_read_b128 a[212:215], v1 offset:12544\n"
"v_add_f32 v27, v27, v39\n"
"v_cvt_pk_bf16_f32 v159, v38, v39\n"
"v_max3_f32 v28, v24, v25, v26\n"
"v_min3_f32 v29, v24, v25, v26\n"
"v_max_f32 v28, v28, v27\n"
"v_min_f32 v29, v29, v27\n"
"v_cmp_gt_f32 vcc, 0x35800000, v29\n"
"s_nop 1\n"
"v_cndmask_b32 v28, v28, v30, vcc\n"
"buffer_load_dwordx4 v17, s[44:47], s24 offen offset:2048 nt lds\n"
"v_cmp_lt_f32 vcc, s27, v28\n"
"s_waitcnt lgkmcnt(0)\n"
"s_cbranch_vccnz .Lmha_rare_tf\n"
".Lmha_ret_tf:\n"
"v_mfma_f32_16x16x32_bf16 v[80:83], a[192:195], v[64:67], v[40:43]\n"
"ds_read_b128 a[216:219], v0 offset:18432\n"
"v_mfma_f32_16x16x32_bf16 v[80:83], a[196:199], v[96:99], v[80:83]\n"
"ds_read_b128 a[220:223], v1 offset:18432\n"
"v_mfma_f32_16x16x32_bf16 v[80:83], a[200:203], v[128:131], v[80:83]\n"
"ds_read_b128 a[224:227], v0 offset:18560\n"
"v_mfma_f32_16x16x32_bf16 v[80:83], a[204:207], v[160:163], v[80:83]\n"
"ds_read_b128 a[228:231], v1 offset:18560\n"
"v_mfma_f32_16x16x32_bf16 v[80:83], a[208:211], v[192:195], v[80:83]\n"
"ds_read_b128 a[232:235], v0 offset:18688\n"
"v_mfma_f32_16x16x32_bf16 v[80:83], a[212:215], v[224:227], v[80:83]\n"
"ds_read_b128 a[236:239], v1 offset:18688\n"
"s_memrealtime s[4:5]\n"
"v_mfma_f32_16x16x32_bf16 v[84:87], a[212:215], v[228:231], v[44:47]\n"
"v_mov_b32 v20, v24\n"
"v_mfma_f32_16x16x32_bf16 v[84:87], a[208:211], v[196:199], v[84:87]\n"
"v_mov_b32 v21, v25\n"
"v_mfma_f32_16x16x32_bf16 v[84:87], a[204:207], v[164:167], v[84:87]\n"
"v_mov_b32 v22, v26\n"
"v_mfma_f32_16x16x32_bf16 v[84:87], a[200:203], v[132:135], v[84:87]\n"
"v_mov_b32 v23, v27\n"
"v_mfma_f32_16x16x32_bf16 v[84:87], a[196:199], v[100:103], v[84:87]\n"
"v_exp_f32 v32, v80\n"
"v_mfma_f32_16x16x32_bf16 v[84:87], a[192:195], v[68:71], v[84:87]\n"
"v_exp_f32 v33, v81\n"
"v_mfma_f32_16x16x32_bf16 v[88:91], a[192:195], v[72:75], v[48:51]\n"
"v_add_f32 v24, v32, v33\n"
"v_cvt_pk_bf16_f32 v176, v32, v33\n"
"s_add_u32 s24, s84, 0\n"
"v_mfma_f32_16x16x32_bf16 v[88:91], a[196:199], v[104:107], v[88:91]\n"
"s_add_u32 m0, s76, 147456\n"
"s_nop 0\n"
"buffer_load_dwordx4 v56, s[60:63], s24 offen lds\n"
"v_mfma_f32_16x16x32_bf16 v[88:91], a[200:203], v[136:139], v[88:91]\n"
"v_exp_f32 v34, v82\n"
"v_mfma_f32_16x16x32_bf16 v[88:91], a[204:207], v[168:171], v[88:91]\n"
"v_add_f32 v24, v24, v34\n"
"v_mfma_f32_16x16x32_bf16 v[88:91], a[208:211], v[200:203], v[88:91]\n"
"v_exp_f32 v35, v83\n"
"v_mfma_f32_16x16x32_bf16 v[88:91], a[212:215], v[232:235], v[88:91]\n"
"v_add_f32 v24, v24, v35\n"
"v_cvt_pk_bf16_f32 v177, v34, v35\n"
"v_mfma_f32_16x16x32_bf16 v[92:95], a[212:215], v[236:239], v[52:55]\n"
"v_mfma_f32_16x16x32_bf16 v[92:95], a[208:211], v[204:207], v[92:95]\n"
"v_exp_f32 v36, v84\n"
"v_mfma_f32_16x16x32_bf16 v[92:95], a[204:207], v[172:175], v[92:95]\n"
"v_mfma_f32_16x16x32_bf16 v[92:95], a[200:203], v[140:143], v[92:95]\n"
"v_exp_f32 v37, v85\n"
"buffer_load_dwordx4 v57, s[60:63], s24 offen offset:1024 lds\n"
"v_mfma_f32_16x16x32_bf16 v[92:95], a[196:199], v[108:111], v[92:95]\n"
"v_add_f32 v25, v36, v37\n"
"v_cvt_pk_bf16_f32 v180, v36, v37\n"
"v_mfma_f32_16x16x32_bf16 v[92:95], a[192:195], v[76:79], v[92:95]\n"
"s_waitcnt lgkmcnt(0)\n"
"v_mfma_f32_16x16x32_bf16 v[112:115], a[216:219], v[64:67], v[40:43]\n"
"ds_read_b64_tr_b16 a[192:193], v4 offset:12288\n"
"ds_read_b64_tr_b16 a[194:195], v4 offset:18432\n"
"v_mfma_f32_16x16x32_bf16 v[112:115], a[220:223], v[96:99], v[112:115]\n"
"v_exp_f32 v38, v86\n"
"v_mfma_f32_16x16x32_bf16 v[112:115], a[224:227], v[128:131], v[112:115]\n"
"v_exp_f32 v39, v87\n"
"v_mfma_f32_16x16x32_bf16 v[112:115], a[228:231], v[160:163], v[112:115]\n"
"ds_read_b64_tr_b16 a[196:197], v5 offset:12288\n"
"ds_read_b64_tr_b16 a[198:199], v5 offset:18432\n"
"v_mfma_f32_16x16x32_bf16 v[112:115], a[232:235], v[192:195], v[112:115]\n"
"v_add_f32 v25, v25, v38\n"
"v_add_f32 v25, v25, v39\n"
"v_mfma_f32_16x16x32_bf16 v[112:115], a[236:239], v[224:227], v[112:115]\n"
"v_exp_f32 v32, v88\n"
"buffer_load_dwordx4 v58, s[60:63], s24 offen offset:2048 lds\n"
"v_mfma_f32_16x16x32_bf16 v[116:119], a[236:239], v[228:231], v[44:47]\n"
"ds_read_b64_tr_b16 a[200:201], v6 offset:12288\n"
"ds_read_b64_tr_b16 a[202:203], v6 offset:18432\n"
"v_mfma_f32_16x16x32_bf16 v[116:119], a[232:235], v[196:199], v[116:119]\n"
"v_exp_f32 v33, v89\n"
"v_mfma_f32_16x16x32_bf16 v[116:119], a[228:231], v[164:167], v[116:119]\n"
"v_cvt_pk_bf16_f32 v181, v38, v39\n"
"v_add_f32 v26, v32, v33\n"
"v_mfma_f32_16x16x32_bf16 v[116:119], a[224:227], v[132:135], v[116:119]\n"
"ds_read_b64_tr_b16 a[204:205], v7 offset:12288\n"
"ds_read_b64_tr_b16 a[206:207], v7 offset:18432\n"
"v_mfma_f32_16x16x32_bf16 v[116:119], a[220:223], v[100:103], v[116:119]\n"
"v_exp_f32 v34, v90\n"
"v_mfma_f32_16x16x32_bf16 v[116:119], a[216:219], v[68:71], v[116:119]\n"
"v_cvt_pk_bf16_f32 v184, v32, v33\n"
"v_add_f32 v26, v26, v34\n"
"v_mfma_f32_16x16x32_bf16 v[120:123], a[216:219], v[72:75], v[48:51]\n"
"ds_read_b64_tr_b16 a[208:209], v4 offset:12416\n"
"ds_read_b64_tr_b16 a[210:211], v4 offset:18560\n"
"v_mfma_f32_16x16x32_bf16 v[120:123], a[220:223], v[104:107], v[120:123]\n"
"v_exp_f32 v35, v91\n"
"v_mfma_f32_16x16x32_bf16 v[120:123], a[224:227], v[136:139], v[120:123]\n"
"v_add_f32 v26, v26, v35\n"
"v_cvt_pk_bf16_f32 v185, v34, v35\n"
"v_mfma_f32_16x16x32_bf16 v[120:123], a[228:231], v[168:171], v[120:123]\n"
"ds_read_b64_tr_b16 a[212:213], v5 offset:12416\n"
"ds_read_b64_tr_b16 a[214:215], v5 offset:18560\n"
"v_mfma_f32_16x16x32_bf16 v[120:123], a[232:235], v[200:203], v[120:123]\n"
"v_exp_f32 v36, v92\n"
"v_mfma_f32_16x16x32_bf16 v[120:123], a[236:239], v[232:235], v[120:123]\n"
"v_exp_f32 v37, v93\n"
"v_mfma_f32_16x16x32_bf16 v[124:127], a[236:239], v[236:239], v[52:55]\n"
"v_add_f32 v27, v36, v37\n"
"v_cvt_pk_bf16_f32 v188, v36, v37\n"
"v_mfma_f32_16x16x32_bf16 v[124:127], a[232:235], v[204:207], v[124:127]\n"
"v_mfma_f32_16x16x32_bf16 v[124:127], a[228:231], v[172:175], v[124:127]\n"
"v_exp_f32 v38, v94\n"
"v_mfma_f32_16x16x32_bf16 v[124:127], a[224:227], v[140:143], v[124:127]\n"
"v_add_f32 v27, v27, v38\n"
"v_mfma_f32_16x16x32_bf16 v[124:127], a[220:223], v[108:111], v[124:127]\n"
"v_exp_f32 v39, v95\n"
"v_mfma_f32_16x16x32_bf16 v[124:127], a[216:219], v[76:79], v[124:127]\n"
"s_waitcnt lgkmcnt(0)\n"
"v_add_f32 v27, v27, v39\n"
"v_mfma_f32_16x16x32_bf16 a[0:3], a[192:195], v[144:147], 0\n"
"ds_read_b64_tr_b16 a[216:217], v6 offset:12416\n"
"ds_read_b64_tr_b16 a[218:219], v6 offset:18560\n"
"v_mfma_f32_16x16x32_bf16 a[4:7], a[192:195], v[148:151], 0\n"
"v_exp_f32 v32, v112\n"
"v_mfma_f32_16x16x32_bf16 a[8:11], a[192:195], v[152:155], 0\n"
"ds_read_b64_tr_b16 a[220:221], v7 offset:12416\n"
"ds_read_b64_tr_b16 a[222:223], v7 offset:18560\n"
"v_mfma_f32_16x16x32_bf16 a[12:15], a[192:195], v[156:159], 0\n"
"v_exp_f32 v33, v113\n"
"v_mfma_f32_16x16x32_bf16 a[28:31], a[196:199], v[156:159], 0\n"
"ds_read_b64_tr_b16 a[224:225], v4 offset:12544\n"
"ds_read_b64_tr_b16 a[226:227], v4 offset:18688\n"
"v_mfma_f32_16x16x32_bf16 a[24:27], a[196:199], v[152:155], 0\n"
"v_cvt_pk_bf16_f32 v189, v38, v39\n"
"v_add_f32 v24, v24, v32\n"
"v_mfma_f32_16x16x32_bf16 a[20:23], a[196:199], v[148:151], 0\n"
"ds_read_b64_tr_b16 a[228:229], v5 offset:12544\n"
"ds_read_b64_tr_b16 a[230:231], v5 offset:18688\n"
"v_mfma_f32_16x16x32_bf16 a[16:19], a[196:199], v[144:147], 0\n"
"v_exp_f32 v34, v114\n"
"v_mfma_f32_16x16x32_bf16 a[32:35], a[200:203], v[144:147], 0\n"
"ds_read_b64_tr_b16 a[232:233], v6 offset:12544\n"
"ds_read_b64_tr_b16 a[234:235], v6 offset:18688\n"
"v_mfma_f32_16x16x32_bf16 a[36:39], a[200:203], v[148:151], 0\n"
"v_exp_f32 v35, v115\n"
"v_mfma_f32_16x16x32_bf16 a[40:43], a[200:203], v[152:155], 0\n"
"ds_read_b64_tr_b16 a[236:237], v7 offset:12544\n"
"ds_read_b64_tr_b16 a[238:239], v7 offset:18688\n"
"v_mfma_f32_16x16x32_bf16 a[44:47], a[200:203], v[156:159], 0\n"
"v_exp_f32 v36, v116\n"
"v_mfma_f32_16x16x32_bf16 a[60:63], a[204:207], v[156:159], 0\n"
"v_add_f32 v24, v24, v33\n"
"v_cvt_pk_bf16_f32 v178, v32, v33\n"
"v_mfma_f32_16x16x32_bf16 a[56:59], a[204:207], v[152:155], 0\n"
"v_exp_f32 v37, v117\n"
"v_mfma_f32_16x16x32_bf16 a[52:55], a[204:207], v[148:151], 0\n"
"v_add_f32 v24, v24, v34\n"
"v_add_f32 v24, v24, v35\n"
"v_mfma_f32_16x16x32_bf16 a[48:51], a[204:207], v[144:147], 0\n"
"v_cvt_pk_bf16_f32 v179, v34, v35\n"
"v_add_f32 v25, v25, v36\n"
"v_mfma_f32_16x16x32_bf16 a[64:67], a[208:211], v[144:147], 0\n"
"v_exp_f32 v38, v118\n"
"v_mfma_f32_16x16x32_bf16 a[68:71], a[208:211], v[148:151], 0\n"
"s_waitcnt vmcnt(6) lgkmcnt(0)\n"
"s_barrier\n"
"v_mfma_f32_16x16x32_bf16 a[72:75], a[208:211], v[152:155], 0\n"
"s_mov_b32 s24, 49152\n"
"s_add_u32 m0, s76, 49152\n"
"v_add_f32 v25, v25, v37\n"
"v_mfma_f32_16x16x32_bf16 a[76:79], a[208:211], v[156:159], 0\n"
"buffer_load_dwordx4 v12, s[40:43], s24 offen nt lds\n"
"v_cvt_pk_bf16_f32 v182, v36, v37\n"
"v_mfma_f32_16x16x32_bf16 a[92:95], a[212:215], v[156:159], 0\n"
"v_exp_f32 v39, v119\n"
"v_mfma_f32_16x16x32_bf16 a[88:91], a[212:215], v[152:155], 0\n"
"v_exp_f32 v32, v120\n"
"v_mfma_f32_16x16x32_bf16 a[84:87], a[212:215], v[148:151], 0\n"
"v_add_f32 v25, v25, v38\n"
"v_add_f32 v25, v25, v39\n"
"v_mfma_f32_16x16x32_bf16 a[80:83], a[212:215], v[144:147], 0\n"
"v_exp_f32 v33, v121\n"
"v_mfma_f32_16x16x32_bf16 a[96:99], a[216:219], v[144:147], 0\n"
"ds_read_b128 a[192:195], v0 offset:24576\n"
"buffer_load_dwordx4 v13, s[40:43], s24 offen offset:1024 nt lds\n"
"v_mfma_f32_16x16x32_bf16 a[100:103], a[216:219], v[148:151], 0\n"
"v_cvt_pk_bf16_f32 v183, v38, v39\n"
"v_add_f32 v26, v26, v32\n"
"v_mfma_f32_16x16x32_bf16 a[104:107], a[216:219], v[152:155], 0\n"
"v_exp_f32 v34, v122\n"
"v_mfma_f32_16x16x32_bf16 a[108:111], a[216:219], v[156:159], 0\n"
"v_add_f32 v26, v26, v33\n"
"v_cvt_pk_bf16_f32 v186, v32, v33\n"
"v_mfma_f32_16x16x32_bf16 a[124:127], a[220:223], v[156:159], 0\n"
"ds_read_b128 a[196:199], v1 offset:24576\n"
"buffer_load_dwordx4 v14, s[40:43], s24 offen offset:2048 nt lds\n"
"v_mfma_f32_16x16x32_bf16 a[120:123], a[220:223], v[152:155], 0\n"
"v_exp_f32 v35, v123\n"
"v_mfma_f32_16x16x32_bf16 a[116:119], a[220:223], v[148:151], 0\n"
"v_add_f32 v26, v26, v34\n"
"v_add_f32 v26, v26, v35\n"
"v_mfma_f32_16x16x32_bf16 a[112:115], a[220:223], v[144:147], 0\n"
"v_exp_f32 v36, v124\n"
"v_mfma_f32_16x16x32_bf16 a[128:131], a[224:227], v[144:147], 0\n"
"ds_read_b128 a[200:203], v0 offset:24704\n"
"s_mov_b32 s24, 36864\n"
"s_add_u32 m0, s76, 110592\n"
"v_mfma_f32_16x16x32_bf16 a[132:135], a[224:227], v[148:151], 0\n"
"v_exp_f32 v37, v125\n"
"v_mfma_f32_16x16x32_bf16 a[136:139], a[224:227], v[152:155], 0\n"
"v_cvt_pk_bf16_f32 v187, v34, v35\n"
"v_add_f32 v27, v27, v36\n"
"v_mfma_f32_16x16x32_bf16 a[140:143], a[224:227], v[156:159], 0\n"
"v_add_f32 v27, v27, v37\n"
"v_cvt_pk_bf16_f32 v190, v36, v37\n"
"v_mfma_f32_16x16x32_bf16 a[156:159], a[228:231], v[156:159], 0\n"
"ds_read_b128 a[204:207], v1 offset:24704\n"
"buffer_load_dwordx4 v15, s[44:47], s24 offen nt lds\n"
"v_mfma_f32_16x16x32_bf16 a[152:155], a[228:231], v[152:155], 0\n"
"v_exp_f32 v38, v126\n"
"v_mfma_f32_16x16x32_bf16 a[148:151], a[228:231], v[148:151], 0\n"
"v_add_f32 v27, v27, v38\n"
"s_max_u32 s6, s6, s4\n"
"v_mfma_f32_16x16x32_bf16 a[144:147], a[228:231], v[144:147], 0\n"
"v_exp_f32 v39, v127\n"
"v_mfma_f32_16x16x32_bf16 a[160:163], a[232:235], v[144:147], 0\n"
"ds_read_b128 a[208:211], v0 offset:24832\n"
"buffer_load_dwordx4 v16, s[44:47], s24 offen offset:1024 nt lds\n"
"v_mfma_f32_16x16x32_bf16 a[164:167], a[232:235], v[148:151], 0\n"
"ds_read_b128 a[212:215], v1 offset:24832\n"
"v_add_f32 v27, v27, v39\n"
"v_mfma_f32_16x16x32_bf16 a[168:171], a[232:235], v[152:155], 0\n"
"v_cvt_pk_bf16_f32 v191, v38, v39\n"
"s_add_u32 s6, s6, s7\n"
"v_mfma_f32_16x16x32_bf16 a[172:175], a[232:235], v[156:159], 0\n"
"v_mfma_f32_16x16x32_bf16 a[188:191], a[236:239], v[156:159], 0\n"
"v_max3_f32 v28, v24, v25, v26\n"
"v_mfma_f32_16x16x32_bf16 a[184:187], a[236:239], v[152:155], 0\n"
"v_max_f32 v28, v28, v27\n"
"buffer_load_dwordx4 v17, s[44:47], s24 offen offset:2048 nt lds\n"
"v_mfma_f32_16x16x32_bf16 a[180:183], a[236:239], v[148:151], 0\n"
"v_cmp_lt_f32 vcc, s27, v28\n"
"v_mfma_f32_16x16x32_bf16 a[176:179], a[236:239], v[144:147], 0\n"
"s_waitcnt lgkmcnt(0)\n"
"s_cbranch_vccnz .Lmha_rare_t0\n"
".Lmha_ret_t0:\n"
".Lmha_loop:\n"
"v_mfma_f32_16x16x32_bf16 v[80:83], a[192:195], v[64:67], v[40:43]\n"
"ds_read_b128 a[216:219], v0 offset:30720\n"
"s_add_u32 s8, s8, 73728\n"
"v_mfma_f32_16x16x32_bf16 v[80:83], a[196:199], v[96:99], v[80:83]\n"
"ds_read_b128 a[220:223], v1 offset:30720\n"
"s_add_u32 s14, s14, 73728\n"
"v_mfma_f32_16x16x32_bf16 v[80:83], a[200:203], v[128:131], v[80:83]\n"
"ds_read_b128 a[224:227], v0 offset:30848\n"
"v_mfma_f32_16x16x32_bf16 v[80:83], a[204:207], v[160:163], v[80:83]\n"
"ds_read_b128 a[228:231], v1 offset:30848\n"
"v_mfma_f32_16x16x32_bf16 v[80:83], a[208:211], v[192:195], v[80:83]\n"
"ds_read_b128 a[232:235], v0 offset:30976\n"
"v_mfma_f32_16x16x32_bf16 v[80:83], a[212:215], v[224:227], v[80:83]\n"
"ds_read_b128 a[236:239], v1 offset:30976\n"
"s_memrealtime s[4:5]\n"
"v_mfma_f32_16x16x32_bf16 v[84:87], a[212:215], v[228:231], v[44:47]\n"
"v_add_f32 v20, v20, v24\n"
"v_mfma_f32_16x16x32_bf16 v[84:87], a[208:211], v[196:199], v[84:87]\n"
"v_add_f32 v21, v21, v25\n"
"v_mfma_f32_16x16x32_bf16 v[84:87], a[204:207], v[164:167], v[84:87]\n"
"v_add_f32 v22, v22, v26\n"
"v_mfma_f32_16x16x32_bf16 v[84:87], a[200:203], v[132:135], v[84:87]\n"
"v_add_f32 v23, v23, v27\n"
"v_mfma_f32_16x16x32_bf16 v[84:87], a[196:199], v[100:103], v[84:87]\n"
"v_exp_f32 v32, v80\n"
"v_mfma_f32_16x16x32_bf16 v[84:87], a[192:195], v[68:71], v[84:87]\n"
"v_exp_f32 v33, v81\n"
"v_mfma_f32_16x16x32_bf16 v[88:91], a[192:195], v[72:75], v[48:51]\n"
"v_add_f32 v24, v32, v33\n"
"v_cvt_pk_bf16_f32 v144, v32, v33\n"
"v_mfma_f32_16x16x32_bf16 v[88:91], a[196:199], v[104:107], v[88:91]\n"
"v_mfma_f32_16x16x32_bf16 v[88:91], a[200:203], v[136:139], v[88:91]\n"
"v_exp_f32 v34, v82\n"
"v_mfma_f32_16x16x32_bf16 v[88:91], a[204:207], v[168:171], v[88:91]\n"
"v_add_f32 v24, v24, v34\n"
"v_mfma_f32_16x16x32_bf16 v[88:91], a[208:211], v[200:203], v[88:91]\n"
"v_exp_f32 v35, v83\n"
"v_mfma_f32_16x16x32_bf16 v[88:91], a[212:215], v[232:235], v[88:91]\n"
"v_add_f32 v24, v24, v35\n"
"v_cvt_pk_bf16_f32 v145, v34, v35\n"
"v_mfma_f32_16x16x32_bf16 v[92:95], a[212:215], v[236:239], v[52:55]\n"
"v_mfma_f32_16x16x32_bf16 v[92:95], a[208:211], v[204:207], v[92:95]\n"
"v_exp_f32 v36, v84\n"
"v_mfma_f32_16x16x32_bf16 v[92:95], a[204:207], v[172:175], v[92:95]\n"
"v_mfma_f32_16x16x32_bf16 v[92:95], a[200:203], v[140:143], v[92:95]\n"
"v_exp_f32 v37, v85\n"
"v_mfma_f32_16x16x32_bf16 v[92:95], a[196:199], v[108:111], v[92:95]\n"
"v_add_f32 v25, v36, v37\n"
"v_cvt_pk_bf16_f32 v148, v36, v37\n"
"v_mfma_f32_16x16x32_bf16 v[92:95], a[192:195], v[76:79], v[92:95]\n"
"s_waitcnt lgkmcnt(0)\n"
"v_mfma_f32_16x16x32_bf16 v[112:115], a[216:219], v[64:67], v[40:43]\n"
"ds_read_b64_tr_b16 a[192:193], v4 offset:24576\n"
"ds_read_b64_tr_b16 a[194:195], v4 offset:30720\n"
"v_mfma_f32_16x16x32_bf16 v[112:115], a[220:223], v[96:99], v[112:115]\n"
"v_exp_f32 v38, v86\n"
"v_mfma_f32_16x16x32_bf16 v[112:115], a[224:227], v[128:131], v[112:115]\n"
"v_exp_f32 v39, v87\n"
"v_mfma_f32_16x16x32_bf16 v[112:115], a[228:231], v[160:163], v[112:115]\n"
"ds_read_b64_tr_b16 a[196:197], v5 offset:24576\n"
"ds_read_b64_tr_b16 a[198:199], v5 offset:30720\n"
"v_mfma_f32_16x16x32_bf16 v[112:115], a[232:235], v[192:195], v[112:115]\n"
"v_add_f32 v25, v25, v38\n"
"v_add_f32 v25, v25, v39\n"
"v_mfma_f32_16x16x32_bf16 v[112:115], a[236:239], v[224:227], v[112:115]\n"
"v_exp_f32 v32, v88\n"
"v_mfma_f32_16x16x32_bf16 v[116:119], a[236:239], v[228:231], v[44:47]\n"
"ds_read_b64_tr_b16 a[200:201], v6 offset:24576\n"
"ds_read_b64_tr_b16 a[202:203], v6 offset:30720\n"
"v_mfma_f32_16x16x32_bf16 v[116:119], a[232:235], v[196:199], v[116:119]\n"
"v_exp_f32 v33, v89\n"
"v_mfma_f32_16x16x32_bf16 v[116:119], a[228:231], v[164:167], v[116:119]\n"
"v_cvt_pk_bf16_f32 v149, v38, v39\n"
"v_add_f32 v26, v32, v33\n"
"v_mfma_f32_16x16x32_bf16 v[116:119], a[224:227], v[132:135], v[116:119]\n"
"ds_read_b64_tr_b16 a[204:205], v7 offset:24576\n"
"ds_read_b64_tr_b16 a[206:207], v7 offset:30720\n"
"v_mfma_f32_16x16x32_bf16 v[116:119], a[220:223], v[100:103], v[116:119]\n"
"v_exp_f32 v34, v90\n"
"v_mfma_f32_16x16x32_bf16 v[116:119], a[216:219], v[68:71], v[116:119]\n"
"v_cvt_pk_bf16_f32 v152, v32, v33\n"
"v_add_f32 v26, v26, v34\n"
"v_mfma_f32_16x16x32_bf16 v[120:123], a[216:219], v[72:75], v[48:51]\n"
"ds_read_b64_tr_b16 a[208:209], v4 offset:24704\n"
"ds_read_b64_tr_b16 a[210:211], v4 offset:30848\n"
"v_mfma_f32_16x16x32_bf16 v[120:123], a[220:223], v[104:107], v[120:123]\n"
"v_exp_f32 v35, v91\n"
"v_mfma_f32_16x16x32_bf16 v[120:123], a[224:227], v[136:139], v[120:123]\n"
"v_add_f32 v26, v26, v35\n"
"v_cvt_pk_bf16_f32 v153, v34, v35\n"
"v_mfma_f32_16x16x32_bf16 v[120:123], a[228:231], v[168:171], v[120:123]\n"
"ds_read_b64_tr_b16 a[212:213], v5 offset:24704\n"
"ds_read_b64_tr_b16 a[214:215], v5 offset:30848\n"
"v_mfma_f32_16x16x32_bf16 v[120:123], a[232:235], v[200:203], v[120:123]\n"
"v_exp_f32 v36, v92\n"
"v_mfma_f32_16x16x32_bf16 v[120:123], a[236:239], v[232:235], v[120:123]\n"
"v_exp_f32 v37, v93\n"
"v_mfma_f32_16x16x32_bf16 v[124:127], a[236:239], v[236:239], v[52:55]\n"
"v_add_f32 v27, v36, v37\n"
"v_cvt_pk_bf16_f32 v156, v36, v37\n"
"v_mfma_f32_16x16x32_bf16 v[124:127], a[232:235], v[204:207], v[124:127]\n"
"s_cmp_lt_u32 s4, s6\n"
"s_cbranch_scc1 .Lmha_slp1\n"
".Lmha_wk1:\n"
"v_mfma_f32_16x16x32_bf16 v[124:127], a[228:231], v[172:175], v[124:127]\n"
"v_exp_f32 v38, v94\n"
"v_mfma_f32_16x16x32_bf16 v[124:127], a[224:227], v[140:143], v[124:127]\n"
"v_add_f32 v27, v27, v38\n"
"s_sub_u32 s4, s4, 20\n"
"v_mfma_f32_16x16x32_bf16 v[124:127], a[220:223], v[108:111], v[124:127]\n"
"v_exp_f32 v39, v95\n"
"v_mfma_f32_16x16x32_bf16 v[124:127], a[216:219], v[76:79], v[124:127]\n"
"s_waitcnt lgkmcnt(0)\n"
"v_add_f32 v27, v27, v39\n"
"v_mfma_f32_16x16x32_bf16 a[0:3], a[192:195], v[176:179], a[0:3]\n"
"ds_read_b64_tr_b16 a[216:217], v6 offset:24704\n"
"ds_read_b64_tr_b16 a[218:219], v6 offset:30848\n"
"v_mfma_f32_16x16x32_bf16 a[4:7], a[192:195], v[180:183], a[4:7]\n"
"v_exp_f32 v32, v112\n"
"v_mfma_f32_16x16x32_bf16 a[8:11], a[192:195], v[184:187], a[8:11]\n"
"ds_read_b64_tr_b16 a[220:221], v7 offset:24704\n"
"ds_read_b64_tr_b16 a[222:223], v7 offset:30848\n"
"v_mfma_f32_16x16x32_bf16 a[12:15], a[192:195], v[188:191], a[12:15]\n"
"v_exp_f32 v33, v113\n"
"v_mfma_f32_16x16x32_bf16 a[28:31], a[196:199], v[188:191], a[28:31]\n"
"ds_read_b64_tr_b16 a[224:225], v4 offset:24832\n"
"ds_read_b64_tr_b16 a[226:227], v4 offset:30976\n"
"v_mfma_f32_16x16x32_bf16 a[24:27], a[196:199], v[184:187], a[24:27]\n"
"v_cvt_pk_bf16_f32 v157, v38, v39\n"
"v_add_f32 v24, v24, v32\n"
"v_mfma_f32_16x16x32_bf16 a[20:23], a[196:199], v[180:183], a[20:23]\n"
"ds_read_b64_tr_b16 a[228:229], v5 offset:24832\n"
"ds_read_b64_tr_b16 a[230:231], v5 offset:30976\n"
"v_mfma_f32_16x16x32_bf16 a[16:19], a[196:199], v[176:179], a[16:19]\n"
"v_exp_f32 v34, v114\n"
"v_mfma_f32_16x16x32_bf16 a[32:35], a[200:203], v[176:179], a[32:35]\n"
"ds_read_b64_tr_b16 a[232:233], v6 offset:24832\n"
"ds_read_b64_tr_b16 a[234:235], v6 offset:30976\n"
"v_mfma_f32_16x16x32_bf16 a[36:39], a[200:203], v[180:183], a[36:39]\n"
"v_exp_f32 v35, v115\n"
"v_mfma_f32_16x16x32_bf16 a[40:43], a[200:203], v[184:187], a[40:43]\n"
"ds_read_b64_tr_b16 a[236:237], v7 offset:24832\n"
"ds_read_b64_tr_b16 a[238:239], v7 offset:30976\n"
"v_mfma_f32_16x16x32_bf16 a[44:47], a[200:203], v[188:191], a[44:47]\n"
"v_exp_f32 v36, v116\n"
"v_mfma_f32_16x16x32_bf16 a[60:63], a[204:207], v[188:191], a[60:63]\n"
"v_add_f32 v24, v24, v33\n"
"v_cvt_pk_bf16_f32 v146, v32, v33\n"
"v_mfma_f32_16x16x32_bf16 a[56:59], a[204:207], v[184:187], a[56:59]\n"
"v_exp_f32 v37, v117\n"
"v_mfma_f32_16x16x32_bf16 a[52:55], a[204:207], v[180:183], a[52:55]\n"
"v_add_f32 v24, v24, v34\n"
"v_add_f32 v24, v24, v35\n"
"v_mfma_f32_16x16x32_bf16 a[48:51], a[204:207], v[176:179], a[48:51]\n"
"v_cvt_pk_bf16_f32 v147, v34, v35\n"
"v_add_f32 v25, v25, v36\n"
"v_mfma_f32_16x16x32_bf16 a[64:67], a[208:211], v[176:179], a[64:67]\n"
"v_exp_f32 v38, v118\n"
"v_mfma_f32_16x16x32_bf16 a[68:71], a[208:211], v[180:183], a[68:71]\n"
"s_waitcnt vmcnt(6) lgkmcnt(0)\n"
"s_barrier\n"
"v_mfma_f32_16x16x32_bf16 a[72:75], a[208:211], v[184:187], a[72:75]\n"
"s_add_u32 m0, s76, 61440\n"
"v_add_f32 v25, v25, v37\n"
"v_mfma_f32_16x16x32_bf16 a[76:79], a[208:211], v[188:191], a[76:79]\n"
"buffer_load_dwordx4 v12, s[40:43], s8 offen nt lds\n"
"v_cvt_pk_bf16_f32 v150, v36, v37\n"
"v_mfma_f32_16x16x32_bf16 a[92:95], a[212:215], v[188:191], a[92:95]\n"
"v_exp_f32 v39, v119\n"
"v_mfma_f32_16x16x32_bf16 a[88:91], a[212:215], v[184:187], a[88:91]\n"
"v_exp_f32 v32, v120\n"
"v_mfma_f32_16x16x32_bf16 a[84:87], a[212:215], v[180:183], a[84:87]\n"
"v_add_f32 v25, v25, v38\n"
"v_add_f32 v25, v25, v39\n"
"v_mfma_f32_16x16x32_bf16 a[80:83], a[212:215], v[176:179], a[80:83]\n"
"v_exp_f32 v33, v121\n"
"v_mfma_f32_16x16x32_bf16 a[96:99], a[216:219], v[176:179], a[96:99]\n"
"ds_read_b128 a[192:195], v2 offset:0\n"
"buffer_load_dwordx4 v13, s[40:43], s8 offen offset:1024 nt lds\n"
"v_mfma_f32_16x16x32_bf16 a[100:103], a[216:219], v[180:183], a[100:103]\n"
"v_cvt_pk_bf16_f32 v151, v38, v39\n"
"v_add_f32 v26, v26, v32\n"
"v_mfma_f32_16x16x32_bf16 a[104:107], a[216:219], v[184:187], a[104:107]\n"
"v_exp_f32 v34, v122\n"
"v_mfma_f32_16x16x32_bf16 a[108:111], a[216:219], v[188:191], a[108:111]\n"
"v_add_f32 v26, v26, v33\n"
"v_cvt_pk_bf16_f32 v154, v32, v33\n"
"v_mfma_f32_16x16x32_bf16 a[124:127], a[220:223], v[188:191], a[124:127]\n"
"ds_read_b128 a[196:199], v3 offset:0\n"
"buffer_load_dwordx4 v14, s[40:43], s8 offen offset:2048 nt lds\n"
"v_mfma_f32_16x16x32_bf16 a[120:123], a[220:223], v[184:187], a[120:123]\n"
"v_exp_f32 v35, v123\n"
"v_mfma_f32_16x16x32_bf16 a[116:119], a[220:223], v[180:183], a[116:119]\n"
"v_add_f32 v26, v26, v34\n"
"v_add_f32 v26, v26, v35\n"
"v_mfma_f32_16x16x32_bf16 a[112:115], a[220:223], v[176:179], a[112:115]\n"
"v_exp_f32 v36, v124\n"
"v_mfma_f32_16x16x32_bf16 a[128:131], a[224:227], v[176:179], a[128:131]\n"
"ds_read_b128 a[200:203], v2 offset:128\n"
"s_add_u32 m0, s76, 122880\n"
"v_mfma_f32_16x16x32_bf16 a[132:135], a[224:227], v[180:183], a[132:135]\n"
"v_exp_f32 v37, v125\n"
"v_mfma_f32_16x16x32_bf16 a[136:139], a[224:227], v[184:187], a[136:139]\n"
"v_cvt_pk_bf16_f32 v155, v34, v35\n"
"v_add_f32 v27, v27, v36\n"
"v_mfma_f32_16x16x32_bf16 a[140:143], a[224:227], v[188:191], a[140:143]\n"
"v_add_f32 v27, v27, v37\n"
"v_cvt_pk_bf16_f32 v158, v36, v37\n"
"v_mfma_f32_16x16x32_bf16 a[156:159], a[228:231], v[188:191], a[156:159]\n"
"ds_read_b128 a[204:207], v3 offset:128\n"
"buffer_load_dwordx4 v15, s[44:47], s14 offen nt lds\n"
"v_mfma_f32_16x16x32_bf16 a[152:155], a[228:231], v[184:187], a[152:155]\n"
"v_exp_f32 v38, v126\n"
"v_mfma_f32_16x16x32_bf16 a[148:151], a[228:231], v[180:183], a[148:151]\n"
"v_add_f32 v27, v27, v38\n"
"s_max_u32 s6, s6, s4\n"
"v_mfma_f32_16x16x32_bf16 a[144:147], a[228:231], v[176:179], a[144:147]\n"
"v_exp_f32 v39, v127\n"
"v_mfma_f32_16x16x32_bf16 a[160:163], a[232:235], v[176:179], a[160:163]\n"
"ds_read_b128 a[208:211], v2 offset:256\n"
"buffer_load_dwordx4 v16, s[44:47], s14 offen offset:1024 nt lds\n"
"v_mfma_f32_16x16x32_bf16 a[164:167], a[232:235], v[180:183], a[164:167]\n"
"ds_read_b128 a[212:215], v3 offset:256\n"
"v_add_f32 v27, v27, v39\n"
"v_mfma_f32_16x16x32_bf16 a[168:171], a[232:235], v[184:187], a[168:171]\n"
"v_cvt_pk_bf16_f32 v159, v38, v39\n"
"s_add_u32 s6, s6, s7\n"
"v_mfma_f32_16x16x32_bf16 a[172:175], a[232:235], v[188:191], a[172:175]\n"
"v_mfma_f32_16x16x32_bf16 a[188:191], a[236:239], v[188:191], a[188:191]\n"
"v_max3_f32 v28, v24, v25, v26\n"
"v_mfma_f32_16x16x32_bf16 a[184:187], a[236:239], v[184:187], a[184:187]\n"
"v_max_f32 v28, v28, v27\n"
"buffer_load_dwordx4 v17, s[44:47], s14 offen offset:2048 nt lds\n"
"v_mfma_f32_16x16x32_bf16 a[180:183], a[236:239], v[180:183], a[180:183]\n"
"v_cmp_lt_f32 vcc, s27, v28\n"
"v_mfma_f32_16x16x32_bf16 a[176:179], a[236:239], v[176:179], a[176:179]\n"
"s_waitcnt lgkmcnt(0)\n"
"s_cbranch_vccnz .Lmha_rare_l1\n"
".Lmha_ret_l1:\n"
"v_mfma_f32_16x16x32_bf16 v[80:83], a[192:195], v[64:67], v[40:43]\n"
"ds_read_b128 a[216:219], v2 offset:6144\n"
"s_add_u32 s9, s9, 73728\n"
"v_mfma_f32_16x16x32_bf16 v[80:83], a[196:199], v[96:99], v[80:83]\n"
"ds_read_b128 a[220:223], v3 offset:6144\n"
"s_add_u32 s15, s15, 73728\n"
"v_mfma_f32_16x16x32_bf16 v[80:83], a[200:203], v[128:131], v[80:83]\n"
"ds_read_b128 a[224:227], v2 offset:6272\n"
"v_mfma_f32_16x16x32_bf16 v[80:83], a[204:207], v[160:163], v[80:83]\n"
"ds_read_b128 a[228:231], v3 offset:6272\n"
"v_mfma_f32_16x16x32_bf16 v[80:83], a[208:211], v[192:195], v[80:83]\n"
"ds_read_b128 a[232:235], v2 offset:6400\n"
"v_mfma_f32_16x16x32_bf16 v[80:83], a[212:215], v[224:227], v[80:83]\n"
"ds_read_b128 a[236:239], v3 offset:6400\n"
"s_memrealtime s[4:5]\n"
"v_mfma_f32_16x16x32_bf16 v[84:87], a[212:215], v[228:231], v[44:47]\n"
"v_add_f32 v20, v20, v24\n"
"v_mfma_f32_16x16x32_bf16 v[84:87], a[208:211], v[196:199], v[84:87]\n"
"v_add_f32 v21, v21, v25\n"
"v_mfma_f32_16x16x32_bf16 v[84:87], a[204:207], v[164:167], v[84:87]\n"
"v_add_f32 v22, v22, v26\n"
"v_mfma_f32_16x16x32_bf16 v[84:87], a[200:203], v[132:135], v[84:87]\n"
"v_add_f32 v23, v23, v27\n"
"v_mfma_f32_16x16x32_bf16 v[84:87], a[196:199], v[100:103], v[84:87]\n"
"v_exp_f32 v32, v80\n"
"v_mfma_f32_16x16x32_bf16 v[84:87], a[192:195], v[68:71], v[84:87]\n"
"v_exp_f32 v33, v81\n"
"v_mfma_f32_16x16x32_bf16 v[88:91], a[192:195], v[72:75], v[48:51]\n"
"v_add_f32 v24, v32, v33\n"
"v_cvt_pk_bf16_f32 v176, v32, v33\n"
"v_mfma_f32_16x16x32_bf16 v[88:91], a[196:199], v[104:107], v[88:91]\n"
"v_mfma_f32_16x16x32_bf16 v[88:91], a[200:203], v[136:139], v[88:91]\n"
"v_exp_f32 v34, v82\n"
"v_mfma_f32_16x16x32_bf16 v[88:91], a[204:207], v[168:171], v[88:91]\n"
"v_add_f32 v24, v24, v34\n"
"v_mfma_f32_16x16x32_bf16 v[88:91], a[208:211], v[200:203], v[88:91]\n"
"v_exp_f32 v35, v83\n"
"v_mfma_f32_16x16x32_bf16 v[88:91], a[212:215], v[232:235], v[88:91]\n"
"v_add_f32 v24, v24, v35\n"
"v_cvt_pk_bf16_f32 v177, v34, v35\n"
"v_mfma_f32_16x16x32_bf16 v[92:95], a[212:215], v[236:239], v[52:55]\n"
"v_mfma_f32_16x16x32_bf16 v[92:95], a[208:211], v[204:207], v[92:95]\n"
"v_exp_f32 v36, v84\n"
"v_mfma_f32_16x16x32_bf16 v[92:95], a[204:207], v[172:175], v[92:95]\n"
"v_mfma_f32_16x16x32_bf16 v[92:95], a[200:203], v[140:143], v[92:95]\n"
"v_exp_f32 v37, v85\n"
"v_mfma_f32_16x16x32_bf16 v[92:95], a[196:199], v[108:111], v[92:95]\n"
"v_add_f32 v25, v36, v37\n"
"v_cvt_pk_bf16_f32 v180, v36, v37\n"
"v_mfma_f32_16x16x32_bf16 v[92:95], a[192:195], v[76:79], v[92:95]\n"
"s_waitcnt lgkmcnt(0)\n"
"v_mfma_f32_16x16x32_bf16 v[112:115], a[216:219], v[64:67], v[40:43]\n"
"ds_read_b64_tr_b16 a[192:193], v4 offset:36864\n"
"ds_read_b64_tr_b16 a[194:195], v4 offset:43008\n"
"v_mfma_f32_16x16x32_bf16 v[112:115], a[220:223], v[96:99], v[112:115]\n"
"v_exp_f32 v38, v86\n"
"v_mfma_f32_16x16x32_bf16 v[112:115], a[224:227], v[128:131], v[112:115]\n"
"v_exp_f32 v39, v87\n"
"v_mfma_f32_16x16x32_bf16 v[112:115], a[228:231], v[160:163], v[112:115]\n"
"ds_read_b64_tr_b16 a[196:197], v5 offset:36864\n"
"ds_read_b64_tr_b16 a[198:199], v5 offset:43008\n"
"v_mfma_f32_16x16x32_bf16 v[112:115], a[232:235], v[192:195], v[112:115]\n"
"v_add_f32 v25, v25, v38\n"
"v_add_f32 v25, v25, v39\n"
"v_mfma_f32_16x16x32_bf16 v[112:115], a[236:239], v[224:227], v[112:115]\n"
"v_exp_f32 v32, v88\n"
"v_mfma_f32_16x16x32_bf16 v[116:119], a[236:239], v[228:231], v[44:47]\n"
"ds_read_b64_tr_b16 a[200:201], v6 offset:36864\n"
"ds_read_b64_tr_b16 a[202:203], v6 offset:43008\n"
"v_mfma_f32_16x16x32_bf16 v[116:119], a[232:235], v[196:199], v[116:119]\n"
"v_exp_f32 v33, v89\n"
"v_mfma_f32_16x16x32_bf16 v[116:119], a[228:231], v[164:167], v[116:119]\n"
"v_cvt_pk_bf16_f32 v181, v38, v39\n"
"v_add_f32 v26, v32, v33\n"
"v_mfma_f32_16x16x32_bf16 v[116:119], a[224:227], v[132:135], v[116:119]\n"
"ds_read_b64_tr_b16 a[204:205], v7 offset:36864\n"
"ds_read_b64_tr_b16 a[206:207], v7 offset:43008\n"
"v_mfma_f32_16x16x32_bf16 v[116:119], a[220:223], v[100:103], v[116:119]\n"
"v_exp_f32 v34, v90\n"
"v_mfma_f32_16x16x32_bf16 v[116:119], a[216:219], v[68:71], v[116:119]\n"
"v_cvt_pk_bf16_f32 v184, v32, v33\n"
"v_add_f32 v26, v26, v34\n"
"v_mfma_f32_16x16x32_bf16 v[120:123], a[216:219], v[72:75], v[48:51]\n"
"ds_read_b64_tr_b16 a[208:209], v4 offset:36992\n"
"ds_read_b64_tr_b16 a[210:211], v4 offset:43136\n"
"v_mfma_f32_16x16x32_bf16 v[120:123], a[220:223], v[104:107], v[120:123]\n"
"v_exp_f32 v35, v91\n"
"v_mfma_f32_16x16x32_bf16 v[120:123], a[224:227], v[136:139], v[120:123]\n"
"v_add_f32 v26, v26, v35\n"
"v_cvt_pk_bf16_f32 v185, v34, v35\n"
"v_mfma_f32_16x16x32_bf16 v[120:123], a[228:231], v[168:171], v[120:123]\n"
"ds_read_b64_tr_b16 a[212:213], v5 offset:36992\n"
"ds_read_b64_tr_b16 a[214:215], v5 offset:43136\n"
"v_mfma_f32_16x16x32_bf16 v[120:123], a[232:235], v[200:203], v[120:123]\n"
"v_exp_f32 v36, v92\n"
"v_mfma_f32_16x16x32_bf16 v[120:123], a[236:239], v[232:235], v[120:123]\n"
"v_exp_f32 v37, v93\n"
"v_mfma_f32_16x16x32_bf16 v[124:127], a[236:239], v[236:239], v[52:55]\n"
"v_add_f32 v27, v36, v37\n"
"v_cvt_pk_bf16_f32 v188, v36, v37\n"
"v_mfma_f32_16x16x32_bf16 v[124:127], a[232:235], v[204:207], v[124:127]\n"
"s_cmp_lt_u32 s4, s6\n"
"s_cbranch_scc1 .Lmha_slp2\n"
".Lmha_wk2:\n"
"v_mfma_f32_16x16x32_bf16 v[124:127], a[228:231], v[172:175], v[124:127]\n"
"v_exp_f32 v38, v94\n"
"v_mfma_f32_16x16x32_bf16 v[124:127], a[224:227], v[140:143], v[124:127]\n"
"v_add_f32 v27, v27, v38\n"
"s_sub_u32 s4, s4, 20\n"
"v_mfma_f32_16x16x32_bf16 v[124:127], a[220:223], v[108:111], v[124:127]\n"
"v_exp_f32 v39, v95\n"
"v_mfma_f32_16x16x32_bf16 v[124:127], a[216:219], v[76:79], v[124:127]\n"
"s_waitcnt lgkmcnt(0)\n"
"v_add_f32 v27, v27, v39\n"
"v_mfma_f32_16x16x32_bf16 a[0:3], a[192:195], v[144:147], a[0:3]\n"
"ds_read_b64_tr_b16 a[216:217], v6 offset:36992\n"
"ds_read_b64_tr_b16 a[218:219], v6 offset:43136\n"
"v_mfma_f32_16x16x32_bf16 a[4:7], a[192:195], v[148:151], a[4:7]\n"
"v_exp_f32 v32, v112\n"
"v_mfma_f32_16x16x32_bf16 a[8:11], a[192:195], v[152:155], a[8:11]\n"
"ds_read_b64_tr_b16 a[220:221], v7 offset:36992\n"
"ds_read_b64_tr_b16 a[222:223], v7 offset:43136\n"
"v_mfma_f32_16x16x32_bf16 a[12:15], a[192:195], v[156:159], a[12:15]\n"
"v_exp_f32 v33, v113\n"
"v_mfma_f32_16x16x32_bf16 a[28:31], a[196:199], v[156:159], a[28:31]\n"
"ds_read_b64_tr_b16 a[224:225], v4 offset:37120\n"
"ds_read_b64_tr_b16 a[226:227], v4 offset:43264\n"
"v_mfma_f32_16x16x32_bf16 a[24:27], a[196:199], v[152:155], a[24:27]\n"
"v_cvt_pk_bf16_f32 v189, v38, v39\n"
"v_add_f32 v24, v24, v32\n"
"v_mfma_f32_16x16x32_bf16 a[20:23], a[196:199], v[148:151], a[20:23]\n"
"ds_read_b64_tr_b16 a[228:229], v5 offset:37120\n"
"ds_read_b64_tr_b16 a[230:231], v5 offset:43264\n"
"v_mfma_f32_16x16x32_bf16 a[16:19], a[196:199], v[144:147], a[16:19]\n"
"v_exp_f32 v34, v114\n"
"v_mfma_f32_16x16x32_bf16 a[32:35], a[200:203], v[144:147], a[32:35]\n"
"ds_read_b64_tr_b16 a[232:233], v6 offset:37120\n"
"ds_read_b64_tr_b16 a[234:235], v6 offset:43264\n"
"v_mfma_f32_16x16x32_bf16 a[36:39], a[200:203], v[148:151], a[36:39]\n"
"v_exp_f32 v35, v115\n"
"v_mfma_f32_16x16x32_bf16 a[40:43], a[200:203], v[152:155], a[40:43]\n"
"ds_read_b64_tr_b16 a[236:237], v7 offset:37120\n"
"ds_read_b64_tr_b16 a[238:239], v7 offset:43264\n"
"v_mfma_f32_16x16x32_bf16 a[44:47], a[200:203], v[156:159], a[44:47]\n"
"v_exp_f32 v36, v116\n"
"v_mfma_f32_16x16x32_bf16 a[60:63], a[204:207], v[156:159], a[60:63]\n"
"v_add_f32 v24, v24, v33\n"
"v_cvt_pk_bf16_f32 v178, v32, v33\n"
"v_mfma_f32_16x16x32_bf16 a[56:59], a[204:207], v[152:155], a[56:59]\n"
"v_exp_f32 v37, v117\n"
"v_mfma_f32_16x16x32_bf16 a[52:55], a[204:207], v[148:151], a[52:55]\n"
"v_add_f32 v24, v24, v34\n"
"v_add_f32 v24, v24, v35\n"
"v_mfma_f32_16x16x32_bf16 a[48:51], a[204:207], v[144:147], a[48:51]\n"
"v_cvt_pk_bf16_f32 v179, v34, v35\n"
"v_add_f32 v25, v25, v36\n"
"v_mfma_f32_16x16x32_bf16 a[64:67], a[208:211], v[144:147], a[64:67]\n"
"v_exp_f32 v38, v118\n"
"v_mfma_f32_16x16x32_bf16 a[68:71], a[208:211], v[148:151], a[68:71]\n"
"s_waitcnt vmcnt(6) lgkmcnt(0)\n"
"s_barrier\n"
"v_mfma_f32_16x16x32_bf16 a[72:75], a[208:211], v[152:155], a[72:75]\n"
"s_add_u32 m0, s76, 0\n"
"v_add_f32 v25, v25, v37\n"
"v_mfma_f32_16x16x32_bf16 a[76:79], a[208:211], v[156:159], a[76:79]\n"
"buffer_load_dwordx4 v12, s[40:43], s9 offen nt lds\n"
"v_cvt_pk_bf16_f32 v182, v36, v37\n"
"v_mfma_f32_16x16x32_bf16 a[92:95], a[212:215], v[156:159], a[92:95]\n"
"v_exp_f32 v39, v119\n"
"v_mfma_f32_16x16x32_bf16 a[88:91], a[212:215], v[152:155], a[88:91]\n"
"v_exp_f32 v32, v120\n"
"v_mfma_f32_16x16x32_bf16 a[84:87], a[212:215], v[148:151], a[84:87]\n"
"v_add_f32 v25, v25, v38\n"
"v_add_f32 v25, v25, v39\n"
"v_mfma_f32_16x16x32_bf16 a[80:83], a[212:215], v[144:147], a[80:83]\n"
"v_exp_f32 v33, v121\n"
"v_mfma_f32_16x16x32_bf16 a[96:99], a[216:219], v[144:147], a[96:99]\n"
"ds_read_b128 a[192:195], v2 offset:12288\n"
"buffer_load_dwordx4 v13, s[40:43], s9 offen offset:1024 nt lds\n"
"v_mfma_f32_16x16x32_bf16 a[100:103], a[216:219], v[148:151], a[100:103]\n"
"v_cvt_pk_bf16_f32 v183, v38, v39\n"
"v_add_f32 v26, v26, v32\n"
"v_mfma_f32_16x16x32_bf16 a[104:107], a[216:219], v[152:155], a[104:107]\n"
"v_exp_f32 v34, v122\n"
"v_mfma_f32_16x16x32_bf16 a[108:111], a[216:219], v[156:159], a[108:111]\n"
"v_add_f32 v26, v26, v33\n"
"v_cvt_pk_bf16_f32 v186, v32, v33\n"
"v_mfma_f32_16x16x32_bf16 a[124:127], a[220:223], v[156:159], a[124:127]\n"
"ds_read_b128 a[196:199], v3 offset:12288\n"
"buffer_load_dwordx4 v14, s[40:43], s9 offen offset:2048 nt lds\n"
"v_mfma_f32_16x16x32_bf16 a[120:123], a[220:223], v[152:155], a[120:123]\n"
"v_exp_f32 v35, v123\n"
"v_mfma_f32_16x16x32_bf16 a[116:119], a[220:223], v[148:151], a[116:119]\n"
"v_add_f32 v26, v26, v34\n"
"v_add_f32 v26, v26, v35\n"
"v_mfma_f32_16x16x32_bf16 a[112:115], a[220:223], v[144:147], a[112:115]\n"
"v_exp_f32 v36, v124\n"
"v_mfma_f32_16x16x32_bf16 a[128:131], a[224:227], v[144:147], a[128:131]\n"
"ds_read_b128 a[200:203], v2 offset:12416\n"
"s_add_u32 m0, s76, 135168\n"
"v_mfma_f32_16x16x32_bf16 a[132:135], a[224:227], v[148:151], a[132:135]\n"
"v_exp_f32 v37, v125\n"
"v_mfma_f32_16x16x32_bf16 a[136:139], a[224:227], v[152:155], a[136:139]\n"
"v_cvt_pk_bf16_f32 v187, v34, v35\n"
"v_add_f32 v27, v27, v36\n"
"v_mfma_f32_16x16x32_bf16 a[140:143], a[224:227], v[156:159], a[140:143]\n"
"v_add_f32 v27, v27, v37\n"
"v_cvt_pk_bf16_f32 v190, v36, v37\n"
"v_mfma_f32_16x16x32_bf16 a[156:159], a[228:231], v[156:159], a[156:159]\n"
"ds_read_b128 a[204:207], v3 offset:12416\n"
"buffer_load_dwordx4 v15, s[44:47], s15 offen nt lds\n"
"v_mfma_f32_16x16x32_bf16 a[152:155], a[228:231], v[152:155], a[152:155]\n"
"v_exp_f32 v38, v126\n"
"v_mfma_f32_16x16x32_bf16 a[148:151], a[228:231], v[148:151], a[148:151]\n"
"v_add_f32 v27, v27, v38\n"
"s_max_u32 s6, s6, s4\n"
"v_mfma_f32_16x16x32_bf16 a[144:147], a[228:231], v[144:147], a[144:147]\n"
"v_exp_f32 v39, v127\n"
"v_mfma_f32_16x16x32_bf16 a[160:163], a[232:235], v[144:147], a[160:163]\n"
"ds_read_b128 a[208:211], v2 offset:12544\n"
"buffer_load_dwordx4 v16, s[44:47], s15 offen offset:1024 nt lds\n"
"v_mfma_f32_16x16x32_bf16 a[164:167], a[232:235], v[148:151], a[164:167]\n"
"ds_read_b128 a[212:215], v3 offset:12544\n"
"v_add_f32 v27, v27, v39\n"
"v_mfma_f32_16x16x32_bf16 a[168:171], a[232:235], v[152:155], a[168:171]\n"
"v_cvt_pk_bf16_f32 v191, v38, v39\n"
"s_add_u32 s6, s6, s7\n"
"v_mfma_f32_16x16x32_bf16 a[172:175], a[232:235], v[156:159], a[172:175]\n"
"v_mfma_f32_16x16x32_bf16 a[188:191], a[236:239], v[156:159], a[188:191]\n"
"v_max3_f32 v28, v24, v25, v26\n"
"v_mfma_f32_16x16x32_bf16 a[184:187], a[236:239], v[152:155], a[184:187]\n"
"v_max_f32 v28, v28, v27\n"
"buffer_load_dwordx4 v17, s[44:47], s15 offen offset:2048 nt lds\n"
"v_mfma_f32_16x16x32_bf16 a[180:183], a[236:239], v[148:151], a[180:183]\n"
"v_cmp_lt_f32 vcc, s27, v28\n"
"v_mfma_f32_16x16x32_bf16 a[176:179], a[236:239], v[144:147], a[176:179]\n"
"s_waitcnt lgkmcnt(0)\n"
"s_cbranch_vccnz .Lmha_rare_l2\n"
".Lmha_ret_l2:\n"
"v_mfma_f32_16x16x32_bf16 v[80:83], a[192:195], v[64:67], v[40:43]\n"
"ds_read_b128 a[216:219], v2 offset:18432\n"
"s_add_u32 s10, s10, 73728\n"
"v_mfma_f32_16x16x32_bf16 v[80:83], a[196:199], v[96:99], v[80:83]\n"
"ds_read_b128 a[220:223], v3 offset:18432\n"
"s_add_u32 s16, s16, 73728\n"
"v_mfma_f32_16x16x32_bf16 v[80:83], a[200:203], v[128:131], v[80:83]\n"
"ds_read_b128 a[224:227], v2 offset:18560\n"
"v_mfma_f32_16x16x32_bf16 v[80:83], a[204:207], v[160:163], v[80:83]\n"
"ds_read_b128 a[228:231], v3 offset:18560\n"
"v_mfma_f32_16x16x32_bf16 v[80:83], a[208:211], v[192:195], v[80:83]\n"
"ds_read_b128 a[232:235], v2 offset:18688\n"
"v_mfma_f32_16x16x32_bf16 v[80:83], a[212:215], v[224:227], v[80:83]\n"
"ds_read_b128 a[236:239], v3 offset:18688\n"
"s_memrealtime s[4:5]\n"
"v_mfma_f32_16x16x32_bf16 v[84:87], a[212:215], v[228:231], v[44:47]\n"
"v_add_f32 v20, v20, v24\n"
"v_mfma_f32_16x16x32_bf16 v[84:87], a[208:211], v[196:199], v[84:87]\n"
"v_add_f32 v21, v21, v25\n"
"v_mfma_f32_16x16x32_bf16 v[84:87], a[204:207], v[164:167], v[84:87]\n"
"v_add_f32 v22, v22, v26\n"
"v_mfma_f32_16x16x32_bf16 v[84:87], a[200:203], v[132:135], v[84:87]\n"
"v_add_f32 v23, v23, v27\n"
"v_mfma_f32_16x16x32_bf16 v[84:87], a[196:199], v[100:103], v[84:87]\n"
"v_exp_f32 v32, v80\n"
"v_mfma_f32_16x16x32_bf16 v[84:87], a[192:195], v[68:71], v[84:87]\n"
"v_exp_f32 v33, v81\n"
"v_mfma_f32_16x16x32_bf16 v[88:91], a[192:195], v[72:75], v[48:51]\n"
"v_add_f32 v24, v32, v33\n"
"v_cvt_pk_bf16_f32 v144, v32, v33\n"
"v_mfma_f32_16x16x32_bf16 v[88:91], a[196:199], v[104:107], v[88:91]\n"
"v_mfma_f32_16x16x32_bf16 v[88:91], a[200:203], v[136:139], v[88:91]\n"
"v_exp_f32 v34, v82\n"
"v_mfma_f32_16x16x32_bf16 v[88:91], a[204:207], v[168:171], v[88:91]\n"
"v_add_f32 v24, v24, v34\n"
"v_mfma_f32_16x16x32_bf16 v[88:91], a[208:211], v[200:203], v[88:91]\n"
"v_exp_f32 v35, v83\n"
"v_mfma_f32_16x16x32_bf16 v[88:91], a[212:215], v[232:235], v[88:91]\n"
"v_add_f32 v24, v24, v35\n"
"v_cvt_pk_bf16_f32 v145, v34, v35\n"
"v_mfma_f32_16x16x32_bf16 v[92:95], a[212:215], v[236:239], v[52:55]\n"
"v_mfma_f32_16x16x32_bf16 v[92:95], a[208:211], v[204:207], v[92:95]\n"
"v_exp_f32 v36, v84\n"
"v_mfma_f32_16x16x32_bf16 v[92:95], a[204:207], v[172:175], v[92:95]\n"
"v_mfma_f32_16x16x32_bf16 v[92:95], a[200:203], v[140:143], v[92:95]\n"
"v_exp_f32 v37, v85\n"
"v_mfma_f32_16x16x32_bf16 v[92:95], a[196:199], v[108:111], v[92:95]\n"
"v_add_f32 v25, v36, v37\n"
"v_cvt_pk_bf16_f32 v148, v36, v37\n"
"v_mfma_f32_16x16x32_bf16 v[92:95], a[192:195], v[76:79], v[92:95]\n"
"s_waitcnt lgkmcnt(0)\n"
"v_mfma_f32_16x16x32_bf16 v[112:115], a[216:219], v[64:67], v[40:43]\n"
"ds_read_b64_tr_b16 a[192:193], v8 offset:0\n"
"ds_read_b64_tr_b16 a[194:195], v8 offset:6144\n"
"v_mfma_f32_16x16x32_bf16 v[112:115], a[220:223], v[96:99], v[112:115]\n"
"v_exp_f32 v38, v86\n"
"v_mfma_f32_16x16x32_bf16 v[112:115], a[224:227], v[128:131], v[112:115]\n"
"v_exp_f32 v39, v87\n"
"v_mfma_f32_16x16x32_bf16 v[112:115], a[228:231], v[160:163], v[112:115]\n"
"ds_read_b64_tr_b16 a[196:197], v9 offset:0\n"
"ds_read_b64_tr_b16 a[198:199], v9 offset:6144\n"
"v_mfma_f32_16x16x32_bf16 v[112:115], a[232:235], v[192:195], v[112:115]\n"
"v_add_f32 v25, v25, v38\n"
"v_add_f32 v25, v25, v39\n"
"v_mfma_f32_16x16x32_bf16 v[112:115], a[236:239], v[224:227], v[112:115]\n"
"v_exp_f32 v32, v88\n"
"v_mfma_f32_16x16x32_bf16 v[116:119], a[236:239], v[228:231], v[44:47]\n"
"ds_read_b64_tr_b16 a[200:201], v10 offset:0\n"
"ds_read_b64_tr_b16 a[202:203], v10 offset:6144\n"
"v_mfma_f32_16x16x32_bf16 v[116:119], a[232:235], v[196:199], v[116:119]\n"
"v_exp_f32 v33, v89\n"
"v_mfma_f32_16x16x32_bf16 v[116:119], a[228:231], v[164:167], v[116:119]\n"
"v_cvt_pk_bf16_f32 v149, v38, v39\n"
"v_add_f32 v26, v32, v33\n"
"v_mfma_f32_16x16x32_bf16 v[116:119], a[224:227], v[132:135], v[116:119]\n"
"ds_read_b64_tr_b16 a[204:205], v11 offset:0\n"
"ds_read_b64_tr_b16 a[206:207], v11 offset:6144\n"
"v_mfma_f32_16x16x32_bf16 v[116:119], a[220:223], v[100:103], v[116:119]\n"
"v_exp_f32 v34, v90\n"
"v_mfma_f32_16x16x32_bf16 v[116:119], a[216:219], v[68:71], v[116:119]\n"
"v_cvt_pk_bf16_f32 v152, v32, v33\n"
"v_add_f32 v26, v26, v34\n"
"v_mfma_f32_16x16x32_bf16 v[120:123], a[216:219], v[72:75], v[48:51]\n"
"ds_read_b64_tr_b16 a[208:209], v8 offset:128\n"
"ds_read_b64_tr_b16 a[210:211], v8 offset:6272\n"
"v_mfma_f32_16x16x32_bf16 v[120:123], a[220:223], v[104:107], v[120:123]\n"
"v_exp_f32 v35, v91\n"
"v_mfma_f32_16x16x32_bf16 v[120:123], a[224:227], v[136:139], v[120:123]\n"
"v_add_f32 v26, v26, v35\n"
"v_cvt_pk_bf16_f32 v153, v34, v35\n"
"v_mfma_f32_16x16x32_bf16 v[120:123], a[228:231], v[168:171], v[120:123]\n"
"ds_read_b64_tr_b16 a[212:213], v9 offset:128\n"
"ds_read_b64_tr_b16 a[214:215], v9 offset:6272\n"
"v_mfma_f32_16x16x32_bf16 v[120:123], a[232:235], v[200:203], v[120:123]\n"
"v_exp_f32 v36, v92\n"
"v_mfma_f32_16x16x32_bf16 v[120:123], a[236:239], v[232:235], v[120:123]\n"
"v_exp_f32 v37, v93\n"
"v_mfma_f32_16x16x32_bf16 v[124:127], a[236:239], v[236:239], v[52:55]\n"
"v_add_f32 v27, v36, v37\n"
"v_cvt_pk_bf16_f32 v156, v36, v37\n"
"v_mfma_f32_16x16x32_bf16 v[124:127], a[232:235], v[204:207], v[124:127]\n"
"s_cmp_lt_u32 s4, s6\n"
"s_cbranch_scc1 .Lmha_slp3\n"
".Lmha_wk3:\n"
"v_mfma_f32_16x16x32_bf16 v[124:127], a[228:231], v[172:175], v[124:127]\n"
"v_exp_f32 v38, v94\n"
"v_mfma_f32_16x16x32_bf16 v[124:127], a[224:227], v[140:143], v[124:127]\n"
"v_add_f32 v27, v27, v38\n"
"s_sub_u32 s4, s4, 20\n"
"v_mfma_f32_16x16x32_bf16 v[124:127], a[220:223], v[108:111], v[124:127]\n"
"v_exp_f32 v39, v95\n"
"v_mfma_f32_16x16x32_bf16 v[124:127], a[216:219], v[76:79], v[124:127]\n"
"s_waitcnt lgkmcnt(0)\n"
"v_add_f32 v27, v27, v39\n"
"v_mfma_f32_16x16x32_bf16 a[0:3], a[192:195], v[176:179], a[0:3]\n"
"ds_read_b64_tr_b16 a[216:217], v10 offset:128\n"
"ds_read_b64_tr_b16 a[218:219], v10 offset:6272\n"
"v_mfma_f32_16x16x32_bf16 a[4:7], a[192:195], v[180:183], a[4:7]\n"
"v_exp_f32 v32, v112\n"
"v_mfma_f32_16x16x32_bf16 a[8:11], a[192:195], v[184:187], a[8:11]\n"
"ds_read_b64_tr_b16 a[220:221], v11 offset:128\n"
"ds_read_b64_tr_b16 a[222:223], v11 offset:6272\n"
"v_mfma_f32_16x16x32_bf16 a[12:15], a[192:195], v[188:191], a[12:15]\n"
"v_exp_f32 v33, v113\n"
"v_mfma_f32_16x16x32_bf16 a[28:31], a[196:199], v[188:191], a[28:31]\n"
"ds_read_b64_tr_b16 a[224:225], v8 offset:256\n"
"ds_read_b64_tr_b16 a[226:227], v8 offset:6400\n"
"v_mfma_f32_16x16x32_bf16 a[24:27], a[196:199], v[184:187], a[24:27]\n"
"v_cvt_pk_bf16_f32 v157, v38, v39\n"
"v_add_f32 v24, v24, v32\n"
"v_mfma_f32_16x16x32_bf16 a[20:23], a[196:199], v[180:183], a[20:23]\n"
"ds_read_b64_tr_b16 a[228:229], v9 offset:256\n"
"ds_read_b64_tr_b16 a[230:231], v9 offset:6400\n"
"v_mfma_f32_16x16x32_bf16 a[16:19], a[196:199], v[176:179], a[16:19]\n"
"v_exp_f32 v34, v114\n"
"v_mfma_f32_16x16x32_bf16 a[32:35], a[200:203], v[176:179], a[32:35]\n"
"ds_read_b64_tr_b16 a[232:233], v10 offset:256\n"
"ds_read_b64_tr_b16 a[234:235], v10 offset:6400\n"
"v_mfma_f32_16x16x32_bf16 a[36:39], a[200:203], v[180:183], a[36:39]\n"
"v_exp_f32 v35, v115\n"
"v_mfma_f32_16x16x32_bf16 a[40:43], a[200:203], v[184:187], a[40:43]\n"
"ds_read_b64_tr_b16 a[236:237], v11 offset:256\n"
"ds_read_b64_tr_b16 a[238:239], v11 offset:6400\n"
"v_mfma_f32_16x16x32_bf16 a[44:47], a[200:203], v[188:191], a[44:47]\n"
"v_exp_f32 v36, v116\n"
"v_mfma_f32_16x16x32_bf16 a[60:63], a[204:207], v[188:191], a[60:63]\n"
"v_add_f32 v24, v24, v33\n"
"v_cvt_pk_bf16_f32 v146, v32, v33\n"
"v_mfma_f32_16x16x32_bf16 a[56:59], a[204:207], v[184:187], a[56:59]\n"
"v_exp_f32 v37, v117\n"
"v_mfma_f32_16x16x32_bf16 a[52:55], a[204:207], v[180:183], a[52:55]\n"
"v_add_f32 v24, v24, v34\n"
"v_add_f32 v24, v24, v35\n"
"v_mfma_f32_16x16x32_bf16 a[48:51], a[204:207], v[176:179], a[48:51]\n"
"v_cvt_pk_bf16_f32 v147, v34, v35\n"
"v_add_f32 v25, v25, v36\n"
"v_mfma_f32_16x16x32_bf16 a[64:67], a[208:211], v[176:179], a[64:67]\n"
"v_exp_f32 v38, v118\n"
"v_mfma_f32_16x16x32_bf16 a[68:71], a[208:211], v[180:183], a[68:71]\n"
"s_waitcnt vmcnt(6) lgkmcnt(0)\n"
"s_barrier\n"
"v_mfma_f32_16x16x32_bf16 a[72:75], a[208:211], v[184:187], a[72:75]\n"
"s_add_u32 m0, s76, 12288\n"
"v_add_f32 v25, v25, v37\n"
"v_mfma_f32_16x16x32_bf16 a[76:79], a[208:211], v[188:191], a[76:79]\n"
"buffer_load_dwordx4 v12, s[40:43], s10 offen nt lds\n"
"v_cvt_pk_bf16_f32 v150, v36, v37\n"
"v_mfma_f32_16x16x32_bf16 a[92:95], a[212:215], v[188:191], a[92:95]\n"
"v_exp_f32 v39, v119\n"
"v_mfma_f32_16x16x32_bf16 a[88:91], a[212:215], v[184:187], a[88:91]\n"
"v_exp_f32 v32, v120\n"
"v_mfma_f32_16x16x32_bf16 a[84:87], a[212:215], v[180:183], a[84:87]\n"
"v_add_f32 v25, v25, v38\n"
"v_add_f32 v25, v25, v39\n"
"v_mfma_f32_16x16x32_bf16 a[80:83], a[212:215], v[176:179], a[80:83]\n"
"v_exp_f32 v33, v121\n"
"v_mfma_f32_16x16x32_bf16 a[96:99], a[216:219], v[176:179], a[96:99]\n"
"ds_read_b128 a[192:195], v2 offset:24576\n"
"buffer_load_dwordx4 v13, s[40:43], s10 offen offset:1024 nt lds\n"
"v_mfma_f32_16x16x32_bf16 a[100:103], a[216:219], v[180:183], a[100:103]\n"
"v_cvt_pk_bf16_f32 v151, v38, v39\n"
"v_add_f32 v26, v26, v32\n"
"v_mfma_f32_16x16x32_bf16 a[104:107], a[216:219], v[184:187], a[104:107]\n"
"v_exp_f32 v34, v122\n"
"v_mfma_f32_16x16x32_bf16 a[108:111], a[216:219], v[188:191], a[108:111]\n"
"v_add_f32 v26, v26, v33\n"
"v_cvt_pk_bf16_f32 v154, v32, v33\n"
"v_mfma_f32_16x16x32_bf16 a[124:127], a[220:223], v[188:191], a[124:127]\n"
"ds_read_b128 a[196:199], v3 offset:24576\n"
"buffer_load_dwordx4 v14, s[40:43], s10 offen offset:2048 nt lds\n"
"v_mfma_f32_16x16x32_bf16 a[120:123], a[220:223], v[184:187], a[120:123]\n"
"v_exp_f32 v35, v123\n"
"v_mfma_f32_16x16x32_bf16 a[116:119], a[220:223], v[180:183], a[116:119]\n"
"v_add_f32 v26, v26, v34\n"
"v_add_f32 v26, v26, v35\n"
"v_mfma_f32_16x16x32_bf16 a[112:115], a[220:223], v[176:179], a[112:115]\n"
"v_exp_f32 v36, v124\n"
"v_mfma_f32_16x16x32_bf16 a[128:131], a[224:227], v[176:179], a[128:131]\n"
"ds_read_b128 a[200:203], v2 offset:24704\n"
"s_add_u32 m0, s76, 73728\n"
"v_mfma_f32_16x16x32_bf16 a[132:135], a[224:227], v[180:183], a[132:135]\n"
"v_exp_f32 v37, v125\n"
"v_mfma_f32_16x16x32_bf16 a[136:139], a[224:227], v[184:187], a[136:139]\n"
"v_cvt_pk_bf16_f32 v155, v34, v35\n"
"v_add_f32 v27, v27, v36\n"
"v_mfma_f32_16x16x32_bf16 a[140:143], a[224:227], v[188:191], a[140:143]\n"
"v_add_f32 v27, v27, v37\n"
"v_cvt_pk_bf16_f32 v158, v36, v37\n"
"v_mfma_f32_16x16x32_bf16 a[156:159], a[228:231], v[188:191], a[156:159]\n"
"ds_read_b128 a[204:207], v3 offset:24704\n"
"buffer_load_dwordx4 v15, s[44:47], s16 offen nt lds\n"
"v_mfma_f32_16x16x32_bf16 a[152:155], a[228:231], v[184:187], a[152:155]\n"
"v_exp_f32 v38, v126\n"
"v_mfma_f32_16x16x32_bf16 a[148:151], a[228:231], v[180:183], a[148:151]\n"
"v_add_f32 v27, v27, v38\n"
"s_max_u32 s6, s6, s4\n"
"v_mfma_f32_16x16x32_bf16 a[144:147], a[228:231], v[176:179], a[144:147]\n"
"v_exp_f32 v39, v127\n"
"v_mfma_f32_16x16x32_bf16 a[160:163], a[232:235], v[176:179], a[160:163]\n"
"ds_read_b128 a[208:211], v2 offset:24832\n"
"buffer_load_dwordx4 v16, s[44:47], s16 offen offset:1024 nt lds\n"
"v_mfma_f32_16x16x32_bf16 a[164:167], a[232:235], v[180:183], a[164:167]\n"
"ds_read_b128 a[212:215], v3 offset:24832\n"
"v_add_f32 v27, v27, v39\n"
"v_mfma_f32_16x16x32_bf16 a[168:171], a[232:235], v[184:187], a[168:171]\n"
"v_cvt_pk_bf16_f32 v159, v38, v39\n"
"s_add_u32 s6, s6, s7\n"
"v_mfma_f32_16x16x32_bf16 a[172:175], a[232:235], v[188:191], a[172:175]\n"
"v_mfma_f32_16x16x32_bf16 a[188:191], a[236:239], v[188:191], a[188:191]\n"
"v_max3_f32 v28, v24, v25, v26\n"
"v_mfma_f32_16x16x32_bf16 a[184:187], a[236:239], v[184:187], a[184:187]\n"
"v_max_f32 v28, v28, v27\n"
"buffer_load_dwordx4 v17, s[44:47], s16 offen offset:2048 nt lds\n"
"v_mfma_f32_16x16x32_bf16 a[180:183], a[236:239], v[180:183], a[180:183]\n"
"v_cmp_lt_f32 vcc, s27, v28\n"
"v_mfma_f32_16x16x32_bf16 a[176:179], a[236:239], v[176:179], a[176:179]\n"
"s_waitcnt lgkmcnt(0)\n"
"s_cbranch_vccnz .Lmha_rare_l3\n"
".Lmha_ret_l3:\n"
"v_mfma_f32_16x16x32_bf16 v[80:83], a[192:195], v[64:67], v[40:43]\n"
"ds_read_b128 a[216:219], v2 offset:30720\n"
"s_add_u32 s11, s11, 73728\n"
"v_mfma_f32_16x16x32_bf16 v[80:83], a[196:199], v[96:99], v[80:83]\n"
"ds_read_b128 a[220:223], v3 offset:30720\n"
"s_add_u32 s17, s17, 73728\n"
"v_mfma_f32_16x16x32_bf16 v[80:83], a[200:203], v[128:131], v[80:83]\n"
"ds_read_b128 a[224:227], v2 offset:30848\n"
"v_mfma_f32_16x16x32_bf16 v[80:83], a[204:207], v[160:163], v[80:83]\n"
"ds_read_b128 a[228:231], v3 offset:30848\n"
"v_mfma_f32_16x16x32_bf16 v[80:83], a[208:211], v[192:195], v[80:83]\n"
"ds_read_b128 a[232:235], v2 offset:30976\n"
"v_mfma_f32_16x16x32_bf16 v[80:83], a[212:215], v[224:227], v[80:83]\n"
"ds_read_b128 a[236:239], v3 offset:30976\n"
"s_memrealtime s[4:5]\n"
"v_mfma_f32_16x16x32_bf16 v[84:87], a[212:215], v[228:231], v[44:47]\n"
"v_add_f32 v20, v20, v24\n"
"v_mfma_f32_16x16x32_bf16 v[84:87], a[208:211], v[196:199], v[84:87]\n"
"v_add_f32 v21, v21, v25\n"
"v_mfma_f32_16x16x32_bf16 v[84:87], a[204:207], v[164:167], v[84:87]\n"
"v_add_f32 v22, v22, v26\n"
"v_mfma_f32_16x16x32_bf16 v[84:87], a[200:203], v[132:135], v[84:87]\n"
"v_add_f32 v23, v23, v27\n"
"v_mfma_f32_16x16x32_bf16 v[84:87], a[196:199], v[100:103], v[84:87]\n"
"v_exp_f32 v32, v80\n"
"v_mfma_f32_16x16x32_bf16 v[84:87], a[192:195], v[68:71], v[84:87]\n"
"v_exp_f32 v33, v81\n"
"v_mfma_f32_16x16x32_bf16 v[88:91], a[192:195], v[72:75], v[48:51]\n"
"v_add_f32 v24, v32, v33\n"
"v_cvt_pk_bf16_f32 v176, v32, v33\n"
"v_mfma_f32_16x16x32_bf16 v[88:91], a[196:199], v[104:107], v[88:91]\n"
"v_mfma_f32_16x16x32_bf16 v[88:91], a[200:203], v[136:139], v[88:91]\n"
"v_exp_f32 v34, v82\n"
"v_mfma_f32_16x16x32_bf16 v[88:91], a[204:207], v[168:171], v[88:91]\n"
"v_add_f32 v24, v24, v34\n"
"v_mfma_f32_16x16x32_bf16 v[88:91], a[208:211], v[200:203], v[88:91]\n"
"v_exp_f32 v35, v83\n"
"v_mfma_f32_16x16x32_bf16 v[88:91], a[212:215], v[232:235], v[88:91]\n"
"v_add_f32 v24, v24, v35\n"
"v_cvt_pk_bf16_f32 v177, v34, v35\n"
"v_mfma_f32_16x16x32_bf16 v[92:95], a[212:215], v[236:239], v[52:55]\n"
"v_mfma_f32_16x16x32_bf16 v[92:95], a[208:211], v[204:207], v[92:95]\n"
"v_exp_f32 v36, v84\n"
"v_mfma_f32_16x16x32_bf16 v[92:95], a[204:207], v[172:175], v[92:95]\n"
"v_mfma_f32_16x16x32_bf16 v[92:95], a[200:203], v[140:143], v[92:95]\n"
"v_exp_f32 v37, v85\n"
"v_mfma_f32_16x16x32_bf16 v[92:95], a[196:199], v[108:111], v[92:95]\n"
"v_add_f32 v25, v36, v37\n"
"v_cvt_pk_bf16_f32 v180, v36, v37\n"
"v_mfma_f32_16x16x32_bf16 v[92:95], a[192:195], v[76:79], v[92:95]\n"
"s_waitcnt lgkmcnt(0)\n"
"v_mfma_f32_16x16x32_bf16 v[112:115], a[216:219], v[64:67], v[40:43]\n"
"ds_read_b64_tr_b16 a[192:193], v8 offset:12288\n"
"ds_read_b64_tr_b16 a[194:195], v8 offset:18432\n"
"v_mfma_f32_16x16x32_bf16 v[112:115], a[220:223], v[96:99], v[112:115]\n"
"v_exp_f32 v38, v86\n"
"v_mfma_f32_16x16x32_bf16 v[112:115], a[224:227], v[128:131], v[112:115]\n"
"v_exp_f32 v39, v87\n"
"v_mfma_f32_16x16x32_bf16 v[112:115], a[228:231], v[160:163], v[112:115]\n"
"ds_read_b64_tr_b16 a[196:197], v9 offset:12288\n"
"ds_read_b64_tr_b16 a[198:199], v9 offset:18432\n"
"v_mfma_f32_16x16x32_bf16 v[112:115], a[232:235], v[192:195], v[112:115]\n"
"v_add_f32 v25, v25, v38\n"
"v_add_f32 v25, v25, v39\n"
"v_mfma_f32_16x16x32_bf16 v[112:115], a[236:239], v[224:227], v[112:115]\n"
"v_exp_f32 v32, v88\n"
"v_mfma_f32_16x16x32_bf16 v[116:119], a[236:239], v[228:231], v[44:47]\n"
"ds_read_b64_tr_b16 a[200:201], v10 offset:12288\n"
"ds_read_b64_tr_b16 a[202:203], v10 offset:18432\n"
"v_mfma_f32_16x16x32_bf16 v[116:119], a[232:235], v[196:199], v[116:119]\n"
"v_exp_f32 v33, v89\n"
"v_mfma_f32_16x16x32_bf16 v[116:119], a[228:231], v[164:167], v[116:119]\n"
"v_cvt_pk_bf16_f32 v181, v38, v39\n"
"v_add_f32 v26, v32, v33\n"
"v_mfma_f32_16x16x32_bf16 v[116:119], a[224:227], v[132:135], v[116:119]\n"
"ds_read_b64_tr_b16 a[204:205], v11 offset:12288\n"
"ds_read_b64_tr_b16 a[206:207], v11 offset:18432\n"
"v_mfma_f32_16x16x32_bf16 v[116:119], a[220:223], v[100:103], v[116:119]\n"
"v_exp_f32 v34, v90\n"
"v_mfma_f32_16x16x32_bf16 v[116:119], a[216:219], v[68:71], v[116:119]\n"
"v_cvt_pk_bf16_f32 v184, v32, v33\n"
"v_add_f32 v26, v26, v34\n"
"v_mfma_f32_16x16x32_bf16 v[120:123], a[216:219], v[72:75], v[48:51]\n"
"ds_read_b64_tr_b16 a[208:209], v8 offset:12416\n"
"ds_read_b64_tr_b16 a[210:211], v8 offset:18560\n"
"v_mfma_f32_16x16x32_bf16 v[120:123], a[220:223], v[104:107], v[120:123]\n"
"v_exp_f32 v35, v91\n"
"v_mfma_f32_16x16x32_bf16 v[120:123], a[224:227], v[136:139], v[120:123]\n"
"v_add_f32 v26, v26, v35\n"
"v_cvt_pk_bf16_f32 v185, v34, v35\n"
"v_mfma_f32_16x16x32_bf16 v[120:123], a[228:231], v[168:171], v[120:123]\n"
"ds_read_b64_tr_b16 a[212:213], v9 offset:12416\n"
"ds_read_b64_tr_b16 a[214:215], v9 offset:18560\n"
"v_mfma_f32_16x16x32_bf16 v[120:123], a[232:235], v[200:203], v[120:123]\n"
"v_exp_f32 v36, v92\n"
"v_mfma_f32_16x16x32_bf16 v[120:123], a[236:239], v[232:235], v[120:123]\n"
"v_exp_f32 v37, v93\n"
"v_mfma_f32_16x16x32_bf16 v[124:127], a[236:239], v[236:239], v[52:55]\n"
"v_add_f32 v27, v36, v37\n"
"v_cvt_pk_bf16_f32 v188, v36, v37\n"
"v_mfma_f32_16x16x32_bf16 v[124:127], a[232:235], v[204:207], v[124:127]\n"
"s_cmp_lt_u32 s4, s6\n"
"s_cbranch_scc1 .Lmha_slp4\n"
".Lmha_wk4:\n"
"v_mfma_f32_16x16x32_bf16 v[124:127], a[228:231], v[172:175], v[124:127]\n"
"v_exp_f32 v38, v94\n"
"v_mfma_f32_16x16x32_bf16 v[124:127], a[224:227], v[140:143], v[124:127]\n"
"v_add_f32 v27, v27, v38\n"
"s_sub_u32 s4, s4, 20\n"
"v_mfma_f32_16x16x32_bf16 v[124:127], a[220:223], v[108:111], v[124:127]\n"
"v_exp_f32 v39, v95\n"
"v_mfma_f32_16x16x32_bf16 v[124:127], a[216:219], v[76:79], v[124:127]\n"
"s_waitcnt lgkmcnt(0)\n"
"v_add_f32 v27, v27, v39\n"
"v_mfma_f32_16x16x32_bf16 a[0:3], a[192:195], v[144:147], a[0:3]\n"
"ds_read_b64_tr_b16 a[216:217], v10 offset:12416\n"
"ds_read_b64_tr_b16 a[218:219], v10 offset:18560\n"
"v_mfma_f32_16x16x32_bf16 a[4:7], a[192:195], v[148:151], a[4:7]\n"
"v_exp_f32 v32, v112\n"
"v_mfma_f32_16x16x32_bf16 a[8:11], a[192:195], v[152:155], a[8:11]\n"
"ds_read_b64_tr_b16 a[220:221], v11 offset:12416\n"
"ds_read_b64_tr_b16 a[222:223], v11 offset:18560\n"
"v_mfma_f32_16x16x32_bf16 a[12:15], a[192:195], v[156:159], a[12:15]\n"
"v_exp_f32 v33, v113\n"
"v_mfma_f32_16x16x32_bf16 a[28:31], a[196:199], v[156:159], a[28:31]\n"
"ds_read_b64_tr_b16 a[224:225], v8 offset:12544\n"
"ds_read_b64_tr_b16 a[226:227], v8 offset:18688\n"
"v_mfma_f32_16x16x32_bf16 a[24:27], a[196:199], v[152:155], a[24:27]\n"
"v_cvt_pk_bf16_f32 v189, v38, v39\n"
"v_add_f32 v24, v24, v32\n"
"v_mfma_f32_16x16x32_bf16 a[20:23], a[196:199], v[148:151], a[20:23]\n"
"ds_read_b64_tr_b16 a[228:229], v9 offset:12544\n"
"ds_read_b64_tr_b16 a[230:231], v9 offset:18688\n"
"v_mfma_f32_16x16x32_bf16 a[16:19], a[196:199], v[144:147], a[16:19]\n"
"v_exp_f32 v34, v114\n"
"v_mfma_f32_16x16x32_bf16 a[32:35], a[200:203], v[144:147], a[32:35]\n"
"ds_read_b64_tr_b16 a[232:233], v10 offset:12544\n"
"ds_read_b64_tr_b16 a[234:235], v10 offset:18688\n"
"v_mfma_f32_16x16x32_bf16 a[36:39], a[200:203], v[148:151], a[36:39]\n"
"v_exp_f32 v35, v115\n"
"v_mfma_f32_16x16x32_bf16 a[40:43], a[200:203], v[152:155], a[40:43]\n"
"ds_read_b64_tr_b16 a[236:237], v11 offset:12544\n"
"ds_read_b64_tr_b16 a[238:239], v11 offset:18688\n"
"v_mfma_f32_16x16x32_bf16 a[44:47], a[200:203], v[156:159], a[44:47]\n"
"v_exp_f32 v36, v116\n"
"v_mfma_f32_16x16x32_bf16 a[60:63], a[204:207], v[156:159], a[60:63]\n"
"v_add_f32 v24, v24, v33\n"
"v_cvt_pk_bf16_f32 v178, v32, v33\n"
"v_mfma_f32_16x16x32_bf16 a[56:59], a[204:207], v[152:155], a[56:59]\n"
"v_exp_f32 v37, v117\n"
"v_mfma_f32_16x16x32_bf16 a[52:55], a[204:207], v[148:151], a[52:55]\n"
"v_add_f32 v24, v24, v34\n"
"v_add_f32 v24, v24, v35\n"
"v_mfma_f32_16x16x32_bf16 a[48:51], a[204:207], v[144:147], a[48:51]\n"
"v_cvt_pk_bf16_f32 v179, v34, v35\n"
"v_add_f32 v25, v25, v36\n"
"v_mfma_f32_16x16x32_bf16 a[64:67], a[208:211], v[144:147], a[64:67]\n"
"v_exp_f32 v38, v118\n"
"v_mfma_f32_16x16x32_bf16 a[68:71], a[208:211], v[148:151], a[68:71]\n"
"s_waitcnt vmcnt(6) lgkmcnt(0)\n"
"s_barrier\n"
"v_mfma_f32_16x16x32_bf16 a[72:75], a[208:211], v[152:155], a[72:75]\n"
"s_add_u32 m0, s76, 24576\n"
"v_add_f32 v25, v25, v37\n"
"v_mfma_f32_16x16x32_bf16 a[76:79], a[208:211], v[156:159], a[76:79]\n"
"buffer_load_dwordx4 v252, s[48:51], s11 offen nt lds\n"
"v_cvt_pk_bf16_f32 v182, v36, v37\n"
"v_mfma_f32_16x16x32_bf16 a[92:95], a[212:215], v[156:159], a[92:95]\n"
"v_exp_f32 v39, v119\n"
"v_mfma_f32_16x16x32_bf16 a[88:91], a[212:215], v[152:155], a[88:91]\n"
"v_exp_f32 v32, v120\n"
"v_mfma_f32_16x16x32_bf16 a[84:87], a[212:215], v[148:151], a[84:87]\n"
"v_add_f32 v25, v25, v38\n"
"v_add_f32 v25, v25, v39\n"
"v_mfma_f32_16x16x32_bf16 a[80:83], a[212:215], v[144:147], a[80:83]\n"
"v_exp_f32 v33, v121\n"
"v_mfma_f32_16x16x32_bf16 a[96:99], a[216:219], v[144:147], a[96:99]\n"
"ds_read_b128 a[192:195], v0 offset:0\n"
"buffer_load_dwordx4 v253, s[48:51], s11 offen offset:1024 nt lds\n"
"v_mfma_f32_16x16x32_bf16 a[100:103], a[216:219], v[148:151], a[100:103]\n"
"v_cvt_pk_bf16_f32 v183, v38, v39\n"
"v_add_f32 v26, v26, v32\n"
"v_mfma_f32_16x16x32_bf16 a[104:107], a[216:219], v[152:155], a[104:107]\n"
"v_exp_f32 v34, v122\n"
"v_mfma_f32_16x16x32_bf16 a[108:111], a[216:219], v[156:159], a[108:111]\n"
"v_add_f32 v26, v26, v33\n"
"v_cvt_pk_bf16_f32 v186, v32, v33\n"
"v_mfma_f32_16x16x32_bf16 a[124:127], a[220:223], v[156:159], a[124:127]\n"
"ds_read_b128 a[196:199], v1 offset:0\n"
"buffer_load_dwordx4 v254, s[48:51], s11 offen offset:2048 nt lds\n"
"v_mfma_f32_16x16x32_bf16 a[120:123], a[220:223], v[152:155], a[120:123]\n"
"v_exp_f32 v35, v123\n"
"v_mfma_f32_16x16x32_bf16 a[116:119], a[220:223], v[148:151], a[116:119]\n"
"v_add_f32 v26, v26, v34\n"
"v_add_f32 v26, v26, v35\n"
"v_mfma_f32_16x16x32_bf16 a[112:115], a[220:223], v[144:147], a[112:115]\n"
"v_exp_f32 v36, v124\n"
"v_mfma_f32_16x16x32_bf16 a[128:131], a[224:227], v[144:147], a[128:131]\n"
"ds_read_b128 a[200:203], v0 offset:128\n"
"s_add_u32 m0, s76, 86016\n"
"v_mfma_f32_16x16x32_bf16 a[132:135], a[224:227], v[148:151], a[132:135]\n"
"v_exp_f32 v37, v125\n"
"v_mfma_f32_16x16x32_bf16 a[136:139], a[224:227], v[152:155], a[136:139]\n"
"v_cvt_pk_bf16_f32 v187, v34, v35\n"
"v_add_f32 v27, v27, v36\n"
"v_mfma_f32_16x16x32_bf16 a[140:143], a[224:227], v[156:159], a[140:143]\n"
"v_add_f32 v27, v27, v37\n"
"v_cvt_pk_bf16_f32 v190, v36, v37\n"
"v_mfma_f32_16x16x32_bf16 a[156:159], a[228:231], v[156:159], a[156:159]\n"
"ds_read_b128 a[204:207], v1 offset:128\n"
"buffer_load_dwordx4 v15, s[44:47], s17 offen nt lds\n"
"v_mfma_f32_16x16x32_bf16 a[152:155], a[228:231], v[152:155], a[152:155]\n"
"v_exp_f32 v38, v126\n"
"v_mfma_f32_16x16x32_bf16 a[148:151], a[228:231], v[148:151], a[148:151]\n"
"v_add_f32 v27, v27, v38\n"
"s_max_u32 s6, s6, s4\n"
"v_mfma_f32_16x16x32_bf16 a[144:147], a[228:231], v[144:147], a[144:147]\n"
"v_exp_f32 v39, v127\n"
"v_mfma_f32_16x16x32_bf16 a[160:163], a[232:235], v[144:147], a[160:163]\n"
"ds_read_b128 a[208:211], v0 offset:256\n"
"buffer_load_dwordx4 v16, s[44:47], s17 offen offset:1024 nt lds\n"
"v_mfma_f32_16x16x32_bf16 a[164:167], a[232:235], v[148:151], a[164:167]\n"
"ds_read_b128 a[212:215], v1 offset:256\n"
"v_add_f32 v27, v27, v39\n"
"v_mfma_f32_16x16x32_bf16 a[168:171], a[232:235], v[152:155], a[168:171]\n"
"v_cvt_pk_bf16_f32 v191, v38, v39\n"
"s_add_u32 s6, s6, s7\n"
"v_mfma_f32_16x16x32_bf16 a[172:175], a[232:235], v[156:159], a[172:175]\n"
"v_mfma_f32_16x16x32_bf16 a[188:191], a[236:239], v[156:159], a[188:191]\n"
"v_max3_f32 v28, v24, v25, v26\n"
"v_mfma_f32_16x16x32_bf16 a[184:187], a[236:239], v[152:155], a[184:187]\n"
"v_max_f32 v28, v28, v27\n"
"buffer_load_dwordx4 v17, s[44:47], s17 offen offset:2048 nt lds\n"
"v_mfma_f32_16x16x32_bf16 a[180:183], a[236:239], v[148:151], a[180:183]\n"
"v_cmp_lt_f32 vcc, s27, v28\n"
"v_mfma_f32_16x16x32_bf16 a[176:179], a[236:239], v[144:147], a[176:179]\n"
"s_waitcnt lgkmcnt(0)\n"
"s_cbranch_vccnz .Lmha_rare_l4\n"
".Lmha_ret_l4:\n"
"v_mfma_f32_16x16x32_bf16 v[80:83], a[192:195], v[64:67], v[40:43]\n"
"ds_read_b128 a[216:219], v0 offset:6144\n"
"s_add_u32 s12, s12, 73728\n"
"v_mfma_f32_16x16x32_bf16 v[80:83], a[196:199], v[96:99], v[80:83]\n"
"ds_read_b128 a[220:223], v1 offset:6144\n"
"s_add_u32 s18, s18, 73728\n"
"v_mfma_f32_16x16x32_bf16 v[80:83], a[200:203], v[128:131], v[80:83]\n"
"ds_read_b128 a[224:227], v0 offset:6272\n"
"v_mfma_f32_16x16x32_bf16 v[80:83], a[204:207], v[160:163], v[80:83]\n"
"ds_read_b128 a[228:231], v1 offset:6272\n"
"v_mfma_f32_16x16x32_bf16 v[80:83], a[208:211], v[192:195], v[80:83]\n"
"ds_read_b128 a[232:235], v0 offset:6400\n"
"v_mfma_f32_16x16x32_bf16 v[80:83], a[212:215], v[224:227], v[80:83]\n"
"ds_read_b128 a[236:239], v1 offset:6400\n"
"s_memrealtime s[4:5]\n"
"v_mfma_f32_16x16x32_bf16 v[84:87], a[212:215], v[228:231], v[44:47]\n"
"v_add_f32 v20, v20, v24\n"
"v_mfma_f32_16x16x32_bf16 v[84:87], a[208:211], v[196:199], v[84:87]\n"
"v_add_f32 v21, v21, v25\n"
"v_mfma_f32_16x16x32_bf16 v[84:87], a[204:207], v[164:167], v[84:87]\n"
"v_add_f32 v22, v22, v26\n"
"v_mfma_f32_16x16x32_bf16 v[84:87], a[200:203], v[132:135], v[84:87]\n"
"v_add_f32 v23, v23, v27\n"
"v_mfma_f32_16x16x32_bf16 v[84:87], a[196:199], v[100:103], v[84:87]\n"
"v_exp_f32 v32, v80\n"
"v_mfma_f32_16x16x32_bf16 v[84:87], a[192:195], v[68:71], v[84:87]\n"
"v_exp_f32 v33, v81\n"
"v_mfma_f32_16x16x32_bf16 v[88:91], a[192:195], v[72:75], v[48:51]\n"
"v_add_f32 v24, v32, v33\n"
"v_cvt_pk_bf16_f32 v144, v32, v33\n"
"v_mfma_f32_16x16x32_bf16 v[88:91], a[196:199], v[104:107], v[88:91]\n"
"v_mfma_f32_16x16x32_bf16 v[88:91], a[200:203], v[136:139], v[88:91]\n"
"v_exp_f32 v34, v82\n"
"v_mfma_f32_16x16x32_bf16 v[88:91], a[204:207], v[168:171], v[88:91]\n"
"v_add_f32 v24, v24, v34\n"
"v_mfma_f32_16x16x32_bf16 v[88:91], a[208:211], v[200:203], v[88:91]\n"
"v_exp_f32 v35, v83\n"
"v_mfma_f32_16x16x32_bf16 v[88:91], a[212:215], v[232:235], v[88:91]\n"
"v_add_f32 v24, v24, v35\n"
"v_cvt_pk_bf16_f32 v145, v34, v35\n"
"v_mfma_f32_16x16x32_bf16 v[92:95], a[212:215], v[236:239], v[52:55]\n"
"v_mfma_f32_16x16x32_bf16 v[92:95], a[208:211], v[204:207], v[92:95]\n"
"v_exp_f32 v36, v84\n"
"v_mfma_f32_16x16x32_bf16 v[92:95], a[204:207], v[172:175], v[92:95]\n"
"v_mfma_f32_16x16x32_bf16 v[92:95], a[200:203], v[140:143], v[92:95]\n"
"v_exp_f32 v37, v85\n"
"v_mfma_f32_16x16x32_bf16 v[92:95], a[196:199], v[108:111], v[92:95]\n"
"v_add_f32 v25, v36, v37\n"
"v_cvt_pk_bf16_f32 v148, v36, v37\n"
"v_mfma_f32_16x16x32_bf16 v[92:95], a[192:195], v[76:79], v[92:95]\n"
"s_waitcnt lgkmcnt(0)\n"
"v_mfma_f32_16x16x32_bf16 v[112:115], a[216:219], v[64:67], v[40:43]\n"
"ds_read_b64_tr_b16 a[192:193], v8 offset:24576\n"
"ds_read_b64_tr_b16 a[194:195], v8 offset:30720\n"
"v_mfma_f32_16x16x32_bf16 v[112:115], a[220:223], v[96:99], v[112:115]\n"
"v_exp_f32 v38, v86\n"
"v_mfma_f32_16x16x32_bf16 v[112:115], a[224:227], v[128:131], v[112:115]\n"
"v_exp_f32 v39, v87\n"
"v_mfma_f32_16x16x32_bf16 v[112:115], a[228:231], v[160:163], v[112:115]\n"
"ds_read_b64_tr_b16 a[196:197], v9 offset:24576\n"
"ds_read_b64_tr_b16 a[198:199], v9 offset:30720\n"
"v_mfma_f32_16x16x32_bf16 v[112:115], a[232:235], v[192:195], v[112:115]\n"
"v_add_f32 v25, v25, v38\n"
"v_add_f32 v25, v25, v39\n"
"v_mfma_f32_16x16x32_bf16 v[112:115], a[236:239], v[224:227], v[112:115]\n"
"v_exp_f32 v32, v88\n"
"v_mfma_f32_16x16x32_bf16 v[116:119], a[236:239], v[228:231], v[44:47]\n"
"ds_read_b64_tr_b16 a[200:201], v10 offset:24576\n"
"ds_read_b64_tr_b16 a[202:203], v10 offset:30720\n"
"v_mfma_f32_16x16x32_bf16 v[116:119], a[232:235], v[196:199], v[116:119]\n"
"v_exp_f32 v33, v89\n"
"v_mfma_f32_16x16x32_bf16 v[116:119], a[228:231], v[164:167], v[116:119]\n"
"v_cvt_pk_bf16_f32 v149, v38, v39\n"
"v_add_f32 v26, v32, v33\n"
"v_mfma_f32_16x16x32_bf16 v[116:119], a[224:227], v[132:135], v[116:119]\n"
"ds_read_b64_tr_b16 a[204:205], v11 offset:24576\n"
"ds_read_b64_tr_b16 a[206:207], v11 offset:30720\n"
"v_mfma_f32_16x16x32_bf16 v[116:119], a[220:223], v[100:103], v[116:119]\n"
"v_exp_f32 v34, v90\n"
"v_mfma_f32_16x16x32_bf16 v[116:119], a[216:219], v[68:71], v[116:119]\n"
"v_cvt_pk_bf16_f32 v152, v32, v33\n"
"v_add_f32 v26, v26, v34\n"
"v_mfma_f32_16x16x32_bf16 v[120:123], a[216:219], v[72:75], v[48:51]\n"
"ds_read_b64_tr_b16 a[208:209], v8 offset:24704\n"
"ds_read_b64_tr_b16 a[210:211], v8 offset:30848\n"
"v_mfma_f32_16x16x32_bf16 v[120:123], a[220:223], v[104:107], v[120:123]\n"
"v_exp_f32 v35, v91\n"
"v_mfma_f32_16x16x32_bf16 v[120:123], a[224:227], v[136:139], v[120:123]\n"
"v_add_f32 v26, v26, v35\n"
"v_cvt_pk_bf16_f32 v153, v34, v35\n"
"v_mfma_f32_16x16x32_bf16 v[120:123], a[228:231], v[168:171], v[120:123]\n"
"ds_read_b64_tr_b16 a[212:213], v9 offset:24704\n"
"ds_read_b64_tr_b16 a[214:215], v9 offset:30848\n"
"v_mfma_f32_16x16x32_bf16 v[120:123], a[232:235], v[200:203], v[120:123]\n"
"v_exp_f32 v36, v92\n"
"v_mfma_f32_16x16x32_bf16 v[120:123], a[236:239], v[232:235], v[120:123]\n"
"v_exp_f32 v37, v93\n"
"v_mfma_f32_16x16x32_bf16 v[124:127], a[236:239], v[236:239], v[52:55]\n"
"v_add_f32 v27, v36, v37\n"
"v_cvt_pk_bf16_f32 v156, v36, v37\n"
"v_mfma_f32_16x16x32_bf16 v[124:127], a[232:235], v[204:207], v[124:127]\n"
"s_cmp_lt_u32 s4, s6\n"
"s_cbranch_scc1 .Lmha_slp5\n"
".Lmha_wk5:\n"
"v_mfma_f32_16x16x32_bf16 v[124:127], a[228:231], v[172:175], v[124:127]\n"
"v_exp_f32 v38, v94\n"
"v_mfma_f32_16x16x32_bf16 v[124:127], a[224:227], v[140:143], v[124:127]\n"
"v_add_f32 v27, v27, v38\n"
"s_sub_u32 s4, s4, 20\n"
"v_mfma_f32_16x16x32_bf16 v[124:127], a[220:223], v[108:111], v[124:127]\n"
"v_exp_f32 v39, v95\n"
"v_mfma_f32_16x16x32_bf16 v[124:127], a[216:219], v[76:79], v[124:127]\n"
"s_waitcnt lgkmcnt(0)\n"
"v_add_f32 v27, v27, v39\n"
"v_mfma_f32_16x16x32_bf16 a[0:3], a[192:195], v[176:179], a[0:3]\n"
"ds_read_b64_tr_b16 a[216:217], v10 offset:24704\n"
"ds_read_b64_tr_b16 a[218:219], v10 offset:30848\n"
"v_mfma_f32_16x16x32_bf16 a[4:7], a[192:195], v[180:183], a[4:7]\n"
"v_exp_f32 v32, v112\n"
"v_mfma_f32_16x16x32_bf16 a[8:11], a[192:195], v[184:187], a[8:11]\n"
"ds_read_b64_tr_b16 a[220:221], v11 offset:24704\n"
"ds_read_b64_tr_b16 a[222:223], v11 offset:30848\n"
"v_mfma_f32_16x16x32_bf16 a[12:15], a[192:195], v[188:191], a[12:15]\n"
"v_exp_f32 v33, v113\n"
"v_mfma_f32_16x16x32_bf16 a[28:31], a[196:199], v[188:191], a[28:31]\n"
"ds_read_b64_tr_b16 a[224:225], v8 offset:24832\n"
"ds_read_b64_tr_b16 a[226:227], v8 offset:30976\n"
"v_mfma_f32_16x16x32_bf16 a[24:27], a[196:199], v[184:187], a[24:27]\n"
"v_cvt_pk_bf16_f32 v157, v38, v39\n"
"v_add_f32 v24, v24, v32\n"
"v_mfma_f32_16x16x32_bf16 a[20:23], a[196:199], v[180:183], a[20:23]\n"
"ds_read_b64_tr_b16 a[228:229], v9 offset:24832\n"
"ds_read_b64_tr_b16 a[230:231], v9 offset:30976\n"
"v_mfma_f32_16x16x32_bf16 a[16:19], a[196:199], v[176:179], a[16:19]\n"
"v_exp_f32 v34, v114\n"
"v_mfma_f32_16x16x32_bf16 a[32:35], a[200:203], v[176:179], a[32:35]\n"
"ds_read_b64_tr_b16 a[232:233], v10 offset:24832\n"
"ds_read_b64_tr_b16 a[234:235], v10 offset:30976\n"
"v_mfma_f32_16x16x32_bf16 a[36:39], a[200:203], v[180:183], a[36:39]\n"
"v_exp_f32 v35, v115\n"
"v_mfma_f32_16x16x32_bf16 a[40:43], a[200:203], v[184:187], a[40:43]\n"
"ds_read_b64_tr_b16 a[236:237], v11 offset:24832\n"
"ds_read_b64_tr_b16 a[238:239], v11 offset:30976\n"
"v_mfma_f32_16x16x32_bf16 a[44:47], a[200:203], v[188:191], a[44:47]\n"
"v_exp_f32 v36, v116\n"
"v_mfma_f32_16x16x32_bf16 a[60:63], a[204:207], v[188:191], a[60:63]\n"
"v_add_f32 v24, v24, v33\n"
"v_cvt_pk_bf16_f32 v146, v32, v33\n"
"v_mfma_f32_16x16x32_bf16 a[56:59], a[204:207], v[184:187], a[56:59]\n"
"v_exp_f32 v37, v117\n"
"v_mfma_f32_16x16x32_bf16 a[52:55], a[204:207], v[180:183], a[52:55]\n"
"v_add_f32 v24, v24, v34\n"
"v_add_f32 v24, v24, v35\n"
"v_mfma_f32_16x16x32_bf16 a[48:51], a[204:207], v[176:179], a[48:51]\n"
"v_cvt_pk_bf16_f32 v147, v34, v35\n"
"v_add_f32 v25, v25, v36\n"
"v_mfma_f32_16x16x32_bf16 a[64:67], a[208:211], v[176:179], a[64:67]\n"
"v_exp_f32 v38, v118\n"
"v_mfma_f32_16x16x32_bf16 a[68:71], a[208:211], v[180:183], a[68:71]\n"
"s_waitcnt vmcnt(6) lgkmcnt(0)\n"
"s_barrier\n"
"v_mfma_f32_16x16x32_bf16 a[72:75], a[208:211], v[184:187], a[72:75]\n"
"s_add_u32 m0, s76, 36864\n"
"v_add_f32 v25, v25, v37\n"
"v_mfma_f32_16x16x32_bf16 a[76:79], a[208:211], v[188:191], a[76:79]\n"
"buffer_load_dwordx4 v252, s[48:51], s12 offen nt lds\n"
"v_cvt_pk_bf16_f32 v150, v36, v37\n"
"v_mfma_f32_16x16x32_bf16 a[92:95], a[212:215], v[188:191], a[92:95]\n"
"v_exp_f32 v39, v119\n"
"v_mfma_f32_16x16x32_bf16 a[88:91], a[212:215], v[184:187], a[88:91]\n"
"v_exp_f32 v32, v120\n"
"v_mfma_f32_16x16x32_bf16 a[84:87], a[212:215], v[180:183], a[84:87]\n"
"v_add_f32 v25, v25, v38\n"
"v_add_f32 v25, v25, v39\n"
"v_mfma_f32_16x16x32_bf16 a[80:83], a[212:215], v[176:179], a[80:83]\n"
"v_exp_f32 v33, v121\n"
"v_mfma_f32_16x16x32_bf16 a[96:99], a[216:219], v[176:179], a[96:99]\n"
"ds_read_b128 a[192:195], v0 offset:12288\n"
"buffer_load_dwordx4 v253, s[48:51], s12 offen offset:1024 nt lds\n"
"v_mfma_f32_16x16x32_bf16 a[100:103], a[216:219], v[180:183], a[100:103]\n"
"v_cvt_pk_bf16_f32 v151, v38, v39\n"
"v_add_f32 v26, v26, v32\n"
"v_mfma_f32_16x16x32_bf16 a[104:107], a[216:219], v[184:187], a[104:107]\n"
"v_exp_f32 v34, v122\n"
"v_mfma_f32_16x16x32_bf16 a[108:111], a[216:219], v[188:191], a[108:111]\n"
"v_add_f32 v26, v26, v33\n"
"v_cvt_pk_bf16_f32 v154, v32, v33\n"
"v_mfma_f32_16x16x32_bf16 a[124:127], a[220:223], v[188:191], a[124:127]\n"
"ds_read_b128 a[196:199], v1 offset:12288\n"
"buffer_load_dwordx4 v254, s[48:51], s12 offen offset:2048 nt lds\n"
"v_mfma_f32_16x16x32_bf16 a[120:123], a[220:223], v[184:187], a[120:123]\n"
"v_exp_f32 v35, v123\n"
"v_mfma_f32_16x16x32_bf16 a[116:119], a[220:223], v[180:183], a[116:119]\n"
"v_add_f32 v26, v26, v34\n"
"v_add_f32 v26, v26, v35\n"
"v_mfma_f32_16x16x32_bf16 a[112:115], a[220:223], v[176:179], a[112:115]\n"
"v_exp_f32 v36, v124\n"
"v_mfma_f32_16x16x32_bf16 a[128:131], a[224:227], v[176:179], a[128:131]\n"
"ds_read_b128 a[200:203], v0 offset:12416\n"
"s_add_u32 m0, s76, 98304\n"
"v_mfma_f32_16x16x32_bf16 a[132:135], a[224:227], v[180:183], a[132:135]\n"
"v_exp_f32 v37, v125\n"
"v_mfma_f32_16x16x32_bf16 a[136:139], a[224:227], v[184:187], a[136:139]\n"
"v_cvt_pk_bf16_f32 v155, v34, v35\n"
"v_add_f32 v27, v27, v36\n"
"v_mfma_f32_16x16x32_bf16 a[140:143], a[224:227], v[188:191], a[140:143]\n"
"v_add_f32 v27, v27, v37\n"
"v_cvt_pk_bf16_f32 v158, v36, v37\n"
"v_mfma_f32_16x16x32_bf16 a[156:159], a[228:231], v[188:191], a[156:159]\n"
"ds_read_b128 a[204:207], v1 offset:12416\n"
"buffer_load_dwordx4 v220, s[52:55], s18 offen nt lds\n"
"v_mfma_f32_16x16x32_bf16 a[152:155], a[228:231], v[184:187], a[152:155]\n"
"v_exp_f32 v38, v126\n"
"v_mfma_f32_16x16x32_bf16 a[148:151], a[228:231], v[180:183], a[148:151]\n"
"v_add_f32 v27, v27, v38\n"
"s_max_u32 s6, s6, s4\n"
"v_mfma_f32_16x16x32_bf16 a[144:147], a[228:231], v[176:179], a[144:147]\n"
"v_exp_f32 v39, v127\n"
"v_mfma_f32_16x16x32_bf16 a[160:163], a[232:235], v[176:179], a[160:163]\n"
"ds_read_b128 a[208:211], v0 offset:12544\n"
"buffer_load_dwordx4 v221, s[52:55], s18 offen offset:1024 nt lds\n"
"v_mfma_f32_16x16x32_bf16 a[164:167], a[232:235], v[180:183], a[164:167]\n"
"ds_read_b128 a[212:215], v1 offset:12544\n"
"v_add_f32 v27, v27, v39\n"
"v_mfma_f32_16x16x32_bf16 a[168:171], a[232:235], v[184:187], a[168:171]\n"
"v_cvt_pk_bf16_f32 v159, v38, v39\n"
"s_add_u32 s6, s6, s7\n"
"v_mfma_f32_16x16x32_bf16 a[172:175], a[232:235], v[188:191], a[172:175]\n"
"v_mfma_f32_16x16x32_bf16 a[188:191], a[236:239], v[188:191], a[188:191]\n"
"v_max3_f32 v28, v24, v25, v26\n"
"v_mfma_f32_16x16x32_bf16 a[184:187], a[236:239], v[184:187], a[184:187]\n"
"v_max_f32 v28, v28, v27\n"
"buffer_load_dwordx4 v222, s[52:55], s18 offen offset:2048 nt lds\n"
"v_mfma_f32_16x16x32_bf16 a[180:183], a[236:239], v[180:183], a[180:183]\n"
"v_cmp_lt_f32 vcc, s27, v28\n"
"v_mfma_f32_16x16x32_bf16 a[176:179], a[236:239], v[176:179], a[176:179]\n"
"s_waitcnt lgkmcnt(0)\n"
"s_cbranch_vccnz .Lmha_rare_l5\n"
".Lmha_ret_l5:\n"
"v_mfma_f32_16x16x32_bf16 v[80:83], a[192:195], v[64:67], v[40:43]\n"
"ds_read_b128 a[216:219], v0 offset:18432\n"
"s_add_u32 s13, s13, 73728\n"
"v_mfma_f32_16x16x32_bf16 v[80:83], a[196:199], v[96:99], v[80:83]\n"
"ds_read_b128 a[220:223], v1 offset:18432\n"
"s_add_u32 s19, s19, 73728\n"
"v_mfma_f32_16x16x32_bf16 v[80:83], a[200:203], v[128:131], v[80:83]\n"
"ds_read_b128 a[224:227], v0 offset:18560\n"
"buffer_load_dword v62, v255, s[20:23], 0 offen\n"
"v_mfma_f32_16x16x32_bf16 v[80:83], a[204:207], v[160:163], v[80:83]\n"
"ds_read_b128 a[228:231], v1 offset:18560\n"
"v_mfma_f32_16x16x32_bf16 v[80:83], a[208:211], v[192:195], v[80:83]\n"
"ds_read_b128 a[232:235], v0 offset:18688\n"
"s_add_u32 s34, s34, 1\n"
"v_mfma_f32_16x16x32_bf16 v[80:83], a[212:215], v[224:227], v[80:83]\n"
"ds_read_b128 a[236:239], v1 offset:18688\n"
"s_memrealtime s[4:5]\n"
"v_mfma_f32_16x16x32_bf16 v[84:87], a[212:215], v[228:231], v[44:47]\n"
"v_add_f32 v20, v20, v24\n"
"v_mfma_f32_16x16x32_bf16 v[84:87], a[208:211], v[196:199], v[84:87]\n"
"v_add_f32 v21, v21, v25\n"
"v_mfma_f32_16x16x32_bf16 v[84:87], a[204:207], v[164:167], v[84:87]\n"
"v_add_f32 v22, v22, v26\n"
"v_mfma_f32_16x16x32_bf16 v[84:87], a[200:203], v[132:135], v[84:87]\n"
"v_add_f32 v23, v23, v27\n"
"v_mfma_f32_16x16x32_bf16 v[84:87], a[196:199], v[100:103], v[84:87]\n"
"v_exp_f32 v32, v80\n"
"v_mfma_f32_16x16x32_bf16 v[84:87], a[192:195], v[68:71], v[84:87]\n"
"v_exp_f32 v33, v81\n"
"v_mfma_f32_16x16x32_bf16 v[88:91], a[192:195], v[72:75], v[48:51]\n"
"v_add_f32 v24, v32, v33\n"
"v_cvt_pk_bf16_f32 v176, v32, v33\n"
"v_mfma_f32_16x16x32_bf16 v[88:91], a[196:199], v[104:107], v[88:91]\n"
"v_mfma_f32_16x16x32_bf16 v[88:91], a[200:203], v[136:139], v[88:91]\n"
"v_exp_f32 v34, v82\n"
"v_mfma_f32_16x16x32_bf16 v[88:91], a[204:207], v[168:171], v[88:91]\n"
"v_add_f32 v24, v24, v34\n"
"v_mfma_f32_16x16x32_bf16 v[88:91], a[208:211], v[200:203], v[88:91]\n"
"v_exp_f32 v35, v83\n"
"v_mfma_f32_16x16x32_bf16 v[88:91], a[212:215], v[232:235], v[88:91]\n"
"v_add_f32 v24, v24, v35\n"
"v_cvt_pk_bf16_f32 v177, v34, v35\n"
"v_mfma_f32_16x16x32_bf16 v[92:95], a[212:215], v[236:239], v[52:55]\n"
"v_mfma_f32_16x16x32_bf16 v[92:95], a[208:211], v[204:207], v[92:95]\n"
"v_exp_f32 v36, v84\n"
"v_mfma_f32_16x16x32_bf16 v[92:95], a[204:207], v[172:175], v[92:95]\n"
"v_mfma_f32_16x16x32_bf16 v[92:95], a[200:203], v[140:143], v[92:95]\n"
"v_exp_f32 v37, v85\n"
"v_mfma_f32_16x16x32_bf16 v[92:95], a[196:199], v[108:111], v[92:95]\n"
"v_add_f32 v25, v36, v37\n"
"v_cvt_pk_bf16_f32 v180, v36, v37\n"
"v_mfma_f32_16x16x32_bf16 v[92:95], a[192:195], v[76:79], v[92:95]\n"
"s_waitcnt lgkmcnt(0)\n"
"v_mfma_f32_16x16x32_bf16 v[112:115], a[216:219], v[64:67], v[40:43]\n"
"ds_read_b64_tr_b16 a[192:193], v4 offset:12288\n"
"ds_read_b64_tr_b16 a[194:195], v4 offset:18432\n"
"v_mfma_f32_16x16x32_bf16 v[112:115], a[220:223], v[96:99], v[112:115]\n"
"v_exp_f32 v38, v86\n"
"v_mfma_f32_16x16x32_bf16 v[112:115], a[224:227], v[128:131], v[112:115]\n"
"v_exp_f32 v39, v87\n"
"v_mfma_f32_16x16x32_bf16 v[112:115], a[228:231], v[160:163], v[112:115]\n"
"ds_read_b64_tr_b16 a[196:197], v5 offset:12288\n"
"ds_read_b64_tr_b16 a[198:199], v5 offset:18432\n"
"v_mfma_f32_16x16x32_bf16 v[112:115], a[232:235], v[192:195], v[112:115]\n"
"v_add_f32 v25, v25, v38\n"
"v_add_f32 v25, v25, v39\n"
"v_mfma_f32_16x16x32_bf16 v[112:115], a[236:239], v[224:227], v[112:115]\n"
"v_exp_f32 v32, v88\n"
"v_mfma_f32_16x16x32_bf16 v[116:119], a[236:239], v[228:231], v[44:47]\n"
"ds_read_b64_tr_b16 a[200:201], v6 offset:12288\n"
"ds_read_b64_tr_b16 a[202:203], v6 offset:18432\n"
"v_mfma_f32_16x16x32_bf16 v[116:119], a[232:235], v[196:199], v[116:119]\n"
"v_exp_f32 v33, v89\n"
"v_mfma_f32_16x16x32_bf16 v[116:119], a[228:231], v[164:167], v[116:119]\n"
"v_cvt_pk_bf16_f32 v181, v38, v39\n"
"v_add_f32 v26, v32, v33\n"
"v_mfma_f32_16x16x32_bf16 v[116:119], a[224:227], v[132:135], v[116:119]\n"
"ds_read_b64_tr_b16 a[204:205], v7 offset:12288\n"
"ds_read_b64_tr_b16 a[206:207], v7 offset:18432\n"
"v_mfma_f32_16x16x32_bf16 v[116:119], a[220:223], v[100:103], v[116:119]\n"
"v_exp_f32 v34, v90\n"
"v_mfma_f32_16x16x32_bf16 v[116:119], a[216:219], v[68:71], v[116:119]\n"
"v_cvt_pk_bf16_f32 v184, v32, v33\n"
"v_add_f32 v26, v26, v34\n"
"v_mfma_f32_16x16x32_bf16 v[120:123], a[216:219], v[72:75], v[48:51]\n"
"ds_read_b64_tr_b16 a[208:209], v4 offset:12416\n"
"ds_read_b64_tr_b16 a[210:211], v4 offset:18560\n"
"v_mfma_f32_16x16x32_bf16 v[120:123], a[220:223], v[104:107], v[120:123]\n"
"v_exp_f32 v35, v91\n"
"v_mfma_f32_16x16x32_bf16 v[120:123], a[224:227], v[136:139], v[120:123]\n"
"v_add_f32 v26, v26, v35\n"
"v_cvt_pk_bf16_f32 v185, v34, v35\n"
"v_mfma_f32_16x16x32_bf16 v[120:123], a[228:231], v[168:171], v[120:123]\n"
"ds_read_b64_tr_b16 a[212:213], v5 offset:12416\n"
"ds_read_b64_tr_b16 a[214:215], v5 offset:18560\n"
"v_mfma_f32_16x16x32_bf16 v[120:123], a[232:235], v[200:203], v[120:123]\n"
"v_exp_f32 v36, v92\n"
"v_mfma_f32_16x16x32_bf16 v[120:123], a[236:239], v[232:235], v[120:123]\n"
"v_exp_f32 v37, v93\n"
"v_mfma_f32_16x16x32_bf16 v[124:127], a[236:239], v[236:239], v[52:55]\n"
"v_add_f32 v27, v36, v37\n"
"v_cvt_pk_bf16_f32 v188, v36, v37\n"
"v_mfma_f32_16x16x32_bf16 v[124:127], a[232:235], v[204:207], v[124:127]\n"
"s_cmp_lt_u32 s4, s6\n"
"s_cbranch_scc1 .Lmha_slp0\n"
".Lmha_wk0:\n"
"v_mfma_f32_16x16x32_bf16 v[124:127], a[228:231], v[172:175], v[124:127]\n"
"v_exp_f32 v38, v94\n"
"v_mfma_f32_16x16x32_bf16 v[124:127], a[224:227], v[140:143], v[124:127]\n"
"v_add_f32 v27, v27, v38\n"
"s_sub_u32 s4, s4, 20\n"
"v_mfma_f32_16x16x32_bf16 v[124:127], a[220:223], v[108:111], v[124:127]\n"
"v_exp_f32 v39, v95\n"
"v_mfma_f32_16x16x32_bf16 v[124:127], a[216:219], v[76:79], v[124:127]\n"
"s_waitcnt lgkmcnt(0)\n"
"v_add_f32 v27, v27, v39\n"
"v_mfma_f32_16x16x32_bf16 a[0:3], a[192:195], v[144:147], a[0:3]\n"
"ds_read_b64_tr_b16 a[216:217], v6 offset:12416\n"
"ds_read_b64_tr_b16 a[218:219], v6 offset:18560\n"
"v_mfma_f32_16x16x32_bf16 a[4:7], a[192:195], v[148:151], a[4:7]\n"
"v_exp_f32 v32, v112\n"
"v_mfma_f32_16x16x32_bf16 a[8:11], a[192:195], v[152:155], a[8:11]\n"
"ds_read_b64_tr_b16 a[220:221], v7 offset:12416\n"
"ds_read_b64_tr_b16 a[222:223], v7 offset:18560\n"
"v_mfma_f32_16x16x32_bf16 a[12:15], a[192:195], v[156:159], a[12:15]\n"
"v_exp_f32 v33, v113\n"
"v_mfma_f32_16x16x32_bf16 a[28:31], a[196:199], v[156:159], a[28:31]\n"
"ds_read_b64_tr_b16 a[224:225], v4 offset:12544\n"
"ds_read_b64_tr_b16 a[226:227], v4 offset:18688\n"
"v_mfma_f32_16x16x32_bf16 a[24:27], a[196:199], v[152:155], a[24:27]\n"
"v_cvt_pk_bf16_f32 v189, v38, v39\n"
"v_add_f32 v24, v24, v32\n"
"v_mfma_f32_16x16x32_bf16 a[20:23], a[196:199], v[148:151], a[20:23]\n"
"ds_read_b64_tr_b16 a[228:229], v5 offset:12544\n"
"ds_read_b64_tr_b16 a[230:231], v5 offset:18688\n"
"v_mfma_f32_16x16x32_bf16 a[16:19], a[196:199], v[144:147], a[16:19]\n"
"v_exp_f32 v34, v114\n"
"v_mfma_f32_16x16x32_bf16 a[32:35], a[200:203], v[144:147], a[32:35]\n"
"ds_read_b64_tr_b16 a[232:233], v6 offset:12544\n"
"ds_read_b64_tr_b16 a[234:235], v6 offset:18688\n"
"v_mfma_f32_16x16x32_bf16 a[36:39], a[200:203], v[148:151], a[36:39]\n"
"v_exp_f32 v35, v115\n"
"v_mfma_f32_16x16x32_bf16 a[40:43], a[200:203], v[152:155], a[40:43]\n"
"ds_read_b64_tr_b16 a[236:237], v7 offset:12544\n"
"ds_read_b64_tr_b16 a[238:239], v7 offset:18688\n"
"v_mfma_f32_16x16x32_bf16 a[44:47], a[200:203], v[156:159], a[44:47]\n"
"v_exp_f32 v36, v116\n"
"v_mfma_f32_16x16x32_bf16 a[60:63], a[204:207], v[156:159], a[60:63]\n"
"v_add_f32 v24, v24, v33\n"
"v_cvt_pk_bf16_f32 v178, v32, v33\n"
"v_mfma_f32_16x16x32_bf16 a[56:59], a[204:207], v[152:155], a[56:59]\n"
"v_exp_f32 v37, v117\n"
"v_mfma_f32_16x16x32_bf16 a[52:55], a[204:207], v[148:151], a[52:55]\n"
"v_add_f32 v24, v24, v34\n"
"v_add_f32 v24, v24, v35\n"
"v_mfma_f32_16x16x32_bf16 a[48:51], a[204:207], v[144:147], a[48:51]\n"
"v_cvt_pk_bf16_f32 v179, v34, v35\n"
"v_add_f32 v25, v25, v36\n"
"v_mfma_f32_16x16x32_bf16 a[64:67], a[208:211], v[144:147], a[64:67]\n"
"v_exp_f32 v38, v118\n"
"v_mfma_f32_16x16x32_bf16 a[68:71], a[208:211], v[148:151], a[68:71]\n"
"s_waitcnt vmcnt(6) lgkmcnt(0)\n"
"s_barrier\n"
"v_mfma_f32_16x16x32_bf16 a[72:75], a[208:211], v[152:155], a[72:75]\n"
"s_add_u32 m0, s76, 49152\n"
"v_add_f32 v25, v25, v37\n"
"v_mfma_f32_16x16x32_bf16 a[76:79], a[208:211], v[156:159], a[76:79]\n"
"buffer_load_dwordx4 v252, s[48:51], s13 offen nt lds\n"
"v_cvt_pk_bf16_f32 v182, v36, v37\n"
"v_mfma_f32_16x16x32_bf16 a[92:95], a[212:215], v[156:159], a[92:95]\n"
"v_exp_f32 v39, v119\n"
"v_mfma_f32_16x16x32_bf16 a[88:91], a[212:215], v[152:155], a[88:91]\n"
"v_exp_f32 v32, v120\n"
"v_mfma_f32_16x16x32_bf16 a[84:87], a[212:215], v[148:151], a[84:87]\n"
"v_add_f32 v25, v25, v38\n"
"v_add_f32 v25, v25, v39\n"
"v_mfma_f32_16x16x32_bf16 a[80:83], a[212:215], v[144:147], a[80:83]\n"
"v_exp_f32 v33, v121\n"
"v_mfma_f32_16x16x32_bf16 a[96:99], a[216:219], v[144:147], a[96:99]\n"
"ds_read_b128 a[192:195], v0 offset:24576\n"
"buffer_load_dwordx4 v253, s[48:51], s13 offen offset:1024 nt lds\n"
"v_mfma_f32_16x16x32_bf16 a[100:103], a[216:219], v[148:151], a[100:103]\n"
"v_cvt_pk_bf16_f32 v183, v38, v39\n"
"v_add_f32 v26, v26, v32\n"
"v_mfma_f32_16x16x32_bf16 a[104:107], a[216:219], v[152:155], a[104:107]\n"
"v_exp_f32 v34, v122\n"
"v_mfma_f32_16x16x32_bf16 a[108:111], a[216:219], v[156:159], a[108:111]\n"
"v_add_f32 v26, v26, v33\n"
"v_cvt_pk_bf16_f32 v186, v32, v33\n"
"v_mfma_f32_16x16x32_bf16 a[124:127], a[220:223], v[156:159], a[124:127]\n"
"ds_read_b128 a[196:199], v1 offset:24576\n"
"buffer_load_dwordx4 v254, s[48:51], s13 offen offset:2048 nt lds\n"
"v_mfma_f32_16x16x32_bf16 a[120:123], a[220:223], v[152:155], a[120:123]\n"
"v_exp_f32 v35, v123\n"
"v_mfma_f32_16x16x32_bf16 a[116:119], a[220:223], v[148:151], a[116:119]\n"
"v_add_f32 v26, v26, v34\n"
"v_add_f32 v26, v26, v35\n"
"v_mfma_f32_16x16x32_bf16 a[112:115], a[220:223], v[144:147], a[112:115]\n"
"v_exp_f32 v36, v124\n"
"v_mfma_f32_16x16x32_bf16 a[128:131], a[224:227], v[144:147], a[128:131]\n"
"ds_read_b128 a[200:203], v0 offset:24704\n"
"s_add_u32 m0, s76, 110592\n"
"v_mfma_f32_16x16x32_bf16 a[132:135], a[224:227], v[148:151], a[132:135]\n"
"v_exp_f32 v37, v125\n"
"v_mfma_f32_16x16x32_bf16 a[136:139], a[224:227], v[152:155], a[136:139]\n"
"v_cvt_pk_bf16_f32 v187, v34, v35\n"
"v_add_f32 v27, v27, v36\n"
"v_mfma_f32_16x16x32_bf16 a[140:143], a[224:227], v[156:159], a[140:143]\n"
"v_add_f32 v27, v27, v37\n"
"v_cvt_pk_bf16_f32 v190, v36, v37\n"
"v_mfma_f32_16x16x32_bf16 a[156:159], a[228:231], v[156:159], a[156:159]\n"
"ds_read_b128 a[204:207], v1 offset:24704\n"
"buffer_load_dwordx4 v220, s[52:55], s19 offen nt lds\n"
"v_mfma_f32_16x16x32_bf16 a[152:155], a[228:231], v[152:155], a[152:155]\n"
"v_exp_f32 v38, v126\n"
"v_mfma_f32_16x16x32_bf16 a[148:151], a[228:231], v[148:151], a[148:151]\n"
"v_add_f32 v27, v27, v38\n"
"s_max_u32 s6, s6, s4\n"
"v_mfma_f32_16x16x32_bf16 a[144:147], a[228:231], v[144:147], a[144:147]\n"
"v_exp_f32 v39, v127\n"
"v_mfma_f32_16x16x32_bf16 a[160:163], a[232:235], v[144:147], a[160:163]\n"
"ds_read_b128 a[208:211], v0 offset:24832\n"
"buffer_load_dwordx4 v221, s[52:55], s19 offen offset:1024 nt lds\n"
"v_mfma_f32_16x16x32_bf16 a[164:167], a[232:235], v[148:151], a[164:167]\n"
"ds_read_b128 a[212:215], v1 offset:24832\n"
"v_add_f32 v27, v27, v39\n"
"v_mfma_f32_16x16x32_bf16 a[168:171], a[232:235], v[152:155], a[168:171]\n"
"v_cvt_pk_bf16_f32 v191, v38, v39\n"
"s_add_u32 s6, s6, s7\n"
"v_mfma_f32_16x16x32_bf16 a[172:175], a[232:235], v[156:159], a[172:175]\n"
"v_mfma_f32_16x16x32_bf16 a[188:191], a[236:239], v[156:159], a[188:191]\n"
"v_max3_f32 v28, v24, v25, v26\n"
"v_mfma_f32_16x16x32_bf16 a[184:187], a[236:239], v[152:155], a[184:187]\n"
"v_max_f32 v28, v28, v27\n"
"buffer_load_dwordx4 v222, s[52:55], s19 offen offset:2048 nt lds\n"
"v_mfma_f32_16x16x32_bf16 a[180:183], a[236:239], v[148:151], a[180:183]\n"
"v_cmp_lt_f32 vcc, s27, v28\n"
"v_mfma_f32_16x16x32_bf16 a[176:179], a[236:239], v[144:147], a[176:179]\n"
"s_waitcnt lgkmcnt(0)\n"
"s_cbranch_vccnz .Lmha_rare_l0\n"
".Lmha_ret_l0:\n"
"s_cmp_lt_u32 s34, 4\n"
"s_cbranch_scc1 .Lmha_loop\n"
"s_cmp_eq_u32 s34, 4\n"
"s_cbranch_scc0 .Lmha_tail\n"
".Lmha_sw:\n"
"s_mov_b64 s[48:49], s[60:61]\n"
"s_mov_b64 s[50:51], s[62:63]\n"
"s_mov_b64 s[52:53], s[60:61]\n"
"s_mov_b64 s[54:55], s[62:63]\n"
"v_mov_b32 v252, v56\n"
"v_mov_b32 v253, v57\n"
"v_mov_b32 v254, v58\n"
"v_mov_b32 v220, v56\n"
"v_mov_b32 v221, v57\n"
"v_mov_b32 v222, v58\n"
"s_add_u32 s11, s84, 4294934528\n"
"s_add_u32 s12, s84, 8192\n"
"s_add_u32 s18, s84, 49152\n"
"s_add_u32 s13, s84, 90112\n"
"s_add_u32 s19, s84, 131072\n"
"s_sub_u32 s7, s7, 4\n"
"s_branch .Lmha_loop\n"
".Lmha_slp0:\n"
"s_sleep 8\n"
"s_branch .Lmha_wk0\n"
".Lmha_slp1:\n"
"s_sleep 8\n"
"s_branch .Lmha_wk1\n"
".Lmha_slp2:\n"
"s_sleep 8\n"
"s_branch .Lmha_wk2\n"
".Lmha_slp3:\n"
"s_sleep 8\n"
"s_branch .Lmha_wk3\n"
".Lmha_slp4:\n"
"s_sleep 8\n"
"s_branch .Lmha_wk4\n"
".Lmha_slp5:\n"
"s_sleep 8\n"
"s_branch .Lmha_wk5\n"
".Lmha_tail:\n"
"ds_read_b64_tr_b16 a[192:193], v4 offset:24576\n"
"ds_read_b64_tr_b16 a[194:195], v4 offset:30720\n"
"ds_read_b64_tr_b16 a[196:197], v5 offset:24576\n"
"ds_read_b64_tr_b16 a[198:199], v5 offset:30720\n"
"ds_read_b64_tr_b16 a[200:201], v6 offset:24576\n"
"ds_read_b64_tr_b16 a[202:203], v6 offset:30720\n"
"ds_read_b64_tr_b16 a[204:205], v7 offset:24576\n"
"ds_read_b64_tr_b16 a[206:207], v7 offset:30720\n"
"ds_read_b64_tr_b16 a[208:209], v4 offset:24704\n"
"ds_read_b64_tr_b16 a[210:211], v4 offset:30848\n"
"ds_read_b64_tr_b16 a[212:213], v5 offset:24704\n"
"ds_read_b64_tr_b16 a[214:215], v5 offset:30848\n"
"s_add_u32 s24, s85, 0\n"
"s_add_u32 m0, s76, 61440\n"
"s_nop 0\n"
"buffer_load_dwordx4 v56, s[60:63], s24 offen lds\n"
"buffer_load_dwordx4 v57, s[60:63], s24 offen offset:1024 lds\n"
"buffer_load_dwordx4 v58, s[60:63], s24 offen offset:2048 lds\n"
"s_add_u32 s24, s86, 0\n"
"s_add_u32 m0, s76, 0\n"
"s_nop 0\n"
"buffer_load_dwordx4 v56, s[60:63], s24 offen lds\n"
"buffer_load_dwordx4 v57, s[60:63], s24 offen offset:1024 lds\n"
"buffer_load_dwordx4 v58, s[60:63], s24 offen offset:2048 lds\n"
"v_add_f32 v20, v20, v24\n"
"v_add_f32 v21, v21, v25\n"
"v_add_f32 v22, v22, v26\n"
"v_add_f32 v23, v23, v27\n"
"v_mov_b32 v28, v20\n"
"v_mov_b32 v29, v21\n"
"v_mov_b32 v30, v22\n"
"v_mov_b32 v31, v23\n"
"v_permlane32_swap_b32 v20, v28\n"
"v_permlane32_swap_b32 v21, v29\n"
"v_permlane32_swap_b32 v22, v30\n"
"v_permlane32_swap_b32 v23, v31\n"
"v_add_f32 v20, v20, v28\n"
"v_add_f32 v21, v21, v29\n"
"v_add_f32 v22, v22, v30\n"
"v_add_f32 v23, v23, v31\n"
"s_waitcnt lgkmcnt(8)\n"
"v_mfma_f32_16x16x32_bf16 a[0:3], a[192:195], v[176:179], a[0:3]\n"
"ds_read_b64_tr_b16 a[216:217], v6 offset:24704\n"
"ds_read_b64_tr_b16 a[218:219], v6 offset:30848\n"
"v_mov_b32 v28, v20\n"
"v_mov_b32 v29, v21\n"
"v_mov_b32 v30, v22\n"
"v_mfma_f32_16x16x32_bf16 a[4:7], a[192:195], v[180:183], a[4:7]\n"
"ds_read_b64_tr_b16 a[220:221], v7 offset:24704\n"
"ds_read_b64_tr_b16 a[222:223], v7 offset:30848\n"
"v_mov_b32 v31, v23\n"
"v_permlane16_swap_b32 v20, v28\n"
"v_permlane16_swap_b32 v21, v29\n"
"v_mfma_f32_16x16x32_bf16 a[8:11], a[192:195], v[184:187], a[8:11]\n"
"ds_read_b64_tr_b16 a[224:225], v4 offset:24832\n"
"ds_read_b64_tr_b16 a[226:227], v4 offset:30976\n"
"v_permlane16_swap_b32 v22, v30\n"
"v_permlane16_swap_b32 v23, v31\n"
"v_add_f32 v20, v20, v28\n"
"v_mfma_f32_16x16x32_bf16 a[12:15], a[192:195], v[188:191], a[12:15]\n"
"ds_read_b64_tr_b16 a[228:229], v5 offset:24832\n"
"ds_read_b64_tr_b16 a[230:231], v5 offset:30976\n"
"v_add_f32 v21, v21, v29\n"
"v_add_f32 v22, v22, v30\n"
"v_add_f32 v23, v23, v31\n"
"v_mfma_f32_16x16x32_bf16 a[28:31], a[196:199], v[188:191], a[28:31]\n"
"ds_read_b64_tr_b16 a[232:233], v6 offset:24832\n"
"ds_read_b64_tr_b16 a[234:235], v6 offset:30976\n"
"v_rcp_f32 v20, v20\n"
"v_mfma_f32_16x16x32_bf16 a[24:27], a[196:199], v[184:187], a[24:27]\n"
"ds_read_b64_tr_b16 a[236:237], v7 offset:24832\n"
"ds_read_b64_tr_b16 a[238:239], v7 offset:30976\n"
"v_rcp_f32 v12, v21\n"
"v_mfma_f32_16x16x32_bf16 a[20:23], a[196:199], v[180:183], a[20:23]\n"
"v_rcp_f32 v22, v22\n"
"v_rcp_f32 v14, v23\n"
"v_subrev_u32 v0, 61440, v4\n"
"v_mfma_f32_16x16x32_bf16 a[16:19], a[196:199], v[176:179], a[16:19]\n"
"s_waitcnt lgkmcnt(12)\n"
"v_subrev_u32 v1, 61440, v5\n"
"v_subrev_u32 v2, 61440, v6\n"
"v_subrev_u32 v3, 61440, v7\n"
"v_mfma_f32_16x16x32_bf16 a[32:35], a[200:203], v[176:179], a[32:35]\n"
"s_add_u32 s25, s80, 0\n"
"v_accvgpr_read_b32 v32, a12\n"
"v_accvgpr_read_b32 v33, a13\n"
"v_accvgpr_read_b32 v34, a14\n"
"v_accvgpr_read_b32 v35, a15\n"
"v_mfma_f32_16x16x32_bf16 a[36:39], a[200:203], v[180:183], a[36:39]\n"
"v_accvgpr_read_b32 v36, a28\n"
"v_accvgpr_read_b32 v37, a29\n"
"v_accvgpr_read_b32 v38, a30\n"
"v_accvgpr_read_b32 v39, a31\n"
"v_pk_mul_f32 v[32:33], v[32:33], v[14:15] op_sel_hi:[1,0]\n"
"v_pk_mul_f32 v[34:35], v[34:35], v[14:15] op_sel_hi:[1,0]\n"
"v_pk_mul_f32 v[36:37], v[36:37], v[14:15] op_sel_hi:[1,0]\n"
"v_pk_mul_f32 v[38:39], v[38:39], v[14:15] op_sel_hi:[1,0]\n"
"v_cvt_pk_bf16_f32 v236, v32, v33\n"
"v_cvt_pk_bf16_f32 v237, v34, v35\n"
"v_cvt_pk_bf16_f32 v238, v36, v37\n"
"v_cvt_pk_bf16_f32 v239, v38, v39\n"
"buffer_store_dwordx4 v[236:239], v18, s[56:59], s25 offen offset:3072 sc1\n"
"v_mfma_f32_16x16x32_bf16 a[40:43], a[200:203], v[184:187], a[40:43]\n"
"v_accvgpr_read_b32 v24, a8\n"
"v_accvgpr_read_b32 v25, a9\n"
"v_accvgpr_read_b32 v26, a10\n"
"v_accvgpr_read_b32 v27, a11\n"
"v_mfma_f32_16x16x32_bf16 a[44:47], a[200:203], v[188:191], a[44:47]\n"
"v_accvgpr_read_b32 v28, a24\n"
"v_accvgpr_read_b32 v29, a25\n"
"v_accvgpr_read_b32 v30, a26\n"
"v_accvgpr_read_b32 v31, a27\n"
"v_pk_mul_f32 v[24:25], v[24:25], v[22:23] op_sel_hi:[1,0]\n"
"v_pk_mul_f32 v[26:27], v[26:27], v[22:23] op_sel_hi:[1,0]\n"
"v_pk_mul_f32 v[28:29], v[28:29], v[22:23] op_sel_hi:[1,0]\n"
"v_pk_mul_f32 v[30:31], v[30:31], v[22:23] op_sel_hi:[1,0]\n"
"v_cvt_pk_bf16_f32 v232, v24, v25\n"
"v_cvt_pk_bf16_f32 v233, v26, v27\n"
"v_cvt_pk_bf16_f32 v234, v28, v29\n"
"v_cvt_pk_bf16_f32 v235, v30, v31\n"
"buffer_store_dwordx4 v[232:235], v18, s[56:59], s25 offen offset:2048 sc1\n"
"v_mfma_f32_16x16x32_bf16 a[60:63], a[204:207], v[188:191], a[60:63]\n"
"v_accvgpr_read_b32 v32, a4\n"
"v_accvgpr_read_b32 v33, a5\n"
"v_accvgpr_read_b32 v34, a6\n"
"v_accvgpr_read_b32 v35, a7\n"
"v_mfma_f32_16x16x32_bf16 a[56:59], a[204:207], v[184:187], a[56:59]\n"
"v_accvgpr_read_b32 v36, a20\n"
"v_accvgpr_read_b32 v37, a21\n"
"v_accvgpr_read_b32 v38, a22\n"
"v_accvgpr_read_b32 v39, a23\n"
"v_pk_mul_f32 v[32:33], v[32:33], v[12:13] op_sel_hi:[1,0]\n"
"v_pk_mul_f32 v[34:35], v[34:35], v[12:13] op_sel_hi:[1,0]\n"
"v_pk_mul_f32 v[36:37], v[36:37], v[12:13] op_sel_hi:[1,0]\n"
"v_pk_mul_f32 v[38:39], v[38:39], v[12:13] op_sel_hi:[1,0]\n"
"v_cvt_pk_bf16_f32 v228, v32, v33\n"
"v_cvt_pk_bf16_f32 v229, v34, v35\n"
"v_cvt_pk_bf16_f32 v230, v36, v37\n"
"v_cvt_pk_bf16_f32 v231, v38, v39\n"
"buffer_store_dwordx4 v[228:231], v18, s[56:59], s25 offen offset:1024 sc1\n"
"v_mfma_f32_16x16x32_bf16 a[52:55], a[204:207], v[180:183], a[52:55]\n"
"v_accvgpr_read_b32 v24, a0\n"
"v_accvgpr_read_b32 v25, a1\n"
"v_accvgpr_read_b32 v26, a2\n"
"v_accvgpr_read_b32 v27, a3\n"
"v_mfma_f32_16x16x32_bf16 a[48:51], a[204:207], v[176:179], a[48:51]\n"
"v_accvgpr_read_b32 v28, a16\n"
"v_accvgpr_read_b32 v29, a17\n"
"v_accvgpr_read_b32 v30, a18\n"
"v_accvgpr_read_b32 v31, a19\n"
"v_pk_mul_f32 v[24:25], v[24:25], v[20:21] op_sel_hi:[1,0]\n"
"v_pk_mul_f32 v[26:27], v[26:27], v[20:21] op_sel_hi:[1,0]\n"
"v_pk_mul_f32 v[28:29], v[28:29], v[20:21] op_sel_hi:[1,0]\n"
"v_pk_mul_f32 v[30:31], v[30:31], v[20:21] op_sel_hi:[1,0]\n"
"v_cvt_pk_bf16_f32 v224, v24, v25\n"
"v_cvt_pk_bf16_f32 v225, v26, v27\n"
"v_cvt_pk_bf16_f32 v226, v28, v29\n"
"v_cvt_pk_bf16_f32 v227, v30, v31\n"
"buffer_store_dwordx4 v[224:227], v18, s[56:59], s25 offen sc1\n"
"v_mfma_f32_16x16x32_bf16 a[64:67], a[208:211], v[176:179], a[64:67]\n"
"s_add_u32 s25, s80, 4096\n"
"v_accvgpr_read_b32 v32, a44\n"
"v_accvgpr_read_b32 v33, a45\n"
"v_accvgpr_read_b32 v34, a46\n"
"v_accvgpr_read_b32 v35, a47\n"
"v_mfma_f32_16x16x32_bf16 a[68:71], a[208:211], v[180:183], a[68:71]\n"
"v_accvgpr_read_b32 v36, a60\n"
"v_accvgpr_read_b32 v37, a61\n"
"v_accvgpr_read_b32 v38, a62\n"
"v_accvgpr_read_b32 v39, a63\n"
"v_pk_mul_f32 v[32:33], v[32:33], v[14:15] op_sel_hi:[1,0]\n"
"v_pk_mul_f32 v[34:35], v[34:35], v[14:15] op_sel_hi:[1,0]\n"
"v_pk_mul_f32 v[36:37], v[36:37], v[14:15] op_sel_hi:[1,0]\n"
"v_pk_mul_f32 v[38:39], v[38:39], v[14:15] op_sel_hi:[1,0]\n"
"v_cvt_pk_bf16_f32 v252, v32, v33\n"
"v_cvt_pk_bf16_f32 v253, v34, v35\n"
"v_cvt_pk_bf16_f32 v254, v36, v37\n"
"v_cvt_pk_bf16_f32 v255, v38, v39\n"
"buffer_store_dwordx4 v[252:255], v18, s[56:59], s25 offen offset:3072 sc1\n"
"v_mfma_f32_16x16x32_bf16 a[72:75], a[208:211], v[184:187], a[72:75]\n"
"v_accvgpr_read_b32 v24, a40\n"
"v_accvgpr_read_b32 v25, a41\n"
"v_accvgpr_read_b32 v26, a42\n"
"v_accvgpr_read_b32 v27, a43\n"
"v_mfma_f32_16x16x32_bf16 a[76:79], a[208:211], v[188:191], a[76:79]\n"
"v_accvgpr_read_b32 v28, a56\n"
"v_accvgpr_read_b32 v29, a57\n"
"v_accvgpr_read_b32 v30, a58\n"
"v_accvgpr_read_b32 v31, a59\n"
"v_pk_mul_f32 v[24:25], v[24:25], v[22:23] op_sel_hi:[1,0]\n"
"v_pk_mul_f32 v[26:27], v[26:27], v[22:23] op_sel_hi:[1,0]\n"
"v_pk_mul_f32 v[28:29], v[28:29], v[22:23] op_sel_hi:[1,0]\n"
"v_pk_mul_f32 v[30:31], v[30:31], v[22:23] op_sel_hi:[1,0]\n"
"v_cvt_pk_bf16_f32 v248, v24, v25\n"
"v_cvt_pk_bf16_f32 v249, v26, v27\n"
"v_cvt_pk_bf16_f32 v250, v28, v29\n"
"v_cvt_pk_bf16_f32 v251, v30, v31\n"
"buffer_store_dwordx4 v[248:251], v18, s[56:59], s25 offen offset:2048 sc1\n"
"v_mfma_f32_16x16x32_bf16 a[92:95], a[212:215], v[188:191], a[92:95]\n"
"v_accvgpr_read_b32 v32, a36\n"
"v_accvgpr_read_b32 v33, a37\n"
"v_accvgpr_read_b32 v34, a38\n"
"v_accvgpr_read_b32 v35, a39\n"
"v_mfma_f32_16x16x32_bf16 a[88:91], a[212:215], v[184:187], a[88:91]\n"
"v_accvgpr_read_b32 v36, a52\n"
"v_accvgpr_read_b32 v37, a53\n"
"v_accvgpr_read_b32 v38, a54\n"
"v_accvgpr_read_b32 v39, a55\n"
"v_pk_mul_f32 v[32:33], v[32:33], v[12:13] op_sel_hi:[1,0]\n"
"v_pk_mul_f32 v[34:35], v[34:35], v[12:13] op_sel_hi:[1,0]\n"
"v_pk_mul_f32 v[36:37], v[36:37], v[12:13] op_sel_hi:[1,0]\n"
"v_pk_mul_f32 v[38:39], v[38:39], v[12:13] op_sel_hi:[1,0]\n"
"v_cvt_pk_bf16_f32 v244, v32, v33\n"
"v_cvt_pk_bf16_f32 v245, v34, v35\n"
"v_cvt_pk_bf16_f32 v246, v36, v37\n"
"v_cvt_pk_bf16_f32 v247, v38, v39\n"
"buffer_store_dwordx4 v[244:247], v18, s[56:59], s25 offen offset:1024 sc1\n"
"v_mfma_f32_16x16x32_bf16 a[84:87], a[212:215], v[180:183], a[84:87]\n"
"v_accvgpr_read_b32 v24, a32\n"
"v_accvgpr_read_b32 v25, a33\n"
"v_accvgpr_read_b32 v26, a34\n"
"v_accvgpr_read_b32 v27, a35\n"
"v_mfma_f32_16x16x32_bf16 a[80:83], a[212:215], v[176:179], a[80:83]\n"
"s_waitcnt vmcnt(13) lgkmcnt(0)\n"
"s_barrier\n"
"v_accvgpr_read_b32 v28, a48\n"
"v_accvgpr_read_b32 v29, a49\n"
"v_accvgpr_read_b32 v30, a50\n"
"v_accvgpr_read_b32 v31, a51\n"
"v_pk_mul_f32 v[24:25], v[24:25], v[20:21] op_sel_hi:[1,0]\n"
"v_pk_mul_f32 v[26:27], v[26:27], v[20:21] op_sel_hi:[1,0]\n"
"v_pk_mul_f32 v[28:29], v[28:29], v[20:21] op_sel_hi:[1,0]\n"
"v_pk_mul_f32 v[30:31], v[30:31], v[20:21] op_sel_hi:[1,0]\n"
"v_cvt_pk_bf16_f32 v240, v24, v25\n"
"v_cvt_pk_bf16_f32 v241, v26, v27\n"
"v_cvt_pk_bf16_f32 v242, v28, v29\n"
"v_cvt_pk_bf16_f32 v243, v30, v31\n"
"buffer_store_dwordx4 v[240:243], v18, s[56:59], s25 offen sc1\n"
"v_mfma_f32_16x16x32_bf16 a[96:99], a[216:219], v[176:179], a[96:99]\n"
"ds_read_b64_tr_b16 a[192:193], v8 offset:36864\n"
"ds_read_b64_tr_b16 a[194:195], v8 offset:43008\n"
"s_add_u32 s25, s80, 8192\n"
"v_accvgpr_read_b32 v32, a76\n"
"v_accvgpr_read_b32 v33, a77\n"
"v_accvgpr_read_b32 v34, a78\n"
"v_accvgpr_read_b32 v35, a79\n"
"v_mfma_f32_16x16x32_bf16 a[100:103], a[216:219], v[180:183], a[100:103]\n"
"ds_read_b64_tr_b16 a[196:197], v9 offset:36864\n"
"ds_read_b64_tr_b16 a[198:199], v9 offset:43008\n"
"v_accvgpr_read_b32 v36, a92\n"
"v_accvgpr_read_b32 v37, a93\n"
"v_accvgpr_read_b32 v38, a94\n"
"v_accvgpr_read_b32 v39, a95\n"
"v_pk_mul_f32 v[32:33], v[32:33], v[14:15] op_sel_hi:[1,0]\n"
"v_pk_mul_f32 v[34:35], v[34:35], v[14:15] op_sel_hi:[1,0]\n"
"v_pk_mul_f32 v[36:37], v[36:37], v[14:15] op_sel_hi:[1,0]\n"
"v_pk_mul_f32 v[38:39], v[38:39], v[14:15] op_sel_hi:[1,0]\n"
"v_cvt_pk_bf16_f32 v52, v32, v33\n"
"v_cvt_pk_bf16_f32 v53, v34, v35\n"
"v_cvt_pk_bf16_f32 v54, v36, v37\n"
"v_cvt_pk_bf16_f32 v55, v38, v39\n"
"buffer_store_dwordx4 v[52:55], v18, s[56:59], s25 offen offset:3072 sc1\n"
"v_mfma_f32_16x16x32_bf16 a[104:107], a[216:219], v[184:187], a[104:107]\n"
"ds_read_b64_tr_b16 a[200:201], v10 offset:36864\n"
"ds_read_b64_tr_b16 a[202:203], v10 offset:43008\n"
"v_accvgpr_read_b32 v24, a72\n"
"v_accvgpr_read_b32 v25, a73\n"
"v_accvgpr_read_b32 v26, a74\n"
"v_accvgpr_read_b32 v27, a75\n"
"v_mfma_f32_16x16x32_bf16 a[108:111], a[216:219], v[188:191], a[108:111]\n"
"v_accvgpr_read_b32 v28, a88\n"
"v_accvgpr_read_b32 v29, a89\n"
"v_accvgpr_read_b32 v30, a90\n"
"v_accvgpr_read_b32 v31, a91\n"
"v_pk_mul_f32 v[24:25], v[24:25], v[22:23] op_sel_hi:[1,0]\n"
"v_pk_mul_f32 v[26:27], v[26:27], v[22:23] op_sel_hi:[1,0]\n"
"v_pk_mul_f32 v[28:29], v[28:29], v[22:23] op_sel_hi:[1,0]\n"
"v_pk_mul_f32 v[30:31], v[30:31], v[22:23] op_sel_hi:[1,0]\n"
"v_cvt_pk_bf16_f32 v48, v24, v25\n"
"v_cvt_pk_bf16_f32 v49, v26, v27\n"
"v_cvt_pk_bf16_f32 v50, v28, v29\n"
"v_cvt_pk_bf16_f32 v51, v30, v31\n"
"buffer_store_dwordx4 v[48:51], v18, s[56:59], s25 offen offset:2048 sc1\n"
"v_mfma_f32_16x16x32_bf16 a[124:127], a[220:223], v[188:191], a[124:127]\n"
"ds_read_b64_tr_b16 a[204:205], v11 offset:36864\n"
"ds_read_b64_tr_b16 a[206:207], v11 offset:43008\n"
"v_accvgpr_read_b32 v32, a68\n"
"v_accvgpr_read_b32 v33, a69\n"
"v_accvgpr_read_b32 v34, a70\n"
"v_accvgpr_read_b32 v35, a71\n"
"v_mfma_f32_16x16x32_bf16 a[120:123], a[220:223], v[184:187], a[120:123]\n"
"v_accvgpr_read_b32 v36, a84\n"
"v_accvgpr_read_b32 v37, a85\n"
"v_accvgpr_read_b32 v38, a86\n"
"v_accvgpr_read_b32 v39, a87\n"
"v_pk_mul_f32 v[32:33], v[32:33], v[12:13] op_sel_hi:[1,0]\n"
"v_pk_mul_f32 v[34:35], v[34:35], v[12:13] op_sel_hi:[1,0]\n"
"v_pk_mul_f32 v[36:37], v[36:37], v[12:13] op_sel_hi:[1,0]\n"
"v_pk_mul_f32 v[38:39], v[38:39], v[12:13] op_sel_hi:[1,0]\n"
"v_cvt_pk_bf16_f32 v44, v32, v33\n"
"v_cvt_pk_bf16_f32 v45, v34, v35\n"
"v_cvt_pk_bf16_f32 v46, v36, v37\n"
"v_cvt_pk_bf16_f32 v47, v38, v39\n"
"buffer_store_dwordx4 v[44:47], v18, s[56:59], s25 offen offset:1024 sc1\n"
"v_mfma_f32_16x16x32_bf16 a[116:119], a[220:223], v[180:183], a[116:119]\n"
"v_accvgpr_read_b32 v24, a64\n"
"v_accvgpr_read_b32 v25, a65\n"
"v_accvgpr_read_b32 v26, a66\n"
"v_accvgpr_read_b32 v27, a67\n"
"v_mfma_f32_16x16x32_bf16 a[112:115], a[220:223], v[176:179], a[112:115]\n"
"v_accvgpr_read_b32 v28, a80\n"
"v_accvgpr_read_b32 v29, a81\n"
"v_accvgpr_read_b32 v30, a82\n"
"v_accvgpr_read_b32 v31, a83\n"
"v_pk_mul_f32 v[24:25], v[24:25], v[20:21] op_sel_hi:[1,0]\n"
"v_pk_mul_f32 v[26:27], v[26:27], v[20:21] op_sel_hi:[1,0]\n"
"v_pk_mul_f32 v[28:29], v[28:29], v[20:21] op_sel_hi:[1,0]\n"
"v_pk_mul_f32 v[30:31], v[30:31], v[20:21] op_sel_hi:[1,0]\n"
"v_cvt_pk_bf16_f32 v40, v24, v25\n"
"v_cvt_pk_bf16_f32 v41, v26, v27\n"
"v_cvt_pk_bf16_f32 v42, v28, v29\n"
"v_cvt_pk_bf16_f32 v43, v30, v31\n"
"buffer_store_dwordx4 v[40:43], v18, s[56:59], s25 offen sc1\n"
"v_mfma_f32_16x16x32_bf16 a[128:131], a[224:227], v[176:179], a[128:131]\n"
"ds_read_b64_tr_b16 a[208:209], v8 offset:36992\n"
"ds_read_b64_tr_b16 a[210:211], v8 offset:43136\n"
"s_add_u32 s25, s80, 12288\n"
"v_accvgpr_read_b32 v32, a108\n"
"v_accvgpr_read_b32 v33, a109\n"
"v_accvgpr_read_b32 v34, a110\n"
"v_accvgpr_read_b32 v35, a111\n"
"v_mfma_f32_16x16x32_bf16 a[132:135], a[224:227], v[180:183], a[132:135]\n"
"v_accvgpr_read_b32 v36, a124\n"
"v_accvgpr_read_b32 v37, a125\n"
"v_accvgpr_read_b32 v38, a126\n"
"v_accvgpr_read_b32 v39, a127\n"
"v_pk_mul_f32 v[32:33], v[32:33], v[14:15] op_sel_hi:[1,0]\n"
"v_pk_mul_f32 v[34:35], v[34:35], v[14:15] op_sel_hi:[1,0]\n"
"v_pk_mul_f32 v[36:37], v[36:37], v[14:15] op_sel_hi:[1,0]\n"
"v_pk_mul_f32 v[38:39], v[38:39], v[14:15] op_sel_hi:[1,0]\n"
"v_cvt_pk_bf16_f32 v172, v32, v33\n"
"v_cvt_pk_bf16_f32 v173, v34, v35\n"
"v_cvt_pk_bf16_f32 v174, v36, v37\n"
"v_cvt_pk_bf16_f32 v175, v38, v39\n"
"buffer_store_dwordx4 v[172:175], v18, s[56:59], s25 offen offset:3072 sc1\n"
"v_mfma_f32_16x16x32_bf16 a[136:139], a[224:227], v[184:187], a[136:139]\n"
"v_accvgpr_read_b32 v24, a104\n"
"v_accvgpr_read_b32 v25, a105\n"
"v_accvgpr_read_b32 v26, a106\n"
"v_accvgpr_read_b32 v27, a107\n"
"v_mfma_f32_16x16x32_bf16 a[140:143], a[224:227], v[188:191], a[140:143]\n"
"v_accvgpr_read_b32 v28, a120\n"
"v_accvgpr_read_b32 v29, a121\n"
"v_accvgpr_read_b32 v30, a122\n"
"v_accvgpr_read_b32 v31, a123\n"
"v_pk_mul_f32 v[24:25], v[24:25], v[22:23] op_sel_hi:[1,0]\n"
"v_pk_mul_f32 v[26:27], v[26:27], v[22:23] op_sel_hi:[1,0]\n"
"v_pk_mul_f32 v[28:29], v[28:29], v[22:23] op_sel_hi:[1,0]\n"
"v_pk_mul_f32 v[30:31], v[30:31], v[22:23] op_sel_hi:[1,0]\n"
"v_cvt_pk_bf16_f32 v168, v24, v25\n"
"v_cvt_pk_bf16_f32 v169, v26, v27\n"
"v_cvt_pk_bf16_f32 v170, v28, v29\n"
"v_cvt_pk_bf16_f32 v171, v30, v31\n"
"buffer_store_dwordx4 v[168:171], v18, s[56:59], s25 offen offset:2048 sc1\n"
"v_mfma_f32_16x16x32_bf16 a[156:159], a[228:231], v[188:191], a[156:159]\n"
"ds_read_b64_tr_b16 a[212:213], v9 offset:36992\n"
"ds_read_b64_tr_b16 a[214:215], v9 offset:43136\n"
"v_accvgpr_read_b32 v32, a100\n"
"v_accvgpr_read_b32 v33, a101\n"
"v_accvgpr_read_b32 v34, a102\n"
"v_accvgpr_read_b32 v35, a103\n"
"v_mfma_f32_16x16x32_bf16 a[152:155], a[228:231], v[184:187], a[152:155]\n"
"v_accvgpr_read_b32 v36, a116\n"
"v_accvgpr_read_b32 v37, a117\n"
"v_accvgpr_read_b32 v38, a118\n"
"v_accvgpr_read_b32 v39, a119\n"
"v_pk_mul_f32 v[32:33], v[32:33], v[12:13] op_sel_hi:[1,0]\n"
"v_pk_mul_f32 v[34:35], v[34:35], v[12:13] op_sel_hi:[1,0]\n"
"v_pk_mul_f32 v[36:37], v[36:37], v[12:13] op_sel_hi:[1,0]\n"
"v_pk_mul_f32 v[38:39], v[38:39], v[12:13] op_sel_hi:[1,0]\n"
"v_cvt_pk_bf16_f32 v164, v32, v33\n"
"v_cvt_pk_bf16_f32 v165, v34, v35\n"
"v_cvt_pk_bf16_f32 v166, v36, v37\n"
"v_cvt_pk_bf16_f32 v167, v38, v39\n"
"buffer_store_dwordx4 v[164:167], v18, s[56:59], s25 offen offset:1024 sc1\n"
"v_mfma_f32_16x16x32_bf16 a[148:151], a[228:231], v[180:183], a[148:151]\n"
"v_accvgpr_read_b32 v24, a96\n"
"v_accvgpr_read_b32 v25, a97\n"
"v_accvgpr_read_b32 v26, a98\n"
"v_accvgpr_read_b32 v27, a99\n"
"v_mfma_f32_16x16x32_bf16 a[144:147], a[228:231], v[176:179], a[144:147]\n"
"v_accvgpr_read_b32 v28, a112\n"
"v_accvgpr_read_b32 v29, a113\n"
"v_accvgpr_read_b32 v30, a114\n"
"v_accvgpr_read_b32 v31, a115\n"
"v_pk_mul_f32 v[24:25], v[24:25], v[20:21] op_sel_hi:[1,0]\n"
"v_pk_mul_f32 v[26:27], v[26:27], v[20:21] op_sel_hi:[1,0]\n"
"v_pk_mul_f32 v[28:29], v[28:29], v[20:21] op_sel_hi:[1,0]\n"
"v_pk_mul_f32 v[30:31], v[30:31], v[20:21] op_sel_hi:[1,0]\n"
"v_cvt_pk_bf16_f32 v160, v24, v25\n"
"v_cvt_pk_bf16_f32 v161, v26, v27\n"
"v_cvt_pk_bf16_f32 v162, v28, v29\n"
"v_cvt_pk_bf16_f32 v163, v30, v31\n"
"buffer_store_dwordx4 v[160:163], v18, s[56:59], s25 offen sc1\n"
"v_mfma_f32_16x16x32_bf16 a[160:163], a[232:235], v[176:179], a[160:163]\n"
"ds_read_b64_tr_b16 a[216:217], v10 offset:36992\n"
"ds_read_b64_tr_b16 a[218:219], v10 offset:43136\n"
"s_add_u32 s25, s80, 16384\n"
"v_accvgpr_read_b32 v32, a140\n"
"v_accvgpr_read_b32 v33, a141\n"
"v_accvgpr_read_b32 v34, a142\n"
"v_accvgpr_read_b32 v35, a143\n"
"v_mfma_f32_16x16x32_bf16 a[164:167], a[232:235], v[180:183], a[164:167]\n"
"v_accvgpr_read_b32 v36, a156\n"
"v_accvgpr_read_b32 v37, a157\n"
"v_accvgpr_read_b32 v38, a158\n"
"v_accvgpr_read_b32 v39, a159\n"
"v_pk_mul_f32 v[32:33], v[32:33], v[14:15] op_sel_hi:[1,0]\n"
"v_pk_mul_f32 v[34:35], v[34:35], v[14:15] op_sel_hi:[1,0]\n"
"v_pk_mul_f32 v[36:37], v[36:37], v[14:15] op_sel_hi:[1,0]\n"
"v_pk_mul_f32 v[38:39], v[38:39], v[14:15] op_sel_hi:[1,0]\n"
"v_cvt_pk_bf16_f32 v204, v32, v33\n"
"v_cvt_pk_bf16_f32 v205, v34, v35\n"
"v_cvt_pk_bf16_f32 v206, v36, v37\n"
"v_cvt_pk_bf16_f32 v207, v38, v39\n"
"buffer_store_dwordx4 v[204:207], v18, s[56:59], s25 offen offset:3072 sc1\n"
"v_mfma_f32_16x16x32_bf16 a[168:171], a[232:235], v[184:187], a[168:171]\n"
"v_accvgpr_read_b32 v24, a136\n"
"v_accvgpr_read_b32 v25, a137\n"
"v_accvgpr_read_b32 v26, a138\n"
"v_accvgpr_read_b32 v27, a139\n"
"v_mfma_f32_16x16x32_bf16 a[172:175], a[232:235], v[188:191], a[172:175]\n"
"v_accvgpr_read_b32 v28, a152\n"
"v_accvgpr_read_b32 v29, a153\n"
"v_accvgpr_read_b32 v30, a154\n"
"v_accvgpr_read_b32 v31, a155\n"
"v_pk_mul_f32 v[24:25], v[24:25], v[22:23] op_sel_hi:[1,0]\n"
"v_pk_mul_f32 v[26:27], v[26:27], v[22:23] op_sel_hi:[1,0]\n"
"v_pk_mul_f32 v[28:29], v[28:29], v[22:23] op_sel_hi:[1,0]\n"
"v_pk_mul_f32 v[30:31], v[30:31], v[22:23] op_sel_hi:[1,0]\n"
"v_cvt_pk_bf16_f32 v200, v24, v25\n"
"v_cvt_pk_bf16_f32 v201, v26, v27\n"
"v_cvt_pk_bf16_f32 v202, v28, v29\n"
"v_cvt_pk_bf16_f32 v203, v30, v31\n"
"buffer_store_dwordx4 v[200:203], v18, s[56:59], s25 offen offset:2048 sc1\n"
"v_mfma_f32_16x16x32_bf16 a[188:191], a[236:239], v[188:191], a[188:191]\n"
"ds_read_b64_tr_b16 a[220:221], v11 offset:36992\n"
"ds_read_b64_tr_b16 a[222:223], v11 offset:43136\n"
"v_accvgpr_read_b32 v32, a132\n"
"v_accvgpr_read_b32 v33, a133\n"
"v_accvgpr_read_b32 v34, a134\n"
"v_accvgpr_read_b32 v35, a135\n"
"v_mfma_f32_16x16x32_bf16 a[184:187], a[236:239], v[184:187], a[184:187]\n"
"v_accvgpr_read_b32 v36, a148\n"
"v_accvgpr_read_b32 v37, a149\n"
"v_accvgpr_read_b32 v38, a150\n"
"v_accvgpr_read_b32 v39, a151\n"
"v_pk_mul_f32 v[32:33], v[32:33], v[12:13] op_sel_hi:[1,0]\n"
"v_pk_mul_f32 v[34:35], v[34:35], v[12:13] op_sel_hi:[1,0]\n"
"v_pk_mul_f32 v[36:37], v[36:37], v[12:13] op_sel_hi:[1,0]\n"
"v_pk_mul_f32 v[38:39], v[38:39], v[12:13] op_sel_hi:[1,0]\n"
"v_cvt_pk_bf16_f32 v196, v32, v33\n"
"v_cvt_pk_bf16_f32 v197, v34, v35\n"
"v_cvt_pk_bf16_f32 v198, v36, v37\n"
"v_cvt_pk_bf16_f32 v199, v38, v39\n"
"buffer_store_dwordx4 v[196:199], v18, s[56:59], s25 offen offset:1024 sc1\n"
"v_mfma_f32_16x16x32_bf16 a[180:183], a[236:239], v[180:183], a[180:183]\n"
"v_accvgpr_read_b32 v24, a128\n"
"v_accvgpr_read_b32 v25, a129\n"
"v_accvgpr_read_b32 v26, a130\n"
"v_accvgpr_read_b32 v27, a131\n"
"v_mfma_f32_16x16x32_bf16 a[176:179], a[236:239], v[176:179], a[176:179]\n"
"s_waitcnt lgkmcnt(8)\n"
"v_accvgpr_read_b32 v28, a144\n"
"v_accvgpr_read_b32 v29, a145\n"
"v_accvgpr_read_b32 v30, a146\n"
"v_accvgpr_read_b32 v31, a147\n"
"v_pk_mul_f32 v[24:25], v[24:25], v[20:21] op_sel_hi:[1,0]\n"
"v_pk_mul_f32 v[26:27], v[26:27], v[20:21] op_sel_hi:[1,0]\n"
"v_pk_mul_f32 v[28:29], v[28:29], v[20:21] op_sel_hi:[1,0]\n"
"v_pk_mul_f32 v[30:31], v[30:31], v[20:21] op_sel_hi:[1,0]\n"
"v_cvt_pk_bf16_f32 v192, v24, v25\n"
"v_cvt_pk_bf16_f32 v193, v26, v27\n"
"v_cvt_pk_bf16_f32 v194, v28, v29\n"
"v_cvt_pk_bf16_f32 v195, v30, v31\n"
"buffer_store_dwordx4 v[192:195], v18, s[56:59], s25 offen sc1\n"
"v_mfma_f32_16x16x32_bf16 v[64:67], a[192:195], v[224:227], 0\n"
"ds_read_b64_tr_b16 a[224:225], v8 offset:37120\n"
"ds_read_b64_tr_b16 a[226:227], v8 offset:43264\n"
"s_add_u32 s25, s80, 20480\n"
"v_accvgpr_read_b32 v32, a172\n"
"v_accvgpr_read_b32 v33, a173\n"
"v_accvgpr_read_b32 v34, a174\n"
"v_accvgpr_read_b32 v35, a175\n"
"v_mfma_f32_16x16x32_bf16 v[68:71], a[192:195], v[228:231], 0\n"
"v_accvgpr_read_b32 v36, a188\n"
"v_accvgpr_read_b32 v37, a189\n"
"v_accvgpr_read_b32 v38, a190\n"
"v_accvgpr_read_b32 v39, a191\n"
"v_pk_mul_f32 v[32:33], v[32:33], v[14:15] op_sel_hi:[1,0]\n"
"v_pk_mul_f32 v[34:35], v[34:35], v[14:15] op_sel_hi:[1,0]\n"
"v_pk_mul_f32 v[36:37], v[36:37], v[14:15] op_sel_hi:[1,0]\n"
"v_pk_mul_f32 v[38:39], v[38:39], v[14:15] op_sel_hi:[1,0]\n"
"v_cvt_pk_bf16_f32 v220, v32, v33\n"
"v_cvt_pk_bf16_f32 v221, v34, v35\n"
"v_cvt_pk_bf16_f32 v222, v36, v37\n"
"v_cvt_pk_bf16_f32 v223, v38, v39\n"
"buffer_store_dwordx4 v[220:223], v18, s[56:59], s25 offen offset:3072 sc1\n"
"v_mfma_f32_16x16x32_bf16 v[72:75], a[192:195], v[232:235], 0\n"
"v_accvgpr_read_b32 v24, a168\n"
"v_accvgpr_read_b32 v25, a169\n"
"v_accvgpr_read_b32 v26, a170\n"
"v_accvgpr_read_b32 v27, a171\n"
"v_mfma_f32_16x16x32_bf16 v[76:79], a[192:195], v[236:239], 0\n"
"v_accvgpr_read_b32 v28, a184\n"
"v_accvgpr_read_b32 v29, a185\n"
"v_accvgpr_read_b32 v30, a186\n"
"v_accvgpr_read_b32 v31, a187\n"
"v_pk_mul_f32 v[24:25], v[24:25], v[22:23] op_sel_hi:[1,0]\n"
"v_pk_mul_f32 v[26:27], v[26:27], v[22:23] op_sel_hi:[1,0]\n"
"v_pk_mul_f32 v[28:29], v[28:29], v[22:23] op_sel_hi:[1,0]\n"
"v_pk_mul_f32 v[30:31], v[30:31], v[22:23] op_sel_hi:[1,0]\n"
"v_cvt_pk_bf16_f32 v216, v24, v25\n"
"v_cvt_pk_bf16_f32 v217, v26, v27\n"
"v_cvt_pk_bf16_f32 v218, v28, v29\n"
"v_cvt_pk_bf16_f32 v219, v30, v31\n"
"buffer_store_dwordx4 v[216:219], v18, s[56:59], s25 offen offset:2048 sc1\n"
"v_mfma_f32_16x16x32_bf16 v[92:95], a[196:199], v[236:239], 0\n"
"ds_read_b64_tr_b16 a[228:229], v9 offset:37120\n"
"ds_read_b64_tr_b16 a[230:231], v9 offset:43264\n"
"v_accvgpr_read_b32 v32, a164\n"
"v_accvgpr_read_b32 v33, a165\n"
"v_accvgpr_read_b32 v34, a166\n"
"v_accvgpr_read_b32 v35, a167\n"
"v_mfma_f32_16x16x32_bf16 v[88:91], a[196:199], v[232:235], 0\n"
"v_accvgpr_read_b32 v36, a180\n"
"v_accvgpr_read_b32 v37, a181\n"
"v_accvgpr_read_b32 v38, a182\n"
"v_accvgpr_read_b32 v39, a183\n"
"v_pk_mul_f32 v[32:33], v[32:33], v[12:13] op_sel_hi:[1,0]\n"
"v_pk_mul_f32 v[34:35], v[34:35], v[12:13] op_sel_hi:[1,0]\n"
"v_pk_mul_f32 v[36:37], v[36:37], v[12:13] op_sel_hi:[1,0]\n"
"v_pk_mul_f32 v[38:39], v[38:39], v[12:13] op_sel_hi:[1,0]\n"
"v_cvt_pk_bf16_f32 v212, v32, v33\n"
"v_cvt_pk_bf16_f32 v213, v34, v35\n"
"v_cvt_pk_bf16_f32 v214, v36, v37\n"
"v_cvt_pk_bf16_f32 v215, v38, v39\n"
"buffer_store_dwordx4 v[212:215], v18, s[56:59], s25 offen offset:1024 sc1\n"
"v_mfma_f32_16x16x32_bf16 v[84:87], a[196:199], v[228:231], 0\n"
"v_accvgpr_read_b32 v24, a160\n"
"v_accvgpr_read_b32 v25, a161\n"
"v_accvgpr_read_b32 v26, a162\n"
"v_accvgpr_read_b32 v27, a163\n"
"v_mfma_f32_16x16x32_bf16 v[80:83], a[196:199], v[224:227], 0\n"
"v_accvgpr_read_b32 v28, a176\n"
"v_accvgpr_read_b32 v29, a177\n"
"v_accvgpr_read_b32 v30, a178\n"
"v_accvgpr_read_b32 v31, a179\n"
"v_pk_mul_f32 v[24:25], v[24:25], v[20:21] op_sel_hi:[1,0]\n"
"v_pk_mul_f32 v[26:27], v[26:27], v[20:21] op_sel_hi:[1,0]\n"
"v_pk_mul_f32 v[28:29], v[28:29], v[20:21] op_sel_hi:[1,0]\n"
"v_pk_mul_f32 v[30:31], v[30:31], v[20:21] op_sel_hi:[1,0]\n"
"v_cvt_pk_bf16_f32 v208, v24, v25\n"
"v_cvt_pk_bf16_f32 v209, v26, v27\n"
"v_cvt_pk_bf16_f32 v210, v28, v29\n"
"v_cvt_pk_bf16_f32 v211, v30, v31\n"
"buffer_store_dwordx4 v[208:211], v18, s[56:59], s25 offen sc1\n"
"v_mfma_f32_16x16x32_bf16 v[96:99], a[200:203], v[224:227], 0\n"
"ds_read_b64_tr_b16 a[232:233], v0 offset:24576\n"
"ds_read_b64_tr_b16 a[234:235], v0 offset:30720\n"
"v_accvgpr_write_b32 a108, v172\n"
"v_mfma_f32_16x16x32_bf16 v[100:103], a[200:203], v[228:231], 0\n"
"v_accvgpr_write_b32 a109, v173\n"
"v_accvgpr_write_b32 a110, v174\n"
"v_accvgpr_write_b32 a111, v175\n"
"v_mfma_f32_16x16x32_bf16 v[104:107], a[200:203], v[232:235], 0\n"
"v_accvgpr_write_b32 a104, v168\n"
"v_accvgpr_write_b32 a105, v169\n"
"v_accvgpr_write_b32 a106, v170\n"
"v_mfma_f32_16x16x32_bf16 v[108:111], a[200:203], v[236:239], 0\n"
"v_accvgpr_write_b32 a107, v171\n"
"v_accvgpr_write_b32 a100, v164\n"
"v_accvgpr_write_b32 a101, v165\n"
"v_mfma_f32_16x16x32_bf16 v[124:127], a[204:207], v[236:239], 0\n"
"ds_read_b64_tr_b16 a[236:237], v1 offset:24576\n"
"ds_read_b64_tr_b16 a[238:239], v1 offset:30720\n"
"v_accvgpr_write_b32 a102, v166\n"
"v_mfma_f32_16x16x32_bf16 v[120:123], a[204:207], v[232:235], 0\n"
"v_accvgpr_write_b32 a103, v167\n"
"v_accvgpr_write_b32 a96, v160\n"
"v_accvgpr_write_b32 a97, v161\n"
"v_mfma_f32_16x16x32_bf16 v[116:119], a[204:207], v[228:231], 0\n"
"v_accvgpr_write_b32 a98, v162\n"
"v_accvgpr_write_b32 a99, v163\n"
"v_accvgpr_write_b32 a140, v204\n"
"v_mfma_f32_16x16x32_bf16 v[112:115], a[204:207], v[224:227], 0\n"
"s_waitcnt lgkmcnt(8)\n"
"v_accvgpr_write_b32 a141, v205\n"
"v_accvgpr_write_b32 a142, v206\n"
"v_mfma_f32_16x16x32_bf16 v[128:131], a[208:211], v[224:227], 0\n"
"ds_read_b64_tr_b16 a[192:193], v2 offset:24576\n"
"ds_read_b64_tr_b16 a[194:195], v2 offset:30720\n"
"v_accvgpr_write_b32 a143, v207\n"
"v_mfma_f32_16x16x32_bf16 v[132:135], a[208:211], v[228:231], 0\n"
"v_accvgpr_write_b32 a136, v200\n"
"v_accvgpr_write_b32 a137, v201\n"
"v_accvgpr_write_b32 a138, v202\n"
"v_mfma_f32_16x16x32_bf16 v[136:139], a[208:211], v[232:235], 0\n"
"v_accvgpr_write_b32 a139, v203\n"
"v_accvgpr_write_b32 a132, v196\n"
"v_accvgpr_write_b32 a133, v197\n"
"v_mfma_f32_16x16x32_bf16 v[140:143], a[208:211], v[236:239], 0\n"
"v_accvgpr_write_b32 a134, v198\n"
"v_accvgpr_write_b32 a135, v199\n"
"v_accvgpr_write_b32 a128, v192\n"
"v_mfma_f32_16x16x32_bf16 v[156:159], a[212:215], v[236:239], 0\n"
"ds_read_b64_tr_b16 a[196:197], v3 offset:24576\n"
"ds_read_b64_tr_b16 a[198:199], v3 offset:30720\n"
"v_accvgpr_write_b32 a129, v193\n"
"v_mfma_f32_16x16x32_bf16 v[152:155], a[212:215], v[232:235], 0\n"
"v_accvgpr_write_b32 a130, v194\n"
"v_accvgpr_write_b32 a131, v195\n"
"v_accvgpr_write_b32 a172, v220\n"
"v_mfma_f32_16x16x32_bf16 v[148:151], a[212:215], v[228:231], 0\n"
"v_accvgpr_write_b32 a173, v221\n"
"v_accvgpr_write_b32 a174, v222\n"
"v_accvgpr_write_b32 a175, v223\n"
"v_mfma_f32_16x16x32_bf16 v[144:147], a[212:215], v[224:227], 0\n"
"v_accvgpr_write_b32 a168, v216\n"
"v_accvgpr_write_b32 a169, v217\n"
"v_accvgpr_write_b32 a170, v218\n"
"v_mfma_f32_16x16x32_bf16 v[160:163], a[216:219], v[224:227], 0\n"
"ds_read_b64_tr_b16 a[200:201], v0 offset:24704\n"
"ds_read_b64_tr_b16 a[202:203], v0 offset:30848\n"
"v_accvgpr_write_b32 a171, v219\n"
"v_mfma_f32_16x16x32_bf16 v[164:167], a[216:219], v[228:231], 0\n"
"v_accvgpr_write_b32 a164, v212\n"
"v_accvgpr_write_b32 a165, v213\n"
"v_accvgpr_write_b32 a166, v214\n"
"v_mfma_f32_16x16x32_bf16 v[168:171], a[216:219], v[232:235], 0\n"
"v_accvgpr_write_b32 a167, v215\n"
"v_accvgpr_write_b32 a160, v208\n"
"v_accvgpr_write_b32 a161, v209\n"
"v_mfma_f32_16x16x32_bf16 v[172:175], a[216:219], v[236:239], 0\n"
"v_accvgpr_write_b32 a162, v210\n"
"v_accvgpr_write_b32 a163, v211\n"
"v_mfma_f32_16x16x32_bf16 v[188:191], a[220:223], v[236:239], 0\n"
"ds_read_b64_tr_b16 a[204:205], v1 offset:24704\n"
"ds_read_b64_tr_b16 a[206:207], v1 offset:30848\n"
"v_mfma_f32_16x16x32_bf16 v[184:187], a[220:223], v[232:235], 0\n"
"v_mfma_f32_16x16x32_bf16 v[180:183], a[220:223], v[228:231], 0\n"
"v_mfma_f32_16x16x32_bf16 v[176:179], a[220:223], v[224:227], 0\n"
"s_waitcnt lgkmcnt(8)\n"
"v_mfma_f32_16x16x32_bf16 v[192:195], a[224:227], v[224:227], 0\n"
"ds_read_b64_tr_b16 a[208:209], v2 offset:24704\n"
"ds_read_b64_tr_b16 a[210:211], v2 offset:30848\n"
"v_mfma_f32_16x16x32_bf16 v[196:199], a[224:227], v[228:231], 0\n"
"v_mfma_f32_16x16x32_bf16 v[200:203], a[224:227], v[232:235], 0\n"
"v_mfma_f32_16x16x32_bf16 v[204:207], a[224:227], v[236:239], 0\n"
"v_mfma_f32_16x16x32_bf16 v[220:223], a[228:231], v[236:239], 0\n"
"ds_read_b64_tr_b16 a[212:213], v3 offset:24704\n"
"ds_read_b64_tr_b16 a[214:215], v3 offset:30848\n"
"v_mfma_f32_16x16x32_bf16 v[216:219], a[228:231], v[232:235], 0\n"
"v_mfma_f32_16x16x32_bf16 v[212:215], a[228:231], v[228:231], 0\n"
"v_mfma_f32_16x16x32_bf16 v[208:211], a[228:231], v[224:227], 0\n"
"v_mfma_f32_16x16x32_bf16 v[64:67], a[232:235], v[240:243], v[64:67]\n"
"ds_read_b64_tr_b16 a[216:217], v0 offset:24832\n"
"ds_read_b64_tr_b16 a[218:219], v0 offset:30976\n"
"v_mfma_f32_16x16x32_bf16 v[68:71], a[232:235], v[244:247], v[68:71]\n"
"v_mfma_f32_16x16x32_bf16 v[72:75], a[232:235], v[248:251], v[72:75]\n"
"v_mfma_f32_16x16x32_bf16 v[76:79], a[232:235], v[252:255], v[76:79]\n"
"v_mfma_f32_16x16x32_bf16 v[92:95], a[236:239], v[252:255], v[92:95]\n"
"ds_read_b64_tr_b16 a[220:221], v1 offset:24832\n"
"ds_read_b64_tr_b16 a[222:223], v1 offset:30976\n"
"v_mfma_f32_16x16x32_bf16 v[88:91], a[236:239], v[248:251], v[88:91]\n"
"v_mfma_f32_16x16x32_bf16 v[84:87], a[236:239], v[244:247], v[84:87]\n"
"v_mfma_f32_16x16x32_bf16 v[80:83], a[236:239], v[240:243], v[80:83]\n"
"s_waitcnt lgkmcnt(8)\n"
"v_mfma_f32_16x16x32_bf16 v[96:99], a[192:195], v[240:243], v[96:99]\n"
"ds_read_b64_tr_b16 a[224:225], v0 offset:36864\n"
"ds_read_b64_tr_b16 a[226:227], v0 offset:43008\n"
"v_mfma_f32_16x16x32_bf16 v[100:103], a[192:195], v[244:247], v[100:103]\n"
"v_mfma_f32_16x16x32_bf16 v[104:107], a[192:195], v[248:251], v[104:107]\n"
"v_mfma_f32_16x16x32_bf16 v[108:111], a[192:195], v[252:255], v[108:111]\n"
"v_mfma_f32_16x16x32_bf16 v[124:127], a[196:199], v[252:255], v[124:127]\n"
"ds_read_b64_tr_b16 a[228:229], v1 offset:36864\n"
"ds_read_b64_tr_b16 a[230:231], v1 offset:43008\n"
"v_mfma_f32_16x16x32_bf16 v[120:123], a[196:199], v[248:251], v[120:123]\n"
"v_mfma_f32_16x16x32_bf16 v[116:119], a[196:199], v[244:247], v[116:119]\n"
"v_mfma_f32_16x16x32_bf16 v[112:115], a[196:199], v[240:243], v[112:115]\n"
"v_mfma_f32_16x16x32_bf16 v[128:131], a[200:203], v[240:243], v[128:131]\n"
"ds_read_b64_tr_b16 a[232:233], v2 offset:36864\n"
"ds_read_b64_tr_b16 a[234:235], v2 offset:43008\n"
"v_mfma_f32_16x16x32_bf16 v[132:135], a[200:203], v[244:247], v[132:135]\n"
"v_mfma_f32_16x16x32_bf16 v[136:139], a[200:203], v[248:251], v[136:139]\n"
"v_mfma_f32_16x16x32_bf16 v[140:143], a[200:203], v[252:255], v[140:143]\n"
"v_mfma_f32_16x16x32_bf16 v[156:159], a[204:207], v[252:255], v[156:159]\n"
"ds_read_b64_tr_b16 a[236:237], v3 offset:36864\n"
"ds_read_b64_tr_b16 a[238:239], v3 offset:43008\n"
"v_mfma_f32_16x16x32_bf16 v[152:155], a[204:207], v[248:251], v[152:155]\n"
"v_mfma_f32_16x16x32_bf16 v[148:151], a[204:207], v[244:247], v[148:151]\n"
"v_mfma_f32_16x16x32_bf16 v[144:147], a[204:207], v[240:243], v[144:147]\n"
"s_waitcnt lgkmcnt(8)\n"
"v_mfma_f32_16x16x32_bf16 v[160:163], a[208:211], v[240:243], v[160:163]\n"
"ds_read_b64_tr_b16 a[192:193], v0 offset:36992\n"
"ds_read_b64_tr_b16 a[194:195], v0 offset:43136\n"
"v_mfma_f32_16x16x32_bf16 v[164:167], a[208:211], v[244:247], v[164:167]\n"
"v_mfma_f32_16x16x32_bf16 v[168:171], a[208:211], v[248:251], v[168:171]\n"
"v_mfma_f32_16x16x32_bf16 v[172:175], a[208:211], v[252:255], v[172:175]\n"
"v_mfma_f32_16x16x32_bf16 v[188:191], a[212:215], v[252:255], v[188:191]\n"
"ds_read_b64_tr_b16 a[196:197], v1 offset:36992\n"
"ds_read_b64_tr_b16 a[198:199], v1 offset:43136\n"
"v_mfma_f32_16x16x32_bf16 v[184:187], a[212:215], v[248:251], v[184:187]\n"
"v_mfma_f32_16x16x32_bf16 v[180:183], a[212:215], v[244:247], v[180:183]\n"
"v_mfma_f32_16x16x32_bf16 v[176:179], a[212:215], v[240:243], v[176:179]\n"
"v_mfma_f32_16x16x32_bf16 v[192:195], a[216:219], v[240:243], v[192:195]\n"
"ds_read_b64_tr_b16 a[200:201], v2 offset:36992\n"
"ds_read_b64_tr_b16 a[202:203], v2 offset:43136\n"
"v_mfma_f32_16x16x32_bf16 v[196:199], a[216:219], v[244:247], v[196:199]\n"
"v_mfma_f32_16x16x32_bf16 v[200:203], a[216:219], v[248:251], v[200:203]\n"
"v_mfma_f32_16x16x32_bf16 v[204:207], a[216:219], v[252:255], v[204:207]\n"
"v_mfma_f32_16x16x32_bf16 v[220:223], a[220:223], v[252:255], v[220:223]\n"
"ds_read_b64_tr_b16 a[204:205], v3 offset:36992\n"
"ds_read_b64_tr_b16 a[206:207], v3 offset:43136\n"
"v_mfma_f32_16x16x32_bf16 v[216:219], a[220:223], v[248:251], v[216:219]\n"
"v_mfma_f32_16x16x32_bf16 v[212:215], a[220:223], v[244:247], v[212:215]\n"
"v_mfma_f32_16x16x32_bf16 v[208:211], a[220:223], v[240:243], v[208:211]\n"
"s_waitcnt lgkmcnt(8)\n"
"v_mfma_f32_16x16x32_bf16 v[64:67], a[224:227], v[40:43], v[64:67]\n"
"ds_read_b64_tr_b16 a[208:209], v0 offset:37120\n"
"ds_read_b64_tr_b16 a[210:211], v0 offset:43264\n"
"v_mfma_f32_16x16x32_bf16 v[68:71], a[224:227], v[44:47], v[68:71]\n"
"v_mfma_f32_16x16x32_bf16 v[72:75], a[224:227], v[48:51], v[72:75]\n"
"v_mfma_f32_16x16x32_bf16 v[76:79], a[224:227], v[52:55], v[76:79]\n"
"v_mfma_f32_16x16x32_bf16 v[92:95], a[228:231], v[52:55], v[92:95]\n"
"ds_read_b64_tr_b16 a[212:213], v1 offset:37120\n"
"ds_read_b64_tr_b16 a[214:215], v1 offset:43264\n"
"v_mfma_f32_16x16x32_bf16 v[88:91], a[228:231], v[48:51], v[88:91]\n"
"v_mfma_f32_16x16x32_bf16 v[84:87], a[228:231], v[44:47], v[84:87]\n"
"v_mfma_f32_16x16x32_bf16 v[80:83], a[228:231], v[40:43], v[80:83]\n"
"v_mfma_f32_16x16x32_bf16 v[96:99], a[232:235], v[40:43], v[96:99]\n"
"ds_read_b64_tr_b16 a[216:217], v4 offset:36864\n"
"ds_read_b64_tr_b16 a[218:219], v4 offset:43008\n"
"v_mfma_f32_16x16x32_bf16 v[100:103], a[232:235], v[44:47], v[100:103]\n"
"v_mfma_f32_16x16x32_bf16 v[104:107], a[232:235], v[48:51], v[104:107]\n"
"v_mfma_f32_16x16x32_bf16 v[108:111], a[232:235], v[52:55], v[108:111]\n"
"v_mfma_f32_16x16x32_bf16 v[124:127], a[236:239], v[52:55], v[124:127]\n"
"ds_read_b64_tr_b16 a[220:221], v5 offset:36864\n"
"ds_read_b64_tr_b16 a[222:223], v5 offset:43008\n"
"v_mfma_f32_16x16x32_bf16 v[120:123], a[236:239], v[48:51], v[120:123]\n"
"s_and_b32 s30, 15, s93\n"
"v_add_u32 v12, 1, v61\n"
"v_mfma_f32_16x16x32_bf16 v[116:119], a[236:239], v[44:47], v[116:119]\n"
"v_cndmask_b32_e64 v13, 2.0, v60, s[30:31]\n"
"v_mfma_f32_16x16x32_bf16 v[112:115], a[236:239], v[40:43], v[112:115]\n"
"s_waitcnt lgkmcnt(8)\n"
"v_mfma_f32_16x16x32_bf16 v[128:131], a[192:195], v[40:43], v[128:131]\n"
"ds_read_b64_tr_b16 a[224:225], v6 offset:36864\n"
"ds_read_b64_tr_b16 a[226:227], v6 offset:43008\n"
"v_mfma_f32_16x16x32_bf16 v[132:135], a[192:195], v[44:47], v[132:135]\n"
"v_mfma_f32_16x16x32_bf16 v[136:139], a[192:195], v[48:51], v[136:139]\n"
"v_mfma_f32_16x16x32_bf16 v[140:143], a[192:195], v[52:55], v[140:143]\n"
"s_waitcnt vmcnt(0) lgkmcnt(0)\n"
"s_barrier\n"
"buffer_store_dword v12, v13, s[72:75], 0 offen sc1\n"
"v_mfma_f32_16x16x32_bf16 v[156:159], a[196:199], v[52:55], v[156:159]\n"
"ds_read_b64_tr_b16 a[228:229], v7 offset:36864\n"
"ds_read_b64_tr_b16 a[230:231], v7 offset:43008\n"
"v_mfma_f32_16x16x32_bf16 v[152:155], a[196:199], v[48:51], v[152:155]\n"
"s_add_u32 s24, s87, 0\n"
"s_add_u32 m0, s76, 12288\n"
"v_mfma_f32_16x16x32_bf16 v[148:151], a[196:199], v[44:47], v[148:151]\n"
"buffer_load_dwordx4 v56, s[60:63], s24 offen lds\n"
"v_mfma_f32_16x16x32_bf16 v[144:147], a[196:199], v[40:43], v[144:147]\n"
"v_mfma_f32_16x16x32_bf16 v[160:163], a[200:203], v[40:43], v[160:163]\n"
"ds_read_b64_tr_b16 a[232:233], v4 offset:36992\n"
"ds_read_b64_tr_b16 a[234:235], v4 offset:43136\n"
"v_mfma_f32_16x16x32_bf16 v[164:167], a[200:203], v[44:47], v[164:167]\n"
"v_mfma_f32_16x16x32_bf16 v[168:171], a[200:203], v[48:51], v[168:171]\n"
"buffer_load_dwordx4 v57, s[60:63], s24 offen offset:1024 lds\n"
"v_mfma_f32_16x16x32_bf16 v[172:175], a[200:203], v[52:55], v[172:175]\n"
"v_mfma_f32_16x16x32_bf16 v[188:191], a[204:207], v[52:55], v[188:191]\n"
"ds_read_b64_tr_b16 a[236:237], v5 offset:36992\n"
"ds_read_b64_tr_b16 a[238:239], v5 offset:43136\n"
"v_mfma_f32_16x16x32_bf16 v[184:187], a[204:207], v[48:51], v[184:187]\n"
"v_mfma_f32_16x16x32_bf16 v[180:183], a[204:207], v[44:47], v[180:183]\n"
"buffer_load_dwordx4 v58, s[60:63], s24 offen offset:2048 lds\n"
"v_mfma_f32_16x16x32_bf16 v[176:179], a[204:207], v[40:43], v[176:179]\n"
"s_waitcnt lgkmcnt(15)\n"
"v_mfma_f32_16x16x32_bf16 v[192:195], a[208:211], v[40:43], v[192:195]\n"
"ds_read_b64_tr_b16 a[192:193], v6 offset:36992\n"
"ds_read_b64_tr_b16 a[194:195], v6 offset:43136\n"
"v_mfma_f32_16x16x32_bf16 v[196:199], a[208:211], v[44:47], v[196:199]\n"
"v_mfma_f32_16x16x32_bf16 v[200:203], a[208:211], v[48:51], v[200:203]\n"
"s_add_u32 s24, s85, 40960\n"
"s_add_u32 m0, s76, 122880\n"
"v_mfma_f32_16x16x32_bf16 v[204:207], a[208:211], v[52:55], v[204:207]\n"
"buffer_load_dwordx4 v56, s[60:63], s24 offen lds\n"
"v_mfma_f32_16x16x32_bf16 v[220:223], a[212:215], v[52:55], v[220:223]\n"
"ds_read_b64_tr_b16 a[196:197], v7 offset:36992\n"
"ds_read_b64_tr_b16 a[198:199], v7 offset:43136\n"
"v_mfma_f32_16x16x32_bf16 v[216:219], a[212:215], v[48:51], v[216:219]\n"
"v_mfma_f32_16x16x32_bf16 v[212:215], a[212:215], v[44:47], v[212:215]\n"
"v_mfma_f32_16x16x32_bf16 v[208:211], a[212:215], v[40:43], v[208:211]\n"
"buffer_load_dwordx4 v57, s[60:63], s24 offen offset:1024 lds\n"
"v_mfma_f32_16x16x32_bf16 v[64:67], a[216:219], a[96:99], v[64:67]\n"
"ds_read_b64_tr_b16 a[200:201], v4 offset:37120\n"
"ds_read_b64_tr_b16 a[202:203], v4 offset:43264\n"
"v_mfma_f32_16x16x32_bf16 v[68:71], a[216:219], a[100:103], v[68:71]\n"
"v_mfma_f32_16x16x32_bf16 v[72:75], a[216:219], a[104:107], v[72:75]\n"
"v_mfma_f32_16x16x32_bf16 v[76:79], a[216:219], a[108:111], v[76:79]\n"
"buffer_load_dwordx4 v58, s[60:63], s24 offen offset:2048 lds\n"
"v_mfma_f32_16x16x32_bf16 v[92:95], a[220:223], a[108:111], v[92:95]\n"
"ds_read_b64_tr_b16 a[204:205], v5 offset:37120\n"
"ds_read_b64_tr_b16 a[206:207], v5 offset:43264\n"
"v_mfma_f32_16x16x32_bf16 v[88:91], a[220:223], a[104:107], v[88:91]\n"
"v_mfma_f32_16x16x32_bf16 v[84:87], a[220:223], a[100:103], v[84:87]\n"
"v_mfma_f32_16x16x32_bf16 v[80:83], a[220:223], a[96:99], v[80:83]\n"
"s_waitcnt lgkmcnt(8)\n"
"s_add_u32 s24, s86, 40960\n"
"v_mfma_f32_16x16x32_bf16 v[96:99], a[224:227], a[96:99], v[96:99]\n"
"ds_read_b64_tr_b16 a[208:209], v0 offset:49152\n"
"ds_read_b64_tr_b16 a[210:211], v0 offset:55296\n"
"v_mfma_f32_16x16x32_bf16 v[100:103], a[224:227], a[100:103], v[100:103]\n"
"s_add_u32 m0, s76, 135168\n"
"s_nop 0\n"
"buffer_load_dwordx4 v56, s[60:63], s24 offen lds\n"
"v_mfma_f32_16x16x32_bf16 v[104:107], a[224:227], a[104:107], v[104:107]\n"
"v_mfma_f32_16x16x32_bf16 v[108:111], a[224:227], a[108:111], v[108:111]\n"
"v_mfma_f32_16x16x32_bf16 v[124:127], a[228:231], a[108:111], v[124:127]\n"
"ds_read_b64_tr_b16 a[212:213], v1 offset:49152\n"
"ds_read_b64_tr_b16 a[214:215], v1 offset:55296\n"
"v_mfma_f32_16x16x32_bf16 v[120:123], a[228:231], a[104:107], v[120:123]\n"
"buffer_load_dwordx4 v57, s[60:63], s24 offen offset:1024 lds\n"
"v_mfma_f32_16x16x32_bf16 v[116:119], a[228:231], a[100:103], v[116:119]\n"
"v_mfma_f32_16x16x32_bf16 v[112:115], a[228:231], a[96:99], v[112:115]\n"
"v_mfma_f32_16x16x32_bf16 v[128:131], a[232:235], a[96:99], v[128:131]\n"
"ds_read_b64_tr_b16 a[216:217], v2 offset:49152\n"
"ds_read_b64_tr_b16 a[218:219], v2 offset:55296\n"
"v_mfma_f32_16x16x32_bf16 v[132:135], a[232:235], a[100:103], v[132:135]\n"
"buffer_load_dwordx4 v58, s[60:63], s24 offen offset:2048 lds\n"
"v_mfma_f32_16x16x32_bf16 v[136:139], a[232:235], a[104:107], v[136:139]\n"
"v_mfma_f32_16x16x32_bf16 v[140:143], a[232:235], a[108:111], v[140:143]\n"
"v_mfma_f32_16x16x32_bf16 v[156:159], a[236:239], a[108:111], v[156:159]\n"
"ds_read_b64_tr_b16 a[220:221], v3 offset:49152\n"
"ds_read_b64_tr_b16 a[222:223], v3 offset:55296\n"
"v_mfma_f32_16x16x32_bf16 v[152:155], a[236:239], a[104:107], v[152:155]\n"
"s_add_u32 s24, s87, 40960\n"
"s_add_u32 m0, s76, 73728\n"
"v_mfma_f32_16x16x32_bf16 v[148:151], a[236:239], a[100:103], v[148:151]\n"
"buffer_load_dwordx4 v56, s[60:63], s24 offen lds\n"
"v_mfma_f32_16x16x32_bf16 v[144:147], a[236:239], a[96:99], v[144:147]\n"
"s_waitcnt lgkmcnt(8)\n"
"v_mfma_f32_16x16x32_bf16 v[160:163], a[192:195], a[96:99], v[160:163]\n"
"ds_read_b64_tr_b16 a[224:225], v0 offset:49280\n"
"ds_read_b64_tr_b16 a[226:227], v0 offset:55424\n"
"v_mfma_f32_16x16x32_bf16 v[164:167], a[192:195], a[100:103], v[164:167]\n"
"v_mfma_f32_16x16x32_bf16 v[168:171], a[192:195], a[104:107], v[168:171]\n"
"buffer_load_dwordx4 v57, s[60:63], s24 offen offset:1024 lds\n"
"v_mfma_f32_16x16x32_bf16 v[172:175], a[192:195], a[108:111], v[172:175]\n"
"v_mfma_f32_16x16x32_bf16 v[188:191], a[196:199], a[108:111], v[188:191]\n"
"ds_read_b64_tr_b16 a[228:229], v1 offset:49280\n"
"ds_read_b64_tr_b16 a[230:231], v1 offset:55424\n"
"v_mfma_f32_16x16x32_bf16 v[184:187], a[196:199], a[104:107], v[184:187]\n"
"v_mfma_f32_16x16x32_bf16 v[180:183], a[196:199], a[100:103], v[180:183]\n"
"buffer_load_dwordx4 v58, s[60:63], s24 offen offset:2048 lds\n"
"v_mfma_f32_16x16x32_bf16 v[176:179], a[196:199], a[96:99], v[176:179]\n"
"s_andn2_b32 s3, 15, s92\n"
"s_mov_b32 s30, s3\n"
"v_mfma_f32_16x16x32_bf16 v[192:195], a[200:203], a[96:99], v[192:195]\n"
"ds_read_b64_tr_b16 a[232:233], v2 offset:49280\n"
"ds_read_b64_tr_b16 a[234:235], v2 offset:55424\n"
"v_mfma_f32_16x16x32_bf16 v[196:199], a[200:203], a[100:103], v[196:199]\n"
"v_cndmask_b32_e64 v14, 2.0, v60, s[30:31]\n"
"buffer_load_dword v15, v14, s[72:75], 0 offen sc1\n"
"v_mfma_f32_16x16x32_bf16 v[200:203], a[200:203], a[104:107], v[200:203]\n"
"v_mfma_f32_16x16x32_bf16 v[204:207], a[200:203], a[108:111], v[204:207]\n"
"s_add_u32 s24, s85, 81920\n"
"s_add_u32 m0, s76, 147456\n"
"v_mfma_f32_16x16x32_bf16 v[220:223], a[204:207], a[108:111], v[220:223]\n"
"ds_read_b64_tr_b16 a[236:237], v3 offset:49280\n"
"ds_read_b64_tr_b16 a[238:239], v3 offset:55424\n"
"v_mfma_f32_16x16x32_bf16 v[216:219], a[204:207], a[104:107], v[216:219]\n"
"buffer_load_dwordx4 v56, s[60:63], s24 offen lds\n"
"v_mfma_f32_16x16x32_bf16 v[212:215], a[204:207], a[100:103], v[212:215]\n"
"v_mfma_f32_16x16x32_bf16 v[208:211], a[204:207], a[96:99], v[208:211]\n"
"s_waitcnt lgkmcnt(8)\n"
"buffer_load_dwordx4 v57, s[60:63], s24 offen offset:1024 lds\n"
"v_mfma_f32_16x16x32_bf16 v[64:67], a[208:211], a[128:131], v[64:67]\n"
"ds_read_b64_tr_b16 a[192:193], v0 offset:49408\n"
"ds_read_b64_tr_b16 a[194:195], v0 offset:55552\n"
"v_mfma_f32_16x16x32_bf16 v[68:71], a[208:211], a[132:135], v[68:71]\n"
"buffer_load_dwordx4 v58, s[60:63], s24 offen offset:2048 lds\n"
"v_mfma_f32_16x16x32_bf16 v[72:75], a[208:211], a[136:139], v[72:75]\n"
"v_mfma_f32_16x16x32_bf16 v[76:79], a[208:211], a[140:143], v[76:79]\n"
"s_add_u32 s24, s86, 81920\n"
"s_add_u32 m0, s76, 24576\n"
"v_mfma_f32_16x16x32_bf16 v[92:95], a[212:215], a[140:143], v[92:95]\n"
"ds_read_b64_tr_b16 a[196:197], v1 offset:49408\n"
"ds_read_b64_tr_b16 a[198:199], v1 offset:55552\n"
"v_mfma_f32_16x16x32_bf16 v[88:91], a[212:215], a[136:139], v[88:91]\n"
"buffer_load_dwordx4 v56, s[60:63], s24 offen lds\n"
"v_mfma_f32_16x16x32_bf16 v[84:87], a[212:215], a[132:135], v[84:87]\n"
"v_mfma_f32_16x16x32_bf16 v[80:83], a[212:215], a[128:131], v[80:83]\n"
"buffer_load_dwordx4 v57, s[60:63], s24 offen offset:1024 lds\n"
"v_mfma_f32_16x16x32_bf16 v[96:99], a[216:219], a[128:131], v[96:99]\n"
"ds_read_b64_tr_b16 a[200:201], v8 offset:0\n"
"ds_read_b64_tr_b16 a[202:203], v8 offset:6144\n"
"v_mfma_f32_16x16x32_bf16 v[100:103], a[216:219], a[132:135], v[100:103]\n"
"buffer_load_dwordx4 v58, s[60:63], s24 offen offset:2048 lds\n"
"v_mfma_f32_16x16x32_bf16 v[104:107], a[216:219], a[136:139], v[104:107]\n"
"v_mfma_f32_16x16x32_bf16 v[108:111], a[216:219], a[140:143], v[108:111]\n"
"v_mfma_f32_16x16x32_bf16 v[124:127], a[220:223], a[140:143], v[124:127]\n"
"ds_read_b64_tr_b16 a[204:205], v9 offset:0\n"
"ds_read_b64_tr_b16 a[206:207], v9 offset:6144\n"
"v_mfma_f32_16x16x32_bf16 v[120:123], a[220:223], a[136:139], v[120:123]\n"
"v_mfma_f32_16x16x32_bf16 v[116:119], a[220:223], a[132:135], v[116:119]\n"
"v_mfma_f32_16x16x32_bf16 v[112:115], a[220:223], a[128:131], v[112:115]\n"
"s_waitcnt lgkmcnt(8)\n"
"v_mfma_f32_16x16x32_bf16 v[128:131], a[224:227], a[128:131], v[128:131]\n"
"ds_read_b64_tr_b16 a[208:209], v10 offset:0\n"
"ds_read_b64_tr_b16 a[210:211], v10 offset:6144\n"
"v_mfma_f32_16x16x32_bf16 v[132:135], a[224:227], a[132:135], v[132:135]\n"
"v_mfma_f32_16x16x32_bf16 v[136:139], a[224:227], a[136:139], v[136:139]\n"
"v_mfma_f32_16x16x32_bf16 v[140:143], a[224:227], a[140:143], v[140:143]\n"
"v_mfma_f32_16x16x32_bf16 v[156:159], a[228:231], a[140:143], v[156:159]\n"
"ds_read_b64_tr_b16 a[212:213], v11 offset:0\n"
"ds_read_b64_tr_b16 a[214:215], v11 offset:6144\n"
"v_mfma_f32_16x16x32_bf16 v[152:155], a[228:231], a[136:139], v[152:155]\n"
"v_mfma_f32_16x16x32_bf16 v[148:151], a[228:231], a[132:135], v[148:151]\n"
"v_mfma_f32_16x16x32_bf16 v[144:147], a[228:231], a[128:131], v[144:147]\n"
"v_mfma_f32_16x16x32_bf16 v[160:163], a[232:235], a[128:131], v[160:163]\n"
"ds_read_b64_tr_b16 a[216:217], v8 offset:128\n"
"ds_read_b64_tr_b16 a[218:219], v8 offset:6272\n"
"v_mfma_f32_16x16x32_bf16 v[164:167], a[232:235], a[132:135], v[164:167]\n"
"v_mfma_f32_16x16x32_bf16 v[168:171], a[232:235], a[136:139], v[168:171]\n"
"v_mfma_f32_16x16x32_bf16 v[172:175], a[232:235], a[140:143], v[172:175]\n"
"v_mfma_f32_16x16x32_bf16 v[188:191], a[236:239], a[140:143], v[188:191]\n"
"ds_read_b64_tr_b16 a[220:221], v9 offset:128\n"
"ds_read_b64_tr_b16 a[222:223], v9 offset:6272\n"
"v_mfma_f32_16x16x32_bf16 v[184:187], a[236:239], a[136:139], v[184:187]\n"
"v_mfma_f32_16x16x32_bf16 v[180:183], a[236:239], a[132:135], v[180:183]\n"
"v_mfma_f32_16x16x32_bf16 v[176:179], a[236:239], a[128:131], v[176:179]\n"
"s_waitcnt lgkmcnt(8)\n"
"v_mfma_f32_16x16x32_bf16 v[192:195], a[192:195], a[128:131], v[192:195]\n"
"ds_read_b64_tr_b16 a[224:225], v10 offset:128\n"
"ds_read_b64_tr_b16 a[226:227], v10 offset:6272\n"
"v_mfma_f32_16x16x32_bf16 v[196:199], a[192:195], a[132:135], v[196:199]\n"
"v_mfma_f32_16x16x32_bf16 v[200:203], a[192:195], a[136:139], v[200:203]\n"
"s_waitcnt vmcnt(6)\n"
"v_cmp_ne_u32 vcc, v15, v61\n"
"s_nop 4\n"
"s_and_b32 s33, vcc_lo, s3\n"
"s_cmp_eq_u32 s33, s3\n"
"s_cbranch_scc0 .Lmha_f_spin\n"
".Lmha_f_ok:\n"
"v_mfma_f32_16x16x32_bf16 v[204:207], a[192:195], a[140:143], v[204:207]\n"
"s_add_u32 s25, s81, 0\n"
"buffer_load_dwordx4 a[0:3], v18, s[56:59], s25 offen sc1\n"
"v_mfma_f32_16x16x32_bf16 v[220:223], a[196:199], a[140:143], v[220:223]\n"
"ds_read_b64_tr_b16 a[228:229], v11 offset:128\n"
"ds_read_b64_tr_b16 a[230:231], v11 offset:6272\n"
"buffer_load_dwordx4 a[4:7], v18, s[56:59], s25 offen offset:1024 sc1\n"
"v_mfma_f32_16x16x32_bf16 v[216:219], a[196:199], a[136:139], v[216:219]\n"
"buffer_load_dwordx4 a[8:11], v18, s[56:59], s25 offen offset:2048 sc1\n"
"v_mfma_f32_16x16x32_bf16 v[212:215], a[196:199], a[132:135], v[212:215]\n"
"buffer_load_dwordx4 a[12:15], v18, s[56:59], s25 offen offset:3072 sc1\n"
"v_mfma_f32_16x16x32_bf16 v[208:211], a[196:199], a[128:131], v[208:211]\n"
"s_add_u32 s25, s82, 0\n"
"buffer_load_dwordx4 a[16:19], v18, s[56:59], s25 offen sc1\n"
"v_mfma_f32_16x16x32_bf16 v[64:67], a[200:203], a[160:163], v[64:67]\n"
"ds_read_b64_tr_b16 a[232:233], v8 offset:256\n"
"ds_read_b64_tr_b16 a[234:235], v8 offset:6400\n"
"buffer_load_dwordx4 a[20:23], v18, s[56:59], s25 offen offset:1024 sc1\n"
"v_mfma_f32_16x16x32_bf16 v[68:71], a[200:203], a[164:167], v[68:71]\n"
"buffer_load_dwordx4 a[24:27], v18, s[56:59], s25 offen offset:2048 sc1\n"
"v_mfma_f32_16x16x32_bf16 v[72:75], a[200:203], a[168:171], v[72:75]\n"
"buffer_load_dwordx4 a[28:31], v18, s[56:59], s25 offen offset:3072 sc1\n"
"v_mfma_f32_16x16x32_bf16 v[76:79], a[200:203], a[172:175], v[76:79]\n"
"s_add_u32 s25, s83, 0\n"
"buffer_load_dwordx4 a[32:35], v18, s[56:59], s25 offen sc1\n"
"v_mfma_f32_16x16x32_bf16 v[92:95], a[204:207], a[172:175], v[92:95]\n"
"ds_read_b64_tr_b16 a[236:237], v9 offset:256\n"
"ds_read_b64_tr_b16 a[238:239], v9 offset:6400\n"
"v_mfma_f32_16x16x32_bf16 v[88:91], a[204:207], a[168:171], v[88:91]\n"
"v_mfma_f32_16x16x32_bf16 v[84:87], a[204:207], a[164:167], v[84:87]\n"
"v_mfma_f32_16x16x32_bf16 v[80:83], a[204:207], a[160:163], v[80:83]\n"
"s_waitcnt lgkmcnt(8)\n"
"buffer_load_dwordx4 a[36:39], v18, s[56:59], s25 offen offset:1024 sc1\n"
"s_waitcnt vmcnt(63) lgkmcnt(0)\n"
"s_barrier\n"
"v_mfma_f32_16x16x32_bf16 v[96:99], a[208:211], a[160:163], v[96:99]\n"
"ds_read_b64_tr_b16 a[192:193], v4 offset:0\n"
"ds_read_b64_tr_b16 a[194:195], v4 offset:6144\n"
"v_mfma_f32_16x16x32_bf16 v[100:103], a[208:211], a[164:167], v[100:103]\n"
"v_mfma_f32_16x16x32_bf16 v[104:107], a[208:211], a[168:171], v[104:107]\n"
"v_mfma_f32_16x16x32_bf16 v[108:111], a[208:211], a[172:175], v[108:111]\n"
"buffer_load_dwordx4 a[40:43], v18, s[56:59], s25 offen offset:2048 sc1\n"
"v_mfma_f32_16x16x32_bf16 v[124:127], a[212:215], a[172:175], v[124:127]\n"
"ds_read_b64_tr_b16 a[196:197], v5 offset:0\n"
"ds_read_b64_tr_b16 a[198:199], v5 offset:6144\n"
"v_mfma_f32_16x16x32_bf16 v[120:123], a[212:215], a[168:171], v[120:123]\n"
"v_mfma_f32_16x16x32_bf16 v[116:119], a[212:215], a[164:167], v[116:119]\n"
"v_mfma_f32_16x16x32_bf16 v[112:115], a[212:215], a[160:163], v[112:115]\n"
"buffer_load_dwordx4 a[44:47], v18, s[56:59], s25 offen offset:3072 sc1\n"
"v_mfma_f32_16x16x32_bf16 v[128:131], a[216:219], a[160:163], v[128:131]\n"
"ds_read_b64_tr_b16 a[200:201], v6 offset:0\n"
"ds_read_b64_tr_b16 a[202:203], v6 offset:6144\n"
"v_mfma_f32_16x16x32_bf16 v[132:135], a[216:219], a[164:167], v[132:135]\n"
"v_mfma_f32_16x16x32_bf16 v[136:139], a[216:219], a[168:171], v[136:139]\n"
"v_mfma_f32_16x16x32_bf16 v[140:143], a[216:219], a[172:175], v[140:143]\n"
"s_add_u32 s25, s81, 4096\n"
"buffer_load_dwordx4 a[48:51], v18, s[56:59], s25 offen sc1\n"
"v_mfma_f32_16x16x32_bf16 v[156:159], a[220:223], a[172:175], v[156:159]\n"
"ds_read_b64_tr_b16 a[204:205], v7 offset:0\n"
"ds_read_b64_tr_b16 a[206:207], v7 offset:6144\n"
"v_mfma_f32_16x16x32_bf16 v[152:155], a[220:223], a[168:171], v[152:155]\n"
"v_mfma_f32_16x16x32_bf16 v[148:151], a[220:223], a[164:167], v[148:151]\n"
"v_mfma_f32_16x16x32_bf16 v[144:147], a[220:223], a[160:163], v[144:147]\n"
"s_waitcnt lgkmcnt(15)\n"
"buffer_load_dwordx4 a[52:55], v18, s[56:59], s25 offen offset:1024 sc1\n"
"v_mfma_f32_16x16x32_bf16 v[160:163], a[224:227], a[160:163], v[160:163]\n"
"ds_read_b64_tr_b16 a[208:209], v4 offset:128\n"
"ds_read_b64_tr_b16 a[210:211], v4 offset:6272\n"
"v_mfma_f32_16x16x32_bf16 v[164:167], a[224:227], a[164:167], v[164:167]\n"
"v_mfma_f32_16x16x32_bf16 v[168:171], a[224:227], a[168:171], v[168:171]\n"
"v_mfma_f32_16x16x32_bf16 v[172:175], a[224:227], a[172:175], v[172:175]\n"
"buffer_load_dwordx4 a[56:59], v18, s[56:59], s25 offen offset:2048 sc1\n"
"v_mfma_f32_16x16x32_bf16 v[188:191], a[228:231], a[172:175], v[188:191]\n"
"ds_read_b64_tr_b16 a[212:213], v5 offset:128\n"
"ds_read_b64_tr_b16 a[214:215], v5 offset:6272\n"
"v_mfma_f32_16x16x32_bf16 v[184:187], a[228:231], a[168:171], v[184:187]\n"
"v_mfma_f32_16x16x32_bf16 v[180:183], a[228:231], a[164:167], v[180:183]\n"
"v_mfma_f32_16x16x32_bf16 v[176:179], a[228:231], a[160:163], v[176:179]\n"
"buffer_load_dwordx4 a[60:63], v18, s[56:59], s25 offen offset:3072 sc1\n"
"v_mfma_f32_16x16x32_bf16 v[192:195], a[232:235], a[160:163], v[192:195]\n"
"ds_read_b64_tr_b16 a[216:217], v6 offset:128\n"
"ds_read_b64_tr_b16 a[218:219], v6 offset:6272\n"
"v_mfma_f32_16x16x32_bf16 v[196:199], a[232:235], a[164:167], v[196:199]\n"
"v_mfma_f32_16x16x32_bf16 v[200:203], a[232:235], a[168:171], v[200:203]\n"
"v_mfma_f32_16x16x32_bf16 v[204:207], a[232:235], a[172:175], v[204:207]\n"
"s_add_u32 s25, s82, 4096\n"
"buffer_load_dwordx4 a[64:67], v18, s[56:59], s25 offen sc1\n"
"v_mfma_f32_16x16x32_bf16 v[220:223], a[236:239], a[172:175], v[220:223]\n"
"ds_read_b64_tr_b16 a[220:221], v7 offset:128\n"
"ds_read_b64_tr_b16 a[222:223], v7 offset:6272\n"
"v_mfma_f32_16x16x32_bf16 v[216:219], a[236:239], a[168:171], v[216:219]\n"
"v_mfma_f32_16x16x32_bf16 v[212:215], a[236:239], a[164:167], v[212:215]\n"
"s_waitcnt vmcnt(13)\n"
"v_mfma_f32_16x16x32_bf16 v[208:211], a[236:239], a[160:163], v[208:211]\n"
"s_waitcnt lgkmcnt(8)\n"
"buffer_load_dwordx4 a[68:71], v18, s[56:59], s25 offen offset:1024 sc1\n"
"v_mfma_f32_16x16x32_bf16 v[64:67], a[192:195], a[0:3], v[64:67]\n"
"ds_read_b64_tr_b16 a[224:225], v4 offset:256\n"
"ds_read_b64_tr_b16 a[226:227], v4 offset:6400\n"
"v_mfma_f32_16x16x32_bf16 v[68:71], a[192:195], a[4:7], v[68:71]\n"
"v_mfma_f32_16x16x32_bf16 v[72:75], a[192:195], a[8:11], v[72:75]\n"
"v_mfma_f32_16x16x32_bf16 v[76:79], a[192:195], a[12:15], v[76:79]\n"
"buffer_load_dwordx4 a[72:75], v18, s[56:59], s25 offen offset:2048 sc1\n"
"v_mfma_f32_16x16x32_bf16 v[92:95], a[196:199], a[12:15], v[92:95]\n"
"ds_read_b64_tr_b16 a[228:229], v5 offset:256\n"
"ds_read_b64_tr_b16 a[230:231], v5 offset:6400\n"
"v_mfma_f32_16x16x32_bf16 v[88:91], a[196:199], a[8:11], v[88:91]\n"
"v_mfma_f32_16x16x32_bf16 v[84:87], a[196:199], a[4:7], v[84:87]\n"
"v_mfma_f32_16x16x32_bf16 v[80:83], a[196:199], a[0:3], v[80:83]\n"
"buffer_load_dwordx4 a[76:79], v18, s[56:59], s25 offen offset:3072 sc1\n"
"v_mfma_f32_16x16x32_bf16 v[96:99], a[200:203], a[0:3], v[96:99]\n"
"ds_read_b64_tr_b16 a[232:233], v0 offset:0\n"
"ds_read_b64_tr_b16 a[234:235], v0 offset:6144\n"
"v_mfma_f32_16x16x32_bf16 v[100:103], a[200:203], a[4:7], v[100:103]\n"
"v_mfma_f32_16x16x32_bf16 v[104:107], a[200:203], a[8:11], v[104:107]\n"
"v_mfma_f32_16x16x32_bf16 v[108:111], a[200:203], a[12:15], v[108:111]\n"
"s_add_u32 s25, s83, 4096\n"
"buffer_load_dwordx4 a[80:83], v18, s[56:59], s25 offen sc1\n"
"v_mfma_f32_16x16x32_bf16 v[124:127], a[204:207], a[12:15], v[124:127]\n"
"ds_read_b64_tr_b16 a[236:237], v1 offset:0\n"
"ds_read_b64_tr_b16 a[238:239], v1 offset:6144\n"
"v_mfma_f32_16x16x32_bf16 v[120:123], a[204:207], a[8:11], v[120:123]\n"
"v_mfma_f32_16x16x32_bf16 v[116:119], a[204:207], a[4:7], v[116:119]\n"
"v_mfma_f32_16x16x32_bf16 v[112:115], a[204:207], a[0:3], v[112:115]\n"
"s_waitcnt lgkmcnt(8)\n"
"buffer_load_dwordx4 a[84:87], v18, s[56:59], s25 offen offset:1024 sc1\n"
"v_mfma_f32_16x16x32_bf16 v[128:131], a[208:211], a[0:3], v[128:131]\n"
"ds_read_b64_tr_b16 a[192:193], v2 offset:0\n"
"ds_read_b64_tr_b16 a[194:195], v2 offset:6144\n"
"v_mfma_f32_16x16x32_bf16 v[132:135], a[208:211], a[4:7], v[132:135]\n"
"v_mfma_f32_16x16x32_bf16 v[136:139], a[208:211], a[8:11], v[136:139]\n"
"v_mfma_f32_16x16x32_bf16 v[140:143], a[208:211], a[12:15], v[140:143]\n"
"buffer_load_dwordx4 a[88:91], v18, s[56:59], s25 offen offset:2048 sc1\n"
"v_mfma_f32_16x16x32_bf16 v[156:159], a[212:215], a[12:15], v[156:159]\n"
"ds_read_b64_tr_b16 a[196:197], v3 offset:0\n"
"ds_read_b64_tr_b16 a[198:199], v3 offset:6144\n"
"v_mfma_f32_16x16x32_bf16 v[152:155], a[212:215], a[8:11], v[152:155]\n"
"v_mfma_f32_16x16x32_bf16 v[148:151], a[212:215], a[4:7], v[148:151]\n"
"v_mfma_f32_16x16x32_bf16 v[144:147], a[212:215], a[0:3], v[144:147]\n"
"buffer_load_dwordx4 a[92:95], v18, s[56:59], s25 offen offset:3072 sc1\n"
"v_mfma_f32_16x16x32_bf16 v[160:163], a[216:219], a[0:3], v[160:163]\n"
"ds_read_b64_tr_b16 a[200:201], v0 offset:128\n"
"ds_read_b64_tr_b16 a[202:203], v0 offset:6272\n"
"v_mfma_f32_16x16x32_bf16 v[164:167], a[216:219], a[4:7], v[164:167]\n"
"v_mfma_f32_16x16x32_bf16 v[168:171], a[216:219], a[8:11], v[168:171]\n"
"v_mfma_f32_16x16x32_bf16 v[172:175], a[216:219], a[12:15], v[172:175]\n"
"s_and_b32 s30, 15, s93\n"
"v_add_u32 v12, 2, v61\n"
"v_mfma_f32_16x16x32_bf16 v[188:191], a[220:223], a[12:15], v[188:191]\n"
"ds_read_b64_tr_b16 a[204:205], v1 offset:128\n"
"ds_read_b64_tr_b16 a[206:207], v1 offset:6272\n"
"v_mfma_f32_16x16x32_bf16 v[184:187], a[220:223], a[8:11], v[184:187]\n"
"v_cndmask_b32_e64 v13, 2.0, v60, s[30:31]\n"
"buffer_store_dword v12, v13, s[72:75], 0 offen sc1\n"
"v_mfma_f32_16x16x32_bf16 v[180:183], a[220:223], a[4:7], v[180:183]\n"
"s_add_u32 s25, s81, 8192\n"
"buffer_load_dwordx4 a[96:99], v18, s[56:59], s25 offen sc1\n"
"v_mfma_f32_16x16x32_bf16 v[176:179], a[220:223], a[0:3], v[176:179]\n"
"s_waitcnt lgkmcnt(8)\n"
"v_mfma_f32_16x16x32_bf16 v[192:195], a[224:227], a[0:3], v[192:195]\n"
"ds_read_b64_tr_b16 a[208:209], v2 offset:128\n"
"ds_read_b64_tr_b16 a[210:211], v2 offset:6272\n"
"v_mfma_f32_16x16x32_bf16 v[196:199], a[224:227], a[4:7], v[196:199]\n"
"v_mfma_f32_16x16x32_bf16 v[200:203], a[224:227], a[8:11], v[200:203]\n"
"buffer_load_dwordx4 a[100:103], v18, s[56:59], s25 offen offset:1024 sc1\n"
"v_mfma_f32_16x16x32_bf16 v[204:207], a[224:227], a[12:15], v[204:207]\n"
"v_mfma_f32_16x16x32_bf16 v[220:223], a[228:231], a[12:15], v[220:223]\n"
"ds_read_b64_tr_b16 a[212:213], v3 offset:128\n"
"ds_read_b64_tr_b16 a[214:215], v3 offset:6272\n"
"v_mfma_f32_16x16x32_bf16 v[216:219], a[228:231], a[8:11], v[216:219]\n"
"v_mfma_f32_16x16x32_bf16 v[212:215], a[228:231], a[4:7], v[212:215]\n"
"s_waitcnt vmcnt(19)\n"
"buffer_load_dwordx4 a[104:107], v18, s[56:59], s25 offen offset:2048 sc1\n"
"v_mfma_f32_16x16x32_bf16 v[208:211], a[228:231], a[0:3], v[208:211]\n"
"v_mfma_f32_16x16x32_bf16 v[64:67], a[232:235], a[16:19], v[64:67]\n"
"ds_read_b64_tr_b16 a[216:217], v0 offset:256\n"
"ds_read_b64_tr_b16 a[218:219], v0 offset:6400\n"
"v_mfma_f32_16x16x32_bf16 v[68:71], a[232:235], a[20:23], v[68:71]\n"
"v_mfma_f32_16x16x32_bf16 v[72:75], a[232:235], a[24:27], v[72:75]\n"
"buffer_load_dwordx4 a[108:111], v18, s[56:59], s25 offen offset:3072 sc1\n"
"v_mfma_f32_16x16x32_bf16 v[76:79], a[232:235], a[28:31], v[76:79]\n"
"v_mfma_f32_16x16x32_bf16 v[92:95], a[236:239], a[28:31], v[92:95]\n"
"ds_read_b64_tr_b16 a[220:221], v1 offset:256\n"
"ds_read_b64_tr_b16 a[222:223], v1 offset:6400\n"
"v_mfma_f32_16x16x32_bf16 v[88:91], a[236:239], a[24:27], v[88:91]\n"
"v_mfma_f32_16x16x32_bf16 v[84:87], a[236:239], a[20:23], v[84:87]\n"
"s_add_u32 s24, s87, 81920\n"
"s_add_u32 m0, s76, 36864\n"
"v_mfma_f32_16x16x32_bf16 v[80:83], a[236:239], a[16:19], v[80:83]\n"
"s_waitcnt lgkmcnt(8)\n"
"buffer_load_dwordx4 v56, s[60:63], s24 offen lds\n"
"v_mfma_f32_16x16x32_bf16 v[96:99], a[192:195], a[16:19], v[96:99]\n"
"ds_read_b64_tr_b16 a[224:225], v0 offset:12288\n"
"ds_read_b64_tr_b16 a[226:227], v0 offset:18432\n"
"v_mfma_f32_16x16x32_bf16 v[100:103], a[192:195], a[20:23], v[100:103]\n"
"v_mfma_f32_16x16x32_bf16 v[104:107], a[192:195], a[24:27], v[104:107]\n"
"v_mfma_f32_16x16x32_bf16 v[108:111], a[192:195], a[28:31], v[108:111]\n"
"buffer_load_dwordx4 v57, s[60:63], s24 offen offset:1024 lds\n"
"v_mfma_f32_16x16x32_bf16 v[124:127], a[196:199], a[28:31], v[124:127]\n"
"ds_read_b64_tr_b16 a[228:229], v1 offset:12288\n"
"ds_read_b64_tr_b16 a[230:231], v1 offset:18432\n"
"v_mfma_f32_16x16x32_bf16 v[120:123], a[196:199], a[24:27], v[120:123]\n"
"v_mfma_f32_16x16x32_bf16 v[116:119], a[196:199], a[20:23], v[116:119]\n"
"v_mfma_f32_16x16x32_bf16 v[112:115], a[196:199], a[16:19], v[112:115]\n"
"buffer_load_dwordx4 v58, s[60:63], s24 offen offset:2048 lds\n"
"v_mfma_f32_16x16x32_bf16 v[128:131], a[200:203], a[16:19], v[128:131]\n"
"ds_read_b64_tr_b16 a[232:233], v2 offset:12288\n"
"ds_read_b64_tr_b16 a[234:235], v2 offset:18432\n"
"v_mfma_f32_16x16x32_bf16 v[132:135], a[200:203], a[20:23], v[132:135]\n"
"v_mfma_f32_16x16x32_bf16 v[136:139], a[200:203], a[24:27], v[136:139]\n"
"v_mfma_f32_16x16x32_bf16 v[140:143], a[200:203], a[28:31], v[140:143]\n"
"s_add_u32 s25, s82, 8192\n"
"buffer_load_dwordx4 a[112:115], v18, s[56:59], s25 offen sc1\n"
"v_mfma_f32_16x16x32_bf16 v[156:159], a[204:207], a[28:31], v[156:159]\n"
"ds_read_b64_tr_b16 a[236:237], v3 offset:12288\n"
"ds_read_b64_tr_b16 a[238:239], v3 offset:18432\n"
"v_mfma_f32_16x16x32_bf16 v[152:155], a[204:207], a[24:27], v[152:155]\n"
"v_mfma_f32_16x16x32_bf16 v[148:151], a[204:207], a[20:23], v[148:151]\n"
"v_mfma_f32_16x16x32_bf16 v[144:147], a[204:207], a[16:19], v[144:147]\n"
"s_waitcnt lgkmcnt(8)\n"
"buffer_load_dwordx4 a[116:119], v18, s[56:59], s25 offen offset:1024 sc1\n"
"v_mfma_f32_16x16x32_bf16 v[160:163], a[208:211], a[16:19], v[160:163]\n"
"ds_read_b64_tr_b16 a[192:193], v0 offset:12416\n"
"ds_read_b64_tr_b16 a[194:195], v0 offset:18560\n"
"v_mfma_f32_16x16x32_bf16 v[164:167], a[208:211], a[20:23], v[164:167]\n"
"v_mfma_f32_16x16x32_bf16 v[168:171], a[208:211], a[24:27], v[168:171]\n"
"v_mfma_f32_16x16x32_bf16 v[172:175], a[208:211], a[28:31], v[172:175]\n"
"buffer_load_dwordx4 a[120:123], v18, s[56:59], s25 offen offset:2048 sc1\n"
"v_mfma_f32_16x16x32_bf16 v[188:191], a[212:215], a[28:31], v[188:191]\n"
"ds_read_b64_tr_b16 a[196:197], v1 offset:12416\n"
"ds_read_b64_tr_b16 a[198:199], v1 offset:18560\n"
"v_mfma_f32_16x16x32_bf16 v[184:187], a[212:215], a[24:27], v[184:187]\n"
"v_mfma_f32_16x16x32_bf16 v[180:183], a[212:215], a[20:23], v[180:183]\n"
"v_mfma_f32_16x16x32_bf16 v[176:179], a[212:215], a[16:19], v[176:179]\n"
"buffer_load_dwordx4 a[124:127], v18, s[56:59], s25 offen offset:3072 sc1\n"
"v_mfma_f32_16x16x32_bf16 v[192:195], a[216:219], a[16:19], v[192:195]\n"
"ds_read_b64_tr_b16 a[200:201], v2 offset:12416\n"
"ds_read_b64_tr_b16 a[202:203], v2 offset:18560\n"
"v_mfma_f32_16x16x32_bf16 v[196:199], a[216:219], a[20:23], v[196:199]\n"
"v_mfma_f32_16x16x32_bf16 v[200:203], a[216:219], a[24:27], v[200:203]\n"
"v_mfma_f32_16x16x32_bf16 v[204:207], a[216:219], a[28:31], v[204:207]\n"
"s_add_u32 s24, s85, 122880\n"
"s_add_u32 m0, s76, 98304\n"
"v_mfma_f32_16x16x32_bf16 v[220:223], a[220:223], a[28:31], v[220:223]\n"
"ds_read_b64_tr_b16 a[204:205], v3 offset:12416\n"
"ds_read_b64_tr_b16 a[206:207], v3 offset:18560\n"
"v_mfma_f32_16x16x32_bf16 v[216:219], a[220:223], a[24:27], v[216:219]\n"
"buffer_load_dwordx4 v56, s[60:63], s24 offen lds\n"
"v_mfma_f32_16x16x32_bf16 v[212:215], a[220:223], a[20:23], v[212:215]\n"
"s_waitcnt vmcnt(25)\n"
"v_mfma_f32_16x16x32_bf16 v[208:211], a[220:223], a[16:19], v[208:211]\n"
"s_waitcnt lgkmcnt(8)\n"
"v_mfma_f32_16x16x32_bf16 v[64:67], a[224:227], a[32:35], v[64:67]\n"
"ds_read_b64_tr_b16 a[208:209], v0 offset:12544\n"
"ds_read_b64_tr_b16 a[210:211], v0 offset:18688\n"
"v_mfma_f32_16x16x32_bf16 v[68:71], a[224:227], a[36:39], v[68:71]\n"
"buffer_load_dwordx4 v57, s[60:63], s24 offen offset:1024 lds\n"
"v_mfma_f32_16x16x32_bf16 v[72:75], a[224:227], a[40:43], v[72:75]\n"
"v_mfma_f32_16x16x32_bf16 v[76:79], a[224:227], a[44:47], v[76:79]\n"
"v_mfma_f32_16x16x32_bf16 v[92:95], a[228:231], a[44:47], v[92:95]\n"
"ds_read_b64_tr_b16 a[212:213], v1 offset:12544\n"
"ds_read_b64_tr_b16 a[214:215], v1 offset:18688\n"
"v_mfma_f32_16x16x32_bf16 v[88:91], a[228:231], a[40:43], v[88:91]\n"
"buffer_load_dwordx4 v58, s[60:63], s24 offen offset:2048 lds\n"
"v_mfma_f32_16x16x32_bf16 v[84:87], a[228:231], a[36:39], v[84:87]\n"
"v_mfma_f32_16x16x32_bf16 v[80:83], a[228:231], a[32:35], v[80:83]\n"
"v_mfma_f32_16x16x32_bf16 v[96:99], a[232:235], a[32:35], v[96:99]\n"
"ds_read_b64_tr_b16 a[216:217], v8 offset:12288\n"
"ds_read_b64_tr_b16 a[218:219], v8 offset:18432\n"
"v_mfma_f32_16x16x32_bf16 v[100:103], a[232:235], a[36:39], v[100:103]\n"
"s_add_u32 s25, s83, 8192\n"
"buffer_load_dwordx4 a[128:131], v18, s[56:59], s25 offen sc1\n"
"v_mfma_f32_16x16x32_bf16 v[104:107], a[232:235], a[40:43], v[104:107]\n"
"v_mfma_f32_16x16x32_bf16 v[108:111], a[232:235], a[44:47], v[108:111]\n"
"v_mfma_f32_16x16x32_bf16 v[124:127], a[236:239], a[44:47], v[124:127]\n"
"ds_read_b64_tr_b16 a[220:221], v9 offset:12288\n"
"ds_read_b64_tr_b16 a[222:223], v9 offset:18432\n"
"v_mfma_f32_16x16x32_bf16 v[120:123], a[236:239], a[40:43], v[120:123]\n"
"buffer_load_dwordx4 a[132:135], v18, s[56:59], s25 offen offset:1024 sc1\n"
"v_mfma_f32_16x16x32_bf16 v[116:119], a[236:239], a[36:39], v[116:119]\n"
"v_mfma_f32_16x16x32_bf16 v[112:115], a[236:239], a[32:35], v[112:115]\n"
"s_waitcnt lgkmcnt(8)\n"
"v_mfma_f32_16x16x32_bf16 v[128:131], a[192:195], a[32:35], v[128:131]\n"
"ds_read_b64_tr_b16 a[224:225], v10 offset:12288\n"
"ds_read_b64_tr_b16 a[226:227], v10 offset:18432\n"
"v_mfma_f32_16x16x32_bf16 v[132:135], a[192:195], a[36:39], v[132:135]\n"
"buffer_load_dwordx4 a[136:139], v18, s[56:59], s25 offen offset:2048 sc1\n"
"v_mfma_f32_16x16x32_bf16 v[136:139], a[192:195], a[40:43], v[136:139]\n"
"v_mfma_f32_16x16x32_bf16 v[140:143], a[192:195], a[44:47], v[140:143]\n"
"v_mfma_f32_16x16x32_bf16 v[156:159], a[196:199], a[44:47], v[156:159]\n"
"ds_read_b64_tr_b16 a[228:229], v11 offset:12288\n"
"ds_read_b64_tr_b16 a[230:231], v11 offset:18432\n"
"v_mfma_f32_16x16x32_bf16 v[152:155], a[196:199], a[40:43], v[152:155]\n"
"buffer_load_dwordx4 a[140:143], v18, s[56:59], s25 offen offset:3072 sc1\n"
"v_lshrrev_b32 v24, 1, v18\n"
"v_mfma_f32_16x16x32_bf16 v[148:151], a[196:199], a[36:39], v[148:151]\n"
"s_lshl_b32 s13, s78, 9\n"
"v_bfe_u32 v26, v18, 6, 2\n"
"v_mfma_f32_16x16x32_bf16 v[144:147], a[196:199], a[32:35], v[144:147]\n"
"v_lshrrev_b32 v27, 8, v18\n"
"v_cmp_eq_u32 vcc, v26, v27\n"
"v_mfma_f32_16x16x32_bf16 v[160:163], a[200:203], a[32:35], v[160:163]\n"
"ds_read_b64_tr_b16 a[232:233], v8 offset:12416\n"
"ds_read_b64_tr_b16 a[234:235], v8 offset:18560\n"
"v_mfma_f32_16x16x32_bf16 v[164:167], a[200:203], a[36:39], v[164:167]\n"
"s_add_u32 s24, s86, 122880\n"
"s_add_u32 m0, s76, 49152\n"
"v_mfma_f32_16x16x32_bf16 v[168:171], a[200:203], a[40:43], v[168:171]\n"
"buffer_load_dwordx4 v56, s[60:63], s24 offen lds\n"
"v_and_b32 v26, 16, v18\n"
"v_mfma_f32_16x16x32_bf16 v[172:175], a[200:203], a[44:47], v[172:175]\n"
"v_mov_b32 v27, 16256\n"
"v_lshlrev_b32 v27, v26, v27\n"
"v_mfma_f32_16x16x32_bf16 v[188:191], a[204:207], a[44:47], v[188:191]\n"
"ds_read_b64_tr_b16 a[236:237], v9 offset:12416\n"
"ds_read_b64_tr_b16 a[238:239], v9 offset:18560\n"
"v_mfma_f32_16x16x32_bf16 v[184:187], a[204:207], a[40:43], v[184:187]\n"
"v_cndmask_b32 v27, 0, v27, vcc\n"
"v_and_b32 v26, 32, v18\n"
"v_mfma_f32_16x16x32_bf16 v[180:183], a[204:207], a[36:39], v[180:183]\n"
"buffer_load_dwordx4 v57, s[60:63], s24 offen offset:1024 lds\n"
"v_cmp_eq_u32 vcc, 0, v26\n"
"v_mfma_f32_16x16x32_bf16 v[176:179], a[204:207], a[32:35], v[176:179]\n"
"s_waitcnt lgkmcnt(8)\n"
"v_mov_b32 v32, 0\n"
"v_mfma_f32_16x16x32_bf16 v[192:195], a[208:211], a[32:35], v[192:195]\n"
"ds_read_b64_tr_b16 a[192:193], v10 offset:12416\n"
"ds_read_b64_tr_b16 a[194:195], v10 offset:18560\n"
"v_mfma_f32_16x16x32_bf16 v[196:199], a[208:211], a[36:39], v[196:199]\n"
"v_mov_b32 v33, 0\n"
"v_cndmask_b32 v34, 0, v27, vcc\n"
"v_mfma_f32_16x16x32_bf16 v[200:203], a[208:211], a[40:43], v[200:203]\n"
"buffer_load_dwordx4 v58, s[60:63], s24 offen offset:2048 lds\n"
"v_sub_u32 v35, v27, v34\n"
"v_mfma_f32_16x16x32_bf16 v[204:207], a[208:211], a[44:47], v[204:207]\n"
"v_mov_b32 v38, 0\n"
"v_mov_b32 v39, 0\n"
"v_mfma_f32_16x16x32_bf16 v[220:223], a[212:215], a[44:47], v[220:223]\n"
"ds_read_b64_tr_b16 a[196:197], v11 offset:12416\n"
"ds_read_b64_tr_b16 a[198:199], v11 offset:18560\n"
"v_mfma_f32_16x16x32_bf16 v[216:219], a[212:215], a[40:43], v[216:219]\n"
"v_mov_b32 v36, v34\n"
"v_mov_b32 v37, v35\n"
"v_mfma_f32_16x16x32_bf16 v[212:215], a[212:215], a[36:39], v[212:215]\n"
"s_waitcnt vmcnt(30)\n"
"s_add_u32 s25, s81, 12288\n"
"v_mfma_f32_16x16x32_bf16 v[208:211], a[212:215], a[32:35], v[208:211]\n"
"buffer_load_dwordx4 a[144:147], v18, s[56:59], s25 offen sc1\n"
"s_lshl_b32 s95, s78, 15\n"
"v_mfma_f32_16x16x32_bf16 v[64:67], a[216:219], a[48:51], v[64:67]\n"
"ds_read_b64_tr_b16 a[200:201], v8 offset:12544\n"
"ds_read_b64_tr_b16 a[202:203], v8 offset:18688\n"
"v_mfma_f32_16x16x32_bf16 v[68:71], a[216:219], a[52:55], v[68:71]\n"
"s_lshl_b32 s8, s78, 5\n"
"s_add_u32 s9, s8, 20480\n"
"v_mfma_f32_16x16x32_bf16 v[72:75], a[216:219], a[56:59], v[72:75]\n"
"s_add_u32 s10, s8, 40960\n"
"s_add_u32 s11, s8, 61440\n"
"v_mfma_f32_16x16x32_bf16 v[76:79], a[216:219], a[60:63], v[76:79]\n"
"buffer_load_dwordx4 a[148:151], v18, s[56:59], s25 offen offset:1024 sc1\n"
"s_and_b32 s34, s78, 3\n"
"v_mfma_f32_16x16x32_bf16 v[92:95], a[220:223], a[60:63], v[92:95]\n"
"ds_read_b64_tr_b16 a[204:205], v9 offset:12544\n"
"ds_read_b64_tr_b16 a[206:207], v9 offset:18688\n"
"v_mfma_f32_16x16x32_bf16 v[88:91], a[220:223], a[56:59], v[88:91]\n"
"s_mul_i32 s12, s34, 20480\n"
"s_lshr_b32 s34, s78, 2\n"
"v_mfma_f32_16x16x32_bf16 v[84:87], a[220:223], a[52:55], v[84:87]\n"
"s_lshl_b32 s34, s34, 5\n"
"s_add_u32 s12, s12, s34\n"
"v_mfma_f32_16x16x32_bf16 v[80:83], a[220:223], a[48:51], v[80:83]\n"
"s_waitcnt lgkmcnt(8)\n"
"buffer_load_dwordx4 a[152:155], v18, s[56:59], s25 offen offset:2048 sc1\n"
"v_mfma_f32_16x16x32_bf16 v[96:99], a[224:227], a[48:51], v[96:99]\n"
"ds_read_b64_tr_b16 a[208:209], v8 offset:24576\n"
"ds_read_b64_tr_b16 a[210:211], v8 offset:30720\n"
"v_mfma_f32_16x16x32_bf16 v[100:103], a[224:227], a[52:55], v[100:103]\n"
"s_add_u32 s12, s12, 256\n"
"s_add_u32 s14, s95, 0\n"
"v_mfma_f32_16x16x32_bf16 v[104:107], a[224:227], a[56:59], v[104:107]\n"
"s_add_u32 s15, s95, 4096\n"
"s_add_u32 s16, s95, 8192\n"
"v_mfma_f32_16x16x32_bf16 v[108:111], a[224:227], a[60:63], v[108:111]\n"
"buffer_load_dwordx4 a[156:159], v18, s[56:59], s25 offen offset:3072 sc1\n"
"s_add_u32 s17, s95, 12288\n"
"v_mfma_f32_16x16x32_bf16 v[124:127], a[228:231], a[60:63], v[124:127]\n"
"ds_read_b64_tr_b16 a[212:213], v9 offset:24576\n"
"ds_read_b64_tr_b16 a[214:215], v9 offset:30720\n"
"v_mfma_f32_16x16x32_bf16 v[120:123], a[228:231], a[56:59], v[120:123]\n"
"s_add_u32 s18, s95, 16384\n"
"v_mfma_f32_16x16x32_bf16 v[116:119], a[228:231], a[52:55], v[116:119]\n"
"v_mfma_f32_16x16x32_bf16 v[112:115], a[228:231], a[48:51], v[112:115]\n"
"s_add_u32 s24, s87, 122880\n"
"s_add_u32 m0, s76, 110592\n"
"v_mfma_f32_16x16x32_bf16 v[128:131], a[232:235], a[48:51], v[128:131]\n"
"ds_read_b64_tr_b16 a[216:217], v10 offset:24576\n"
"ds_read_b64_tr_b16 a[218:219], v10 offset:30720\n"
"v_mfma_f32_16x16x32_bf16 v[132:135], a[232:235], a[52:55], v[132:135]\n"
"buffer_load_dwordx4 v56, s[60:63], s24 offen lds\n"
"v_mfma_f32_16x16x32_bf16 v[136:139], a[232:235], a[56:59], v[136:139]\n"
"v_mfma_f32_16x16x32_bf16 v[140:143], a[232:235], a[60:63], v[140:143]\n"
"v_mfma_f32_16x16x32_bf16 v[156:159], a[236:239], a[60:63], v[156:159]\n"
"ds_read_b64_tr_b16 a[220:221], v11 offset:24576\n"
"ds_read_b64_tr_b16 a[222:223], v11 offset:30720\n"
"v_mfma_f32_16x16x32_bf16 v[152:155], a[236:239], a[56:59], v[152:155]\n"
"buffer_load_dwordx4 v57, s[60:63], s24 offen offset:1024 lds\n"
"v_mfma_f32_16x16x32_bf16 v[148:151], a[236:239], a[52:55], v[148:151]\n"
"v_mfma_f32_16x16x32_bf16 v[144:147], a[236:239], a[48:51], v[144:147]\n"
"s_waitcnt lgkmcnt(8)\n"
"v_mfma_f32_16x16x32_bf16 v[160:163], a[192:195], a[48:51], v[160:163]\n"
"ds_read_b64_tr_b16 a[224:225], v8 offset:24704\n"
"ds_read_b64_tr_b16 a[226:227], v8 offset:30848\n"
"v_mfma_f32_16x16x32_bf16 v[164:167], a[192:195], a[52:55], v[164:167]\n"
"buffer_load_dwordx4 v58, s[60:63], s24 offen offset:2048 lds\n"
"v_mfma_f32_16x16x32_bf16 v[168:171], a[192:195], a[56:59], v[168:171]\n"
"v_mfma_f32_16x16x32_bf16 v[172:175], a[192:195], a[60:63], v[172:175]\n"
"v_mfma_f32_16x16x32_bf16 v[188:191], a[196:199], a[60:63], v[188:191]\n"
"ds_read_b64_tr_b16 a[228:229], v9 offset:24704\n"
"ds_read_b64_tr_b16 a[230:231], v9 offset:30848\n"
"v_mfma_f32_16x16x32_bf16 v[184:187], a[196:199], a[56:59], v[184:187]\n"
"s_add_u32 s25, s82, 12288\n"
"buffer_load_dwordx4 a[160:163], v18, s[56:59], s25 offen sc1\n"
"v_mfma_f32_16x16x32_bf16 v[180:183], a[196:199], a[52:55], v[180:183]\n"
"v_mfma_f32_16x16x32_bf16 v[176:179], a[196:199], a[48:51], v[176:179]\n"
"v_mfma_f32_16x16x32_bf16 v[192:195], a[200:203], a[48:51], v[192:195]\n"
"ds_read_b64_tr_b16 a[232:233], v10 offset:24704\n"
"ds_read_b64_tr_b16 a[234:235], v10 offset:30848\n"
"v_mfma_f32_16x16x32_bf16 v[196:199], a[200:203], a[52:55], v[196:199]\n"
"buffer_load_dwordx4 a[164:167], v18, s[56:59], s25 offen offset:1024 sc1\n"
"v_mfma_f32_16x16x32_bf16 v[200:203], a[200:203], a[56:59], v[200:203]\n"
"v_mfma_f32_16x16x32_bf16 v[204:207], a[200:203], a[60:63], v[204:207]\n"
"v_mfma_f32_16x16x32_bf16 v[220:223], a[204:207], a[60:63], v[220:223]\n"
"ds_read_b64_tr_b16 a[236:237], v11 offset:24704\n"
"ds_read_b64_tr_b16 a[238:239], v11 offset:30848\n"
"v_mfma_f32_16x16x32_bf16 v[216:219], a[204:207], a[56:59], v[216:219]\n"
"buffer_load_dwordx4 a[168:171], v18, s[56:59], s25 offen offset:2048 sc1\n"
"v_mfma_f32_16x16x32_bf16 v[212:215], a[204:207], a[52:55], v[212:215]\n"
"s_waitcnt vmcnt(36)\n"
"v_mfma_f32_16x16x32_bf16 v[208:211], a[204:207], a[48:51], v[208:211]\n"
"s_waitcnt lgkmcnt(8)\n"
"v_mfma_f32_16x16x32_bf16 v[64:67], a[208:211], a[64:67], v[64:67]\n"
"ds_read_b64_tr_b16 a[192:193], v8 offset:24832\n"
"ds_read_b64_tr_b16 a[194:195], v8 offset:30976\n"
"v_mfma_f32_16x16x32_bf16 v[68:71], a[208:211], a[68:71], v[68:71]\n"
"buffer_load_dwordx4 a[172:175], v18, s[56:59], s25 offen offset:3072 sc1\n"
"v_mfma_f32_16x16x32_bf16 v[72:75], a[208:211], a[72:75], v[72:75]\n"
"v_mfma_f32_16x16x32_bf16 v[76:79], a[208:211], a[76:79], v[76:79]\n"
"v_mfma_f32_16x16x32_bf16 v[92:95], a[212:215], a[76:79], v[92:95]\n"
"ds_read_b64_tr_b16 a[196:197], v9 offset:24832\n"
"ds_read_b64_tr_b16 a[198:199], v9 offset:30976\n"
"v_mfma_f32_16x16x32_bf16 v[88:91], a[212:215], a[72:75], v[88:91]\n"
"s_add_u32 s25, s83, 12288\n"
"buffer_load_dwordx4 a[176:179], v18, s[56:59], s25 offen sc1\n"
"v_mfma_f32_16x16x32_bf16 v[84:87], a[212:215], a[68:71], v[84:87]\n"
"v_mfma_f32_16x16x32_bf16 v[80:83], a[212:215], a[64:67], v[80:83]\n"
"v_mfma_f32_16x16x32_bf16 v[96:99], a[216:219], a[64:67], v[96:99]\n"
"ds_read_b64_tr_b16 a[200:201], v4 offset:12288\n"
"ds_read_b64_tr_b16 a[202:203], v4 offset:18432\n"
"v_mfma_f32_16x16x32_bf16 v[100:103], a[216:219], a[68:71], v[100:103]\n"
"buffer_load_dwordx4 a[180:183], v18, s[56:59], s25 offen offset:1024 sc1\n"
"v_mfma_f32_16x16x32_bf16 v[104:107], a[216:219], a[72:75], v[104:107]\n"
"v_mfma_f32_16x16x32_bf16 v[108:111], a[216:219], a[76:79], v[108:111]\n"
"v_mfma_f32_16x16x32_bf16 v[124:127], a[220:223], a[76:79], v[124:127]\n"
"ds_read_b64_tr_b16 a[204:205], v5 offset:12288\n"
"ds_read_b64_tr_b16 a[206:207], v5 offset:18432\n"
"v_mfma_f32_16x16x32_bf16 v[120:123], a[220:223], a[72:75], v[120:123]\n"
"buffer_load_dwordx4 a[184:187], v18, s[56:59], s25 offen offset:2048 sc1\n"
"v_mfma_f32_16x16x32_bf16 v[116:119], a[220:223], a[68:71], v[116:119]\n"
"v_mfma_f32_16x16x32_bf16 v[112:115], a[220:223], a[64:67], v[112:115]\n"
"s_waitcnt lgkmcnt(8)\n"
"v_mfma_f32_16x16x32_bf16 v[128:131], a[224:227], a[64:67], v[128:131]\n"
"ds_read_b64_tr_b16 a[208:209], v6 offset:12288\n"
"ds_read_b64_tr_b16 a[210:211], v6 offset:18432\n"
"v_mfma_f32_16x16x32_bf16 v[132:135], a[224:227], a[68:71], v[132:135]\n"
"buffer_load_dwordx4 a[188:191], v18, s[56:59], s25 offen offset:3072 sc1\n"
"v_mfma_f32_16x16x32_bf16 v[136:139], a[224:227], a[72:75], v[136:139]\n"
"v_mfma_f32_16x16x32_bf16 v[140:143], a[224:227], a[76:79], v[140:143]\n"
"v_mfma_f32_16x16x32_bf16 v[156:159], a[228:231], a[76:79], v[156:159]\n"
"ds_read_b64_tr_b16 a[212:213], v7 offset:12288\n"
"ds_read_b64_tr_b16 a[214:215], v7 offset:18432\n"
"v_mfma_f32_16x16x32_bf16 v[152:155], a[228:231], a[72:75], v[152:155]\n"
"v_mfma_f32_16x16x32_bf16 v[148:151], a[228:231], a[68:71], v[148:151]\n"
"v_mfma_f32_16x16x32_bf16 v[144:147], a[228:231], a[64:67], v[144:147]\n"
"v_mfma_f32_16x16x32_bf16 v[160:163], a[232:235], a[64:67], v[160:163]\n"
"ds_read_b64_tr_b16 a[216:217], v4 offset:12416\n"
"ds_read_b64_tr_b16 a[218:219], v4 offset:18560\n"
"v_mfma_f32_16x16x32_bf16 v[164:167], a[232:235], a[68:71], v[164:167]\n"
"v_mfma_f32_16x16x32_bf16 v[168:171], a[232:235], a[72:75], v[168:171]\n"
"v_mfma_f32_16x16x32_bf16 v[172:175], a[232:235], a[76:79], v[172:175]\n"
"v_mfma_f32_16x16x32_bf16 v[188:191], a[236:239], a[76:79], v[188:191]\n"
"ds_read_b64_tr_b16 a[220:221], v5 offset:12416\n"
"ds_read_b64_tr_b16 a[222:223], v5 offset:18560\n"
"v_mfma_f32_16x16x32_bf16 v[184:187], a[236:239], a[72:75], v[184:187]\n"
"v_mfma_f32_16x16x32_bf16 v[180:183], a[236:239], a[68:71], v[180:183]\n"
"v_mfma_f32_16x16x32_bf16 v[176:179], a[236:239], a[64:67], v[176:179]\n"
"s_waitcnt lgkmcnt(8)\n"
"v_mfma_f32_16x16x32_bf16 v[192:195], a[192:195], a[64:67], v[192:195]\n"
"ds_read_b64_tr_b16 a[224:225], v6 offset:12416\n"
"ds_read_b64_tr_b16 a[226:227], v6 offset:18560\n"
"v_mfma_f32_16x16x32_bf16 v[196:199], a[192:195], a[68:71], v[196:199]\n"
"v_mfma_f32_16x16x32_bf16 v[200:203], a[192:195], a[72:75], v[200:203]\n"
"v_mfma_f32_16x16x32_bf16 v[204:207], a[192:195], a[76:79], v[204:207]\n"
"s_add_u32 s25, s81, 16384\n"
"buffer_load_dwordx4 a[240:243], v18, s[56:59], s25 offen sc1\n"
"v_mfma_f32_16x16x32_bf16 v[220:223], a[196:199], a[76:79], v[220:223]\n"
"ds_read_b64_tr_b16 a[228:229], v7 offset:12416\n"
"ds_read_b64_tr_b16 a[230:231], v7 offset:18560\n"
"v_mfma_f32_16x16x32_bf16 v[216:219], a[196:199], a[72:75], v[216:219]\n"
"v_mfma_f32_16x16x32_bf16 v[212:215], a[196:199], a[68:71], v[212:215]\n"
"s_waitcnt vmcnt(38)\n"
"v_mfma_f32_16x16x32_bf16 v[208:211], a[196:199], a[64:67], v[208:211]\n"
"buffer_load_dwordx4 a[244:247], v18, s[56:59], s25 offen offset:1024 sc1\n"
"v_mfma_f32_16x16x32_bf16 v[64:67], a[200:203], a[80:83], v[64:67]\n"
"ds_read_b64_tr_b16 a[232:233], v4 offset:12544\n"
"ds_read_b64_tr_b16 a[234:235], v4 offset:18688\n"
"v_mfma_f32_16x16x32_bf16 v[68:71], a[200:203], a[84:87], v[68:71]\n"
"v_mfma_f32_16x16x32_bf16 v[72:75], a[200:203], a[88:91], v[72:75]\n"
"v_mfma_f32_16x16x32_bf16 v[76:79], a[200:203], a[92:95], v[76:79]\n"
"buffer_load_dwordx4 a[248:251], v18, s[56:59], s25 offen offset:2048 sc1\n"
"v_mfma_f32_16x16x32_bf16 v[92:95], a[204:207], a[92:95], v[92:95]\n"
"ds_read_b64_tr_b16 a[236:237], v5 offset:12544\n"
"ds_read_b64_tr_b16 a[238:239], v5 offset:18688\n"
"v_mfma_f32_16x16x32_bf16 v[88:91], a[204:207], a[88:91], v[88:91]\n"
"v_mfma_f32_16x16x32_bf16 v[84:87], a[204:207], a[84:87], v[84:87]\n"
"v_mfma_f32_16x16x32_bf16 v[80:83], a[204:207], a[80:83], v[80:83]\n"
"s_waitcnt lgkmcnt(8)\n"
"s_waitcnt vmcnt(11) lgkmcnt(0)\n"
"s_barrier\n"
"v_mfma_f32_16x16x32_bf16 v[96:99], a[208:211], a[80:83], v[96:99]\n"
"buffer_load_dwordx4 a[252:255], v18, s[56:59], s25 offen offset:3072 sc1\n"
"v_mfma_f32_16x16x32_bf16 v[100:103], a[208:211], a[84:87], v[100:103]\n"
"v_mfma_f32_16x16x32_bf16 v[104:107], a[208:211], a[88:91], v[104:107]\n"
"v_mfma_f32_16x16x32_bf16 v[108:111], a[208:211], a[92:95], v[108:111]\n"
"v_mfma_f32_16x16x32_bf16 v[124:127], a[212:215], a[92:95], v[124:127]\n"
"s_add_u32 s24, s85, 163840\n"
"s_add_u32 m0, s76, 86016\n"
"v_mfma_f32_16x16x32_bf16 v[120:123], a[212:215], a[88:91], v[120:123]\n"
"buffer_load_dwordx4 v56, s[60:63], s24 offen lds\n"
"v_mfma_f32_16x16x32_bf16 v[116:119], a[212:215], a[84:87], v[116:119]\n"
"v_mfma_f32_16x16x32_bf16 v[112:115], a[212:215], a[80:83], v[112:115]\n"
"v_mfma_f32_16x16x32_bf16 v[128:131], a[216:219], a[80:83], v[128:131]\n"
"ds_read_b64_tr_b16 a[192:193], v8 offset:36864\n"
"ds_read_b64_tr_b16 a[194:195], v8 offset:43008\n"
"v_mfma_f32_16x16x32_bf16 v[132:135], a[216:219], a[84:87], v[132:135]\n"
"buffer_load_dwordx4 v57, s[60:63], s24 offen offset:1024 lds\n"
"ds_read_b64_tr_b16 a[196:197], v0 offset:24576\n"
"v_mfma_f32_16x16x32_bf16 v[136:139], a[216:219], a[88:91], v[136:139]\n"
"ds_read_b64_tr_b16 a[198:199], v0 offset:30720\n"
"ds_read_b64_tr_b16 a[200:201], v0 offset:36864\n"
"v_mfma_f32_16x16x32_bf16 v[140:143], a[216:219], a[92:95], v[140:143]\n"
"ds_read_b64_tr_b16 a[202:203], v0 offset:43008\n"
"ds_read_b64_tr_b16 a[204:205], v4 offset:36864\n"
"v_mfma_f32_16x16x32_bf16 v[156:159], a[220:223], a[92:95], v[156:159]\n"
"ds_read_b64_tr_b16 a[206:207], v4 offset:43008\n"
"ds_read_b64_tr_b16 a[208:209], v0 offset:49152\n"
"v_mfma_f32_16x16x32_bf16 v[152:155], a[220:223], a[88:91], v[152:155]\n"
"buffer_load_dwordx4 v58, s[60:63], s24 offen offset:2048 lds\n"
"ds_read_b64_tr_b16 a[210:211], v0 offset:55296\n"
"v_mfma_f32_16x16x32_bf16 v[148:151], a[220:223], a[84:87], v[148:151]\n"
"ds_read_b64_tr_b16 a[212:213], v8 offset:0\n"
"ds_read_b64_tr_b16 a[214:215], v8 offset:6144\n"
"v_mfma_f32_16x16x32_bf16 v[144:147], a[220:223], a[80:83], v[144:147]\n"
"s_waitcnt lgkmcnt(15)\n"
"v_mfma_f32_16x16x32_bf16 v[160:163], a[224:227], a[80:83], v[160:163]\n"
"v_mfma_f32_16x16x32_bf16 v[164:167], a[224:227], a[84:87], v[164:167]\n"
"s_add_u32 s25, s82, 16384\n"
"buffer_load_dwordx4 v[224:227], v18, s[56:59], s25 offen sc1\n"
"v_mfma_f32_16x16x32_bf16 v[168:171], a[224:227], a[88:91], v[168:171]\n"
"v_mfma_f32_16x16x32_bf16 v[172:175], a[224:227], a[92:95], v[172:175]\n"
"v_mfma_f32_16x16x32_bf16 v[188:191], a[228:231], a[92:95], v[188:191]\n"
"v_mfma_f32_16x16x32_bf16 v[184:187], a[228:231], a[88:91], v[184:187]\n"
"buffer_load_dwordx4 v[228:231], v18, s[56:59], s25 offen offset:1024 sc1\n"
"v_mfma_f32_16x16x32_bf16 v[180:183], a[228:231], a[84:87], v[180:183]\n"
"v_mfma_f32_16x16x32_bf16 v[176:179], a[228:231], a[80:83], v[176:179]\n"
"v_mfma_f32_16x16x32_bf16 v[192:195], a[232:235], a[80:83], v[192:195]\n"
"v_mfma_f32_16x16x32_bf16 v[196:199], a[232:235], a[84:87], v[196:199]\n"
"buffer_load_dwordx4 v[232:235], v18, s[56:59], s25 offen offset:2048 sc1\n"
"v_mfma_f32_16x16x32_bf16 v[200:203], a[232:235], a[88:91], v[200:203]\n"
"v_mfma_f32_16x16x32_bf16 v[204:207], a[232:235], a[92:95], v[204:207]\n"
"v_mfma_f32_16x16x32_bf16 v[220:223], a[236:239], a[92:95], v[220:223]\n"
"v_mfma_f32_16x16x32_bf16 v[216:219], a[236:239], a[88:91], v[216:219]\n"
"buffer_load_dwordx4 v[236:239], v18, s[56:59], s25 offen offset:3072 sc1\n"
"v_mfma_f32_16x16x32_bf16 v[212:215], a[236:239], a[84:87], v[212:215]\n"
"s_waitcnt vmcnt(11)\n"
"v_mfma_f32_16x16x32_bf16 v[208:211], a[236:239], a[80:83], v[208:211]\n"
"s_waitcnt lgkmcnt(0)\n"
"v_mfma_f32_16x16x32_bf16 v[64:67], a[192:195], a[96:99], v[64:67]\n"
"ds_read_b64_tr_b16 a[216:217], v9 offset:36864\n"
"ds_read_b64_tr_b16 a[218:219], v9 offset:43008\n"
"v_mfma_f32_16x16x32_bf16 v[64:67], a[196:199], a[112:115], v[64:67]\n"
"s_add_u32 s24, s86, 163840\n"
"s_add_u32 m0, s76, 61440\n"
"v_mfma_f32_16x16x32_bf16 v[64:67], a[200:203], a[128:131], v[64:67]\n"
"buffer_load_dwordx4 v56, s[60:63], s24 offen lds\n"
"ds_read_b64_tr_b16 a[220:221], v1 offset:24576\n"
"v_mfma_f32_16x16x32_bf16 v[64:67], a[204:207], a[144:147], v[64:67]\n"
"ds_read_b64_tr_b16 a[222:223], v1 offset:30720\n"
"ds_read_b64_tr_b16 a[224:225], v1 offset:36864\n"
"v_mfma_f32_16x16x32_bf16 v[64:67], a[208:211], a[160:163], v[64:67]\n"
"ds_read_b64_tr_b16 a[226:227], v1 offset:43008\n"
"ds_read_b64_tr_b16 a[228:229], v5 offset:36864\n"
"v_mfma_f32_16x16x32_bf16 v[64:67], a[212:215], a[176:179], v[64:67]\n"
"ds_read_b64_tr_b16 a[230:231], v5 offset:43008\n"
"ds_read_b64_tr_b16 a[232:233], v1 offset:49152\n"
"v_mfma_f32_16x16x32_bf16 v[68:71], a[212:215], a[180:183], v[68:71]\n"
"buffer_load_dwordx4 v57, s[60:63], s24 offen offset:1024 lds\n"
"ds_read_b64_tr_b16 a[234:235], v1 offset:55296\n"
"v_mfma_f32_16x16x32_bf16 v[68:71], a[208:211], a[164:167], v[68:71]\n"
"ds_read_b64_tr_b16 a[236:237], v9 offset:0\n"
"ds_read_b64_tr_b16 a[238:239], v9 offset:6144\n"
"v_mfma_f32_16x16x32_bf16 v[68:71], a[204:207], a[148:151], v[68:71]\n"
"v_mfma_f32_16x16x32_bf16 v[68:71], a[200:203], a[132:135], v[68:71]\n"
"v_mfma_f32_16x16x32_bf16 v[68:71], a[196:199], a[116:119], v[68:71]\n"
"buffer_load_dwordx4 v58, s[60:63], s24 offen offset:2048 lds\n"
"v_mfma_f32_16x16x32_bf16 v[68:71], a[192:195], a[100:103], v[68:71]\n"
"v_mfma_f32_16x16x32_bf16 v[72:75], a[192:195], a[104:107], v[72:75]\n"
"v_mfma_f32_16x16x32_bf16 v[72:75], a[196:199], a[120:123], v[72:75]\n"
"v_mfma_f32_16x16x32_bf16 v[72:75], a[200:203], a[136:139], v[72:75]\n"
"s_add_u32 s25, s83, 16384\n"
"buffer_load_dwordx4 v[240:243], v18, s[56:59], s25 offen sc1\n"
"v_mfma_f32_16x16x32_bf16 v[72:75], a[204:207], a[152:155], v[72:75]\n"
"v_mfma_f32_16x16x32_bf16 v[72:75], a[208:211], a[168:171], v[72:75]\n"
"v_mfma_f32_16x16x32_bf16 v[72:75], a[212:215], a[184:187], v[72:75]\n"
"v_mfma_f32_16x16x32_bf16 v[76:79], a[212:215], a[188:191], v[76:79]\n"
"buffer_load_dwordx4 v[244:247], v18, s[56:59], s25 offen offset:1024 sc1\n"
"v_mfma_f32_16x16x32_bf16 v[76:79], a[208:211], a[172:175], v[76:79]\n"
"v_mfma_f32_16x16x32_bf16 v[76:79], a[204:207], a[156:159], v[76:79]\n"
"v_mfma_f32_16x16x32_bf16 v[76:79], a[200:203], a[140:143], v[76:79]\n"
"v_mfma_f32_16x16x32_bf16 v[76:79], a[196:199], a[124:127], v[76:79]\n"
"buffer_load_dwordx4 v[248:251], v18, s[56:59], s25 offen offset:2048 sc1\n"
"v_mfma_f32_16x16x32_bf16 v[76:79], a[192:195], a[108:111], v[76:79]\n"
"s_waitcnt lgkmcnt(0)\n"
"v_mfma_f32_16x16x32_bf16 v[80:83], a[216:219], a[96:99], v[80:83]\n"
"ds_read_b64_tr_b16 a[192:193], v10 offset:36864\n"
"ds_read_b64_tr_b16 a[194:195], v10 offset:43008\n"
"v_mfma_f32_16x16x32_bf16 v[80:83], a[220:223], a[112:115], v[80:83]\n"
"ds_read_b64_tr_b16 a[196:197], v2 offset:24576\n"
"ds_read_b64_tr_b16 a[198:199], v2 offset:30720\n"
"v_mfma_f32_16x16x32_bf16 v[80:83], a[224:227], a[128:131], v[80:83]\n"
"buffer_load_dwordx4 v[252:255], v18, s[56:59], s25 offen offset:3072 sc1\n"
"ds_read_b64_tr_b16 a[200:201], v2 offset:36864\n"
"v_mfma_f32_16x16x32_bf16 v[80:83], a[228:231], a[144:147], v[80:83]\n"
"ds_read_b64_tr_b16 a[202:203], v2 offset:43008\n"
"ds_read_b64_tr_b16 a[204:205], v6 offset:36864\n"
"v_mfma_f32_16x16x32_bf16 v[80:83], a[232:235], a[160:163], v[80:83]\n"
"ds_read_b64_tr_b16 a[206:207], v6 offset:43008\n"
"ds_read_b64_tr_b16 a[208:209], v2 offset:49152\n"
"v_mfma_f32_16x16x32_bf16 v[80:83], a[236:239], a[176:179], v[80:83]\n"
"ds_read_b64_tr_b16 a[210:211], v2 offset:55296\n"
"ds_read_b64_tr_b16 a[212:213], v10 offset:0\n"
"v_mfma_f32_16x16x32_bf16 v[84:87], a[236:239], a[180:183], v[84:87]\n"
"s_add_u32 s24, s87, 163840\n"
"s_add_u32 m0, s76, 0\n"
"v_mfma_f32_16x16x32_bf16 v[84:87], a[232:235], a[164:167], v[84:87]\n"
"buffer_load_dwordx4 v56, s[60:63], s24 offen lds\n"
"ds_read_b64_tr_b16 a[214:215], v10 offset:6144\n"
"v_mfma_f32_16x16x32_bf16 v[84:87], a[228:231], a[148:151], v[84:87]\n"
"v_mfma_f32_16x16x32_bf16 v[84:87], a[224:227], a[132:135], v[84:87]\n"
"v_mfma_f32_16x16x32_bf16 v[84:87], a[220:223], a[116:119], v[84:87]\n"
"v_mfma_f32_16x16x32_bf16 v[84:87], a[216:219], a[100:103], v[84:87]\n"
"buffer_load_dwordx4 v57, s[60:63], s24 offen offset:1024 lds\n"
"v_mfma_f32_16x16x32_bf16 v[88:91], a[216:219], a[104:107], v[88:91]\n"
"v_mfma_f32_16x16x32_bf16 v[88:91], a[220:223], a[120:123], v[88:91]\n"
"v_mfma_f32_16x16x32_bf16 v[88:91], a[224:227], a[136:139], v[88:91]\n"
"v_mfma_f32_16x16x32_bf16 v[88:91], a[228:231], a[152:155], v[88:91]\n"
"buffer_load_dwordx4 v58, s[60:63], s24 offen offset:2048 lds\n"
"v_mfma_f32_16x16x32_bf16 v[88:91], a[232:235], a[168:171], v[88:91]\n"
"v_mfma_f32_16x16x32_bf16 v[88:91], a[236:239], a[184:187], v[88:91]\n"
"v_mfma_f32_16x16x32_bf16 v[92:95], a[236:239], a[188:191], v[92:95]\n"
"v_mfma_f32_16x16x32_bf16 v[92:95], a[232:235], a[172:175], v[92:95]\n"
"s_add_u32 s25, s81, 20480\n"
"buffer_load_dwordx4 v[40:43], v18, s[56:59], s25 offen sc1\n"
"v_mfma_f32_16x16x32_bf16 v[92:95], a[228:231], a[156:159], v[92:95]\n"
"v_mfma_f32_16x16x32_bf16 v[92:95], a[224:227], a[140:143], v[92:95]\n"
"v_mfma_f32_16x16x32_bf16 v[92:95], a[220:223], a[124:127], v[92:95]\n"
"v_mfma_f32_16x16x32_bf16 v[92:95], a[216:219], a[108:111], v[92:95]\n"
"buffer_load_dwordx4 v[44:47], v18, s[56:59], s25 offen offset:1024 sc1\n"
"s_waitcnt lgkmcnt(0)\n"
"v_mfma_f32_16x16x32_bf16 v[96:99], a[192:195], a[96:99], v[96:99]\n"
"ds_read_b64_tr_b16 a[216:217], v11 offset:36864\n"
"ds_read_b64_tr_b16 a[218:219], v11 offset:43008\n"
"v_mfma_f32_16x16x32_bf16 v[96:99], a[196:199], a[112:115], v[96:99]\n"
"ds_read_b64_tr_b16 a[220:221], v3 offset:24576\n"
"ds_read_b64_tr_b16 a[222:223], v3 offset:30720\n"
"v_mfma_f32_16x16x32_bf16 v[96:99], a[200:203], a[128:131], v[96:99]\n"
"ds_read_b64_tr_b16 a[224:225], v3 offset:36864\n"
"ds_read_b64_tr_b16 a[226:227], v3 offset:43008\n"
"v_mfma_f32_16x16x32_bf16 v[96:99], a[204:207], a[144:147], v[96:99]\n"
"buffer_load_dwordx4 v[48:51], v18, s[56:59], s25 offen offset:2048 sc1\n"
"ds_read_b64_tr_b16 a[228:229], v7 offset:36864\n"
"v_mfma_f32_16x16x32_bf16 v[96:99], a[208:211], a[160:163], v[96:99]\n"
"ds_read_b64_tr_b16 a[230:231], v7 offset:43008\n"
"ds_read_b64_tr_b16 a[232:233], v3 offset:49152\n"
"v_mfma_f32_16x16x32_bf16 v[96:99], a[212:215], a[176:179], v[96:99]\n"
"ds_read_b64_tr_b16 a[234:235], v3 offset:55296\n"
"ds_read_b64_tr_b16 a[236:237], v11 offset:0\n"
"v_mfma_f32_16x16x32_bf16 v[100:103], a[212:215], a[180:183], v[100:103]\n"
"ds_read_b64_tr_b16 a[238:239], v11 offset:6144\n"
"v_mfma_f32_16x16x32_bf16 v[100:103], a[208:211], a[164:167], v[100:103]\n"
"buffer_load_dwordx4 v[52:55], v18, s[56:59], s25 offen offset:3072 sc1\n"
"v_mfma_f32_16x16x32_bf16 v[100:103], a[204:207], a[148:151], v[100:103]\n"
"v_mfma_f32_16x16x32_bf16 v[100:103], a[200:203], a[132:135], v[100:103]\n"
"v_mfma_f32_16x16x32_bf16 v[100:103], a[196:199], a[116:119], v[100:103]\n"
"v_mfma_f32_16x16x32_bf16 v[100:103], a[192:195], a[100:103], v[100:103]\n"
"s_add_u32 s24, s85, 204800\n"
"s_add_u32 m0, s76, 12288\n"
"v_mfma_f32_16x16x32_bf16 v[104:107], a[192:195], a[104:107], v[104:107]\n"
"buffer_load_dwordx4 v56, s[60:63], s24 offen lds\n"
"v_mfma_f32_16x16x32_bf16 v[104:107], a[196:199], a[120:123], v[104:107]\n"
"v_mfma_f32_16x16x32_bf16 v[104:107], a[200:203], a[136:139], v[104:107]\n"
"v_mfma_f32_16x16x32_bf16 v[104:107], a[204:207], a[152:155], v[104:107]\n"
"v_mfma_f32_16x16x32_bf16 v[104:107], a[208:211], a[168:171], v[104:107]\n"
"buffer_load_dwordx4 v57, s[60:63], s24 offen offset:1024 lds\n"
"v_mfma_f32_16x16x32_bf16 v[104:107], a[212:215], a[184:187], v[104:107]\n"
"v_mfma_f32_16x16x32_bf16 v[108:111], a[212:215], a[188:191], v[108:111]\n"
"v_mfma_f32_16x16x32_bf16 v[108:111], a[208:211], a[172:175], v[108:111]\n"
"v_mfma_f32_16x16x32_bf16 v[108:111], a[204:207], a[156:159], v[108:111]\n"
"buffer_load_dwordx4 v58, s[60:63], s24 offen offset:2048 lds\n"
"v_mfma_f32_16x16x32_bf16 v[108:111], a[200:203], a[140:143], v[108:111]\n"
"v_mfma_f32_16x16x32_bf16 v[108:111], a[196:199], a[124:127], v[108:111]\n"
"v_mfma_f32_16x16x32_bf16 v[108:111], a[192:195], a[108:111], v[108:111]\n"
"s_waitcnt lgkmcnt(0)\n"
"v_mfma_f32_16x16x32_bf16 v[112:115], a[216:219], a[96:99], v[112:115]\n"
"s_add_u32 s25, s82, 20480\n"
"buffer_load_dwordx4 a[0:3], v18, s[56:59], s25 offen sc1\n"
"v_mfma_f32_16x16x32_bf16 v[112:115], a[220:223], a[112:115], v[112:115]\n"
"ds_read_b64_tr_b16 a[192:193], v8 offset:36992\n"
"ds_read_b64_tr_b16 a[194:195], v8 offset:43136\n"
"v_mfma_f32_16x16x32_bf16 v[112:115], a[224:227], a[128:131], v[112:115]\n"
"ds_read_b64_tr_b16 a[196:197], v0 offset:24704\n"
"ds_read_b64_tr_b16 a[198:199], v0 offset:30848\n"
"v_mfma_f32_16x16x32_bf16 v[112:115], a[228:231], a[144:147], v[112:115]\n"
"ds_read_b64_tr_b16 a[200:201], v0 offset:36992\n"
"ds_read_b64_tr_b16 a[202:203], v0 offset:43136\n"
"v_mfma_f32_16x16x32_bf16 v[112:115], a[232:235], a[160:163], v[112:115]\n"
"buffer_load_dwordx4 a[4:7], v18, s[56:59], s25 offen offset:1024 sc1\n"
"ds_read_b64_tr_b16 a[204:205], v4 offset:36992\n"
"v_mfma_f32_16x16x32_bf16 v[112:115], a[236:239], a[176:179], v[112:115]\n"
"ds_read_b64_tr_b16 a[206:207], v4 offset:43136\n"
"ds_read_b64_tr_b16 a[208:209], v0 offset:49280\n"
"v_mfma_f32_16x16x32_bf16 v[116:119], a[236:239], a[180:183], v[116:119]\n"
"ds_read_b64_tr_b16 a[210:211], v0 offset:55424\n"
"ds_read_b64_tr_b16 a[212:213], v8 offset:128\n"
"v_mfma_f32_16x16x32_bf16 v[116:119], a[232:235], a[164:167], v[116:119]\n"
"ds_read_b64_tr_b16 a[214:215], v8 offset:6272\n"
"v_mfma_f32_16x16x32_bf16 v[116:119], a[228:231], a[148:151], v[116:119]\n"
"buffer_load_dwordx4 a[8:11], v18, s[56:59], s25 offen offset:2048 sc1\n"
"v_mfma_f32_16x16x32_bf16 v[116:119], a[224:227], a[132:135], v[116:119]\n"
"v_mfma_f32_16x16x32_bf16 v[116:119], a[220:223], a[116:119], v[116:119]\n"
"v_mfma_f32_16x16x32_bf16 v[116:119], a[216:219], a[100:103], v[116:119]\n"
"v_mfma_f32_16x16x32_bf16 v[120:123], a[216:219], a[104:107], v[120:123]\n"
"buffer_load_dwordx4 a[12:15], v18, s[56:59], s25 offen offset:3072 sc1\n"
"v_mfma_f32_16x16x32_bf16 v[120:123], a[220:223], a[120:123], v[120:123]\n"
"v_mfma_f32_16x16x32_bf16 v[120:123], a[224:227], a[136:139], v[120:123]\n"
"v_mfma_f32_16x16x32_bf16 v[120:123], a[228:231], a[152:155], v[120:123]\n"
"v_mfma_f32_16x16x32_bf16 v[120:123], a[232:235], a[168:171], v[120:123]\n"
"s_add_u32 s24, s86, 204800\n"
"s_add_u32 m0, s76, 122880\n"
"v_mfma_f32_16x16x32_bf16 v[120:123], a[236:239], a[184:187], v[120:123]\n"
"buffer_load_dwordx4 v56, s[60:63], s24 offen lds\n"
"v_mfma_f32_16x16x32_bf16 v[124:127], a[236:239], a[188:191], v[124:127]\n"
"v_mfma_f32_16x16x32_bf16 v[124:127], a[232:235], a[172:175], v[124:127]\n"
"v_mfma_f32_16x16x32_bf16 v[124:127], a[228:231], a[156:159], v[124:127]\n"
"v_mfma_f32_16x16x32_bf16 v[124:127], a[224:227], a[140:143], v[124:127]\n"
"buffer_load_dwordx4 v57, s[60:63], s24 offen offset:1024 lds\n"
"v_mfma_f32_16x16x32_bf16 v[124:127], a[220:223], a[124:127], v[124:127]\n"
"v_mfma_f32_16x16x32_bf16 v[124:127], a[216:219], a[108:111], v[124:127]\n"
"s_waitcnt lgkmcnt(0)\n"
"v_mfma_f32_16x16x32_bf16 v[128:131], a[192:195], a[96:99], v[128:131]\n"
"ds_read_b64_tr_b16 a[216:217], v9 offset:36992\n"
"ds_read_b64_tr_b16 a[218:219], v9 offset:43136\n"
"v_mfma_f32_16x16x32_bf16 v[128:131], a[196:199], a[112:115], v[128:131]\n"
"buffer_load_dwordx4 v58, s[60:63], s24 offen offset:2048 lds\n"
"ds_read_b64_tr_b16 a[220:221], v1 offset:24704\n"
"v_mfma_f32_16x16x32_bf16 v[128:131], a[200:203], a[128:131], v[128:131]\n"
"ds_read_b64_tr_b16 a[222:223], v1 offset:30848\n"
"ds_read_b64_tr_b16 a[224:225], v1 offset:36992\n"
"v_mfma_f32_16x16x32_bf16 v[128:131], a[204:207], a[144:147], v[128:131]\n"
"ds_read_b64_tr_b16 a[226:227], v1 offset:43136\n"
"ds_read_b64_tr_b16 a[228:229], v5 offset:36992\n"
"v_mfma_f32_16x16x32_bf16 v[128:131], a[208:211], a[160:163], v[128:131]\n"
"ds_read_b64_tr_b16 a[230:231], v5 offset:43136\n"
"ds_read_b64_tr_b16 a[232:233], v1 offset:49280\n"
"v_mfma_f32_16x16x32_bf16 v[128:131], a[212:215], a[176:179], v[128:131]\n"
"s_add_u32 s25, s83, 20480\n"
"buffer_load_dwordx4 a[16:19], v18, s[56:59], s25 offen sc1\n"
"v_mfma_f32_16x16x32_bf16 v[132:135], a[212:215], a[180:183], v[132:135]\n"
"ds_read_b64_tr_b16 a[234:235], v1 offset:55424\n"
"ds_read_b64_tr_b16 a[236:237], v9 offset:128\n"
"v_mfma_f32_16x16x32_bf16 v[132:135], a[208:211], a[164:167], v[132:135]\n"
"ds_read_b64_tr_b16 a[238:239], v9 offset:6272\n"
"v_mfma_f32_16x16x32_bf16 v[132:135], a[204:207], a[148:151], v[132:135]\n"
"v_mfma_f32_16x16x32_bf16 v[132:135], a[200:203], a[132:135], v[132:135]\n"
"buffer_load_dwordx4 a[20:23], v18, s[56:59], s25 offen offset:1024 sc1\n"
"v_mfma_f32_16x16x32_bf16 v[132:135], a[196:199], a[116:119], v[132:135]\n"
"v_mfma_f32_16x16x32_bf16 v[132:135], a[192:195], a[100:103], v[132:135]\n"
"v_mfma_f32_16x16x32_bf16 v[136:139], a[192:195], a[104:107], v[136:139]\n"
"v_mfma_f32_16x16x32_bf16 v[136:139], a[196:199], a[120:123], v[136:139]\n"
"buffer_load_dwordx4 a[24:27], v18, s[56:59], s25 offen offset:2048 sc1\n"
"v_mfma_f32_16x16x32_bf16 v[136:139], a[200:203], a[136:139], v[136:139]\n"
"v_mfma_f32_16x16x32_bf16 v[136:139], a[204:207], a[152:155], v[136:139]\n"
"v_mfma_f32_16x16x32_bf16 v[136:139], a[208:211], a[168:171], v[136:139]\n"
"v_mfma_f32_16x16x32_bf16 v[136:139], a[212:215], a[184:187], v[136:139]\n"
"buffer_load_dwordx4 a[28:31], v18, s[56:59], s25 offen offset:3072 sc1\n"
"v_mfma_f32_16x16x32_bf16 v[140:143], a[212:215], a[188:191], v[140:143]\n"
"v_mfma_f32_16x16x32_bf16 v[140:143], a[208:211], a[172:175], v[140:143]\n"
"v_mfma_f32_16x16x32_bf16 v[140:143], a[204:207], a[156:159], v[140:143]\n"
"v_mfma_f32_16x16x32_bf16 v[140:143], a[200:203], a[140:143], v[140:143]\n"
"s_add_u32 s24, s87, 204800\n"
"s_add_u32 m0, s76, 135168\n"
"v_mfma_f32_16x16x32_bf16 v[140:143], a[196:199], a[124:127], v[140:143]\n"
"buffer_load_dwordx4 v56, s[60:63], s24 offen lds\n"
"v_mfma_f32_16x16x32_bf16 v[140:143], a[192:195], a[108:111], v[140:143]\n"
"s_waitcnt lgkmcnt(0)\n"
"v_mfma_f32_16x16x32_bf16 v[144:147], a[216:219], a[96:99], v[144:147]\n"
"ds_read_b64_tr_b16 a[192:193], v10 offset:36992\n"
"ds_read_b64_tr_b16 a[194:195], v10 offset:43136\n"
"v_mfma_f32_16x16x32_bf16 v[144:147], a[220:223], a[112:115], v[144:147]\n"
"ds_read_b64_tr_b16 a[196:197], v2 offset:24704\n"
"ds_read_b64_tr_b16 a[198:199], v2 offset:30848\n"
"v_mfma_f32_16x16x32_bf16 v[144:147], a[224:227], a[128:131], v[144:147]\n"
"buffer_load_dwordx4 v57, s[60:63], s24 offen offset:1024 lds\n"
"ds_read_b64_tr_b16 a[200:201], v2 offset:36992\n"
"v_mfma_f32_16x16x32_bf16 v[144:147], a[228:231], a[144:147], v[144:147]\n"
"ds_read_b64_tr_b16 a[202:203], v2 offset:43136\n"
"ds_read_b64_tr_b16 a[204:205], v6 offset:36992\n"
"v_mfma_f32_16x16x32_bf16 v[144:147], a[232:235], a[160:163], v[144:147]\n"
"ds_read_b64_tr_b16 a[206:207], v6 offset:43136\n"
"ds_read_b64_tr_b16 a[208:209], v2 offset:49280\n"
"v_mfma_f32_16x16x32_bf16 v[144:147], a[236:239], a[176:179], v[144:147]\n"
"ds_read_b64_tr_b16 a[210:211], v2 offset:55424\n"
"ds_read_b64_tr_b16 a[212:213], v10 offset:128\n"
"v_mfma_f32_16x16x32_bf16 v[148:151], a[236:239], a[180:183], v[148:151]\n"
"buffer_load_dwordx4 v58, s[60:63], s24 offen offset:2048 lds\n"
"ds_read_b64_tr_b16 a[214:215], v10 offset:6272\n"
"v_mfma_f32_16x16x32_bf16 v[148:151], a[232:235], a[164:167], v[148:151]\n"
"v_mfma_f32_16x16x32_bf16 v[148:151], a[228:231], a[148:151], v[148:151]\n"
"v_mfma_f32_16x16x32_bf16 v[148:151], a[224:227], a[132:135], v[148:151]\n"
"v_mfma_f32_16x16x32_bf16 v[148:151], a[220:223], a[116:119], v[148:151]\n"
"v_mfma_f32_16x16x32_bf16 v[148:151], a[216:219], a[100:103], v[148:151]\n"
"v_mfma_f32_16x16x32_bf16 v[152:155], a[216:219], a[104:107], v[152:155]\n"
"v_mfma_f32_16x16x32_bf16 v[152:155], a[220:223], a[120:123], v[152:155]\n"
"v_mfma_f32_16x16x32_bf16 v[152:155], a[224:227], a[136:139], v[152:155]\n"
"v_mfma_f32_16x16x32_bf16 v[152:155], a[228:231], a[152:155], v[152:155]\n"
"v_mfma_f32_16x16x32_bf16 v[152:155], a[232:235], a[168:171], v[152:155]\n"
"v_mfma_f32_16x16x32_bf16 v[152:155], a[236:239], a[184:187], v[152:155]\n"
"v_mfma_f32_16x16x32_bf16 v[156:159], a[236:239], a[188:191], v[156:159]\n"
"v_mfma_f32_16x16x32_bf16 v[156:159], a[232:235], a[172:175], v[156:159]\n"
"v_mfma_f32_16x16x32_bf16 v[156:159], a[228:231], a[156:159], v[156:159]\n"
"v_mfma_f32_16x16x32_bf16 v[156:159], a[224:227], a[140:143], v[156:159]\n"
"v_mfma_f32_16x16x32_bf16 v[156:159], a[220:223], a[124:127], v[156:159]\n"
"v_mfma_f32_16x16x32_bf16 v[156:159], a[216:219], a[108:111], v[156:159]\n"
"s_waitcnt lgkmcnt(0)\n"
"v_mfma_f32_16x16x32_bf16 v[160:163], a[192:195], a[96:99], v[160:163]\n"
"ds_read_b64_tr_b16 a[216:217], v11 offset:36992\n"
"ds_read_b64_tr_b16 a[218:219], v11 offset:43136\n"
"v_mfma_f32_16x16x32_bf16 v[160:163], a[196:199], a[112:115], v[160:163]\n"
"ds_read_b64_tr_b16 a[220:221], v3 offset:24704\n"
"ds_read_b64_tr_b16 a[222:223], v3 offset:30848\n"
"v_mfma_f32_16x16x32_bf16 v[160:163], a[200:203], a[128:131], v[160:163]\n"
"ds_read_b64_tr_b16 a[224:225], v3 offset:36992\n"
"ds_read_b64_tr_b16 a[226:227], v3 offset:43136\n"
"v_mfma_f32_16x16x32_bf16 v[160:163], a[204:207], a[144:147], v[160:163]\n"
"ds_read_b64_tr_b16 a[228:229], v7 offset:36992\n"
"ds_read_b64_tr_b16 a[230:231], v7 offset:43136\n"
"v_mfma_f32_16x16x32_bf16 v[160:163], a[208:211], a[160:163], v[160:163]\n"
"ds_read_b64_tr_b16 a[232:233], v3 offset:49280\n"
"ds_read_b64_tr_b16 a[234:235], v3 offset:55424\n"
"v_mfma_f32_16x16x32_bf16 v[160:163], a[212:215], a[176:179], v[160:163]\n"
"ds_read_b64_tr_b16 a[236:237], v11 offset:128\n"
"ds_read_b64_tr_b16 a[238:239], v11 offset:6272\n"
"v_mfma_f32_16x16x32_bf16 v[164:167], a[212:215], a[180:183], v[164:167]\n"
"v_mfma_f32_16x16x32_bf16 v[164:167], a[208:211], a[164:167], v[164:167]\n"
"v_mfma_f32_16x16x32_bf16 v[164:167], a[204:207], a[148:151], v[164:167]\n"
"v_mfma_f32_16x16x32_bf16 v[164:167], a[200:203], a[132:135], v[164:167]\n"
"v_mfma_f32_16x16x32_bf16 v[164:167], a[196:199], a[116:119], v[164:167]\n"
"v_mfma_f32_16x16x32_bf16 v[164:167], a[192:195], a[100:103], v[164:167]\n"
"v_mfma_f32_16x16x32_bf16 v[168:171], a[192:195], a[104:107], v[168:171]\n"
"v_mfma_f32_16x16x32_bf16 v[168:171], a[196:199], a[120:123], v[168:171]\n"
"v_mfma_f32_16x16x32_bf16 v[168:171], a[200:203], a[136:139], v[168:171]\n"
"v_mfma_f32_16x16x32_bf16 v[168:171], a[204:207], a[152:155], v[168:171]\n"
"v_mfma_f32_16x16x32_bf16 v[168:171], a[208:211], a[168:171], v[168:171]\n"
"v_mfma_f32_16x16x32_bf16 v[168:171], a[212:215], a[184:187], v[168:171]\n"
"v_mfma_f32_16x16x32_bf16 v[172:175], a[212:215], a[188:191], v[172:175]\n"
"v_mfma_f32_16x16x32_bf16 v[172:175], a[208:211], a[172:175], v[172:175]\n"
"v_mfma_f32_16x16x32_bf16 v[172:175], a[204:207], a[156:159], v[172:175]\n"
"v_mfma_f32_16x16x32_bf16 v[172:175], a[200:203], a[140:143], v[172:175]\n"
"v_mfma_f32_16x16x32_bf16 v[172:175], a[196:199], a[124:127], v[172:175]\n"
"v_mfma_f32_16x16x32_bf16 v[172:175], a[192:195], a[108:111], v[172:175]\n"
"s_waitcnt lgkmcnt(0)\n"
"v_mfma_f32_16x16x32_bf16 v[176:179], a[216:219], a[96:99], v[176:179]\n"
"ds_read_b64_tr_b16 a[192:193], v8 offset:37120\n"
"ds_read_b64_tr_b16 a[194:195], v8 offset:43264\n"
"v_mfma_f32_16x16x32_bf16 v[176:179], a[220:223], a[112:115], v[176:179]\n"
"ds_read_b64_tr_b16 a[196:197], v0 offset:24832\n"
"ds_read_b64_tr_b16 a[198:199], v0 offset:30976\n"
"v_mfma_f32_16x16x32_bf16 v[176:179], a[224:227], a[128:131], v[176:179]\n"
"ds_read_b64_tr_b16 a[200:201], v0 offset:37120\n"
"ds_read_b64_tr_b16 a[202:203], v0 offset:43264\n"
"v_mfma_f32_16x16x32_bf16 v[176:179], a[228:231], a[144:147], v[176:179]\n"
"ds_read_b64_tr_b16 a[204:205], v4 offset:37120\n"
"ds_read_b64_tr_b16 a[206:207], v4 offset:43264\n"
"v_mfma_f32_16x16x32_bf16 v[176:179], a[232:235], a[160:163], v[176:179]\n"
"ds_read_b64_tr_b16 a[208:209], v0 offset:49408\n"
"ds_read_b64_tr_b16 a[210:211], v0 offset:55552\n"
"v_mfma_f32_16x16x32_bf16 v[176:179], a[236:239], a[176:179], v[176:179]\n"
"ds_read_b64_tr_b16 a[212:213], v8 offset:256\n"
"ds_read_b64_tr_b16 a[214:215], v8 offset:6400\n"
"v_mfma_f32_16x16x32_bf16 v[180:183], a[236:239], a[180:183], v[180:183]\n"
"v_mfma_f32_16x16x32_bf16 v[180:183], a[232:235], a[164:167], v[180:183]\n"
"v_mfma_f32_16x16x32_bf16 v[180:183], a[228:231], a[148:151], v[180:183]\n"
"v_mfma_f32_16x16x32_bf16 v[180:183], a[224:227], a[132:135], v[180:183]\n"
"v_mfma_f32_16x16x32_bf16 v[180:183], a[220:223], a[116:119], v[180:183]\n"
"v_mfma_f32_16x16x32_bf16 v[180:183], a[216:219], a[100:103], v[180:183]\n"
"v_mfma_f32_16x16x32_bf16 v[184:187], a[216:219], a[104:107], v[184:187]\n"
"v_mfma_f32_16x16x32_bf16 v[184:187], a[220:223], a[120:123], v[184:187]\n"
"v_mfma_f32_16x16x32_bf16 v[184:187], a[224:227], a[136:139], v[184:187]\n"
"v_mfma_f32_16x16x32_bf16 v[184:187], a[228:231], a[152:155], v[184:187]\n"
"v_mfma_f32_16x16x32_bf16 v[184:187], a[232:235], a[168:171], v[184:187]\n"
"v_mfma_f32_16x16x32_bf16 v[184:187], a[236:239], a[184:187], v[184:187]\n"
"v_mfma_f32_16x16x32_bf16 v[188:191], a[236:239], a[188:191], v[188:191]\n"
"v_mfma_f32_16x16x32_bf16 v[188:191], a[232:235], a[172:175], v[188:191]\n"
"v_mfma_f32_16x16x32_bf16 v[188:191], a[228:231], a[156:159], v[188:191]\n"
"v_mfma_f32_16x16x32_bf16 v[188:191], a[224:227], a[140:143], v[188:191]\n"
"v_mfma_f32_16x16x32_bf16 v[188:191], a[220:223], a[124:127], v[188:191]\n"
"v_mfma_f32_16x16x32_bf16 v[188:191], a[216:219], a[108:111], v[188:191]\n"
"s_waitcnt vmcnt(0)\n"
"s_barrier\n"
"s_waitcnt lgkmcnt(0)\n"
"v_mfma_f32_16x16x32_bf16 v[192:195], a[192:195], a[96:99], v[192:195]\n"
"ds_read_b64_tr_b16 a[216:217], v4 offset:24576\n"
"ds_read_b64_tr_b16 a[218:219], v4 offset:30720\n"
"v_mfma_f32_16x16x32_bf16 v[192:195], a[196:199], a[112:115], v[192:195]\n"
"ds_read_b64_tr_b16 a[220:221], v4 offset:0\n"
"ds_read_b64_tr_b16 a[222:223], v4 offset:6144\n"
"v_mfma_f32_16x16x32_bf16 v[192:195], a[200:203], a[128:131], v[192:195]\n"
"ds_read_b64_tr_b16 a[224:225], v0 offset:0\n"
"ds_read_b64_tr_b16 a[226:227], v0 offset:6144\n"
"v_mfma_f32_16x16x32_bf16 v[192:195], a[204:207], a[144:147], v[192:195]\n"
"ds_read_b64_tr_b16 a[228:229], v0 offset:12288\n"
"ds_read_b64_tr_b16 a[230:231], v0 offset:18432\n"
"v_mfma_f32_16x16x32_bf16 v[192:195], a[208:211], a[160:163], v[192:195]\n"
"ds_read_b64_tr_b16 a[232:233], v8 offset:12288\n"
"ds_read_b64_tr_b16 a[234:235], v8 offset:18432\n"
"v_mfma_f32_16x16x32_bf16 v[192:195], a[212:215], a[176:179], v[192:195]\n"
"ds_read_b64_tr_b16 a[236:237], v8 offset:24576\n"
"ds_read_b64_tr_b16 a[238:239], v8 offset:30720\n"
"v_mfma_f32_16x16x32_bf16 v[196:199], a[212:215], a[180:183], v[196:199]\n"
"v_mfma_f32_16x16x32_bf16 v[196:199], a[208:211], a[164:167], v[196:199]\n"
"v_mfma_f32_16x16x32_bf16 v[196:199], a[204:207], a[148:151], v[196:199]\n"
"v_mfma_f32_16x16x32_bf16 v[196:199], a[200:203], a[132:135], v[196:199]\n"
"v_mfma_f32_16x16x32_bf16 v[196:199], a[196:199], a[116:119], v[196:199]\n"
"v_mfma_f32_16x16x32_bf16 v[196:199], a[192:195], a[100:103], v[196:199]\n"
"v_mfma_f32_16x16x32_bf16 v[200:203], a[192:195], a[104:107], v[200:203]\n"
"v_mfma_f32_16x16x32_bf16 v[200:203], a[196:199], a[120:123], v[200:203]\n"
"v_mfma_f32_16x16x32_bf16 v[200:203], a[200:203], a[136:139], v[200:203]\n"
"v_mfma_f32_16x16x32_bf16 v[200:203], a[204:207], a[152:155], v[200:203]\n"
"v_mfma_f32_16x16x32_bf16 v[200:203], a[208:211], a[168:171], v[200:203]\n"
"v_mfma_f32_16x16x32_bf16 v[200:203], a[212:215], a[184:187], v[200:203]\n"
"v_mfma_f32_16x16x32_bf16 v[204:207], a[212:215], a[188:191], v[204:207]\n"
"v_mfma_f32_16x16x32_bf16 v[204:207], a[208:211], a[172:175], v[204:207]\n"
"v_mfma_f32_16x16x32_bf16 v[204:207], a[204:207], a[156:159], v[204:207]\n"
"v_mfma_f32_16x16x32_bf16 v[204:207], a[200:203], a[140:143], v[204:207]\n"
"v_mfma_f32_16x16x32_bf16 v[204:207], a[196:199], a[124:127], v[204:207]\n"
"s_waitcnt vmcnt(63)\n"
"v_mfma_f32_16x16x32_bf16 v[204:207], a[192:195], a[108:111], v[204:207]\n"
"s_waitcnt lgkmcnt(0)\n"
"v_mfma_f32_16x16x32_bf16 v[64:67], a[216:219], a[240:243], v[64:67]\n"
"ds_read_b64_tr_b16 a[192:193], v5 offset:24576\n"
"ds_read_b64_tr_b16 a[194:195], v5 offset:30720\n"
"v_mfma_f32_16x16x32_bf16 v[64:67], a[220:223], v[224:227], v[64:67]\n"
"ds_read_b64_tr_b16 a[196:197], v5 offset:0\n"
"ds_read_b64_tr_b16 a[198:199], v5 offset:6144\n"
"v_mfma_f32_16x16x32_bf16 v[64:67], a[224:227], v[240:243], v[64:67]\n"
"ds_read_b64_tr_b16 a[200:201], v1 offset:0\n"
"ds_read_b64_tr_b16 a[202:203], v1 offset:6144\n"
"v_mfma_f32_16x16x32_bf16 v[64:67], a[228:231], v[40:43], v[64:67]\n"
"ds_read_b64_tr_b16 a[204:205], v1 offset:12288\n"
"ds_read_b64_tr_b16 a[206:207], v1 offset:18432\n"
"v_mfma_f32_16x16x32_bf16 v[64:67], a[232:235], a[0:3], v[64:67]\n"
"ds_read_b64_tr_b16 a[208:209], v9 offset:12288\n"
"ds_read_b64_tr_b16 a[210:211], v9 offset:18432\n"
"v_mfma_f32_16x16x32_bf16 v[64:67], a[236:239], a[16:19], v[64:67]\n"
"ds_read_b64_tr_b16 a[212:213], v9 offset:24576\n"
"ds_read_b64_tr_b16 a[214:215], v9 offset:30720\n"
"v_mfma_f32_16x16x32_bf16 v[68:71], a[236:239], a[20:23], v[68:71]\n"
"v_mfma_f32_16x16x32_bf16 v[68:71], a[232:235], a[4:7], v[68:71]\n"
"v_mfma_f32_16x16x32_bf16 v[68:71], a[228:231], v[44:47], v[68:71]\n"
"v_mfma_f32_16x16x32_bf16 v[68:71], a[224:227], v[244:247], v[68:71]\n"
"s_sub_u32 s25, 0, s13\n"
"s_and_b32 s25, s25, 3584\n"
"v_mfma_f32_16x16x32_bf16 v[68:71], a[220:223], v[228:231], v[68:71]\n"
"s_add_u32 s25, s25, 16384\n"
"v_cvt_pk_bf16_f32 v64, v64, v65\n"
"v_mfma_f32_16x16x32_bf16 v[68:71], a[216:219], a[244:247], v[68:71]\n"
"v_cvt_pk_bf16_f32 v65, v66, v67\n"
"buffer_store_dwordx2 v[64:65], v24, s[64:67], s25 offen sc1\n"
"v_mfma_f32_16x16x32_bf16 v[72:75], a[216:219], a[248:251], v[72:75]\n"
"v_mfma_f32_16x16x32_bf16 v[72:75], a[220:223], v[232:235], v[72:75]\n"
"v_mfma_f32_16x16x32_bf16 v[72:75], a[224:227], v[248:251], v[72:75]\n"
"v_mfma_f32_16x16x32_bf16 v[72:75], a[228:231], v[48:51], v[72:75]\n"
"s_sub_u32 s25, 512, s13\n"
"s_and_b32 s25, s25, 3584\n"
"v_mfma_f32_16x16x32_bf16 v[72:75], a[232:235], a[8:11], v[72:75]\n"
"s_add_u32 s25, s25, 49152\n"
"v_cvt_pk_bf16_f32 v68, v68, v69\n"
"v_mfma_f32_16x16x32_bf16 v[72:75], a[236:239], a[24:27], v[72:75]\n"
"v_cvt_pk_bf16_f32 v69, v70, v71\n"
"buffer_store_dwordx2 v[68:69], v24, s[64:67], s25 offen sc1\n"
"v_mfma_f32_16x16x32_bf16 v[76:79], a[236:239], a[28:31], v[76:79]\n"
"v_mfma_f32_16x16x32_bf16 v[76:79], a[232:235], a[12:15], v[76:79]\n"
"v_mfma_f32_16x16x32_bf16 v[76:79], a[228:231], v[52:55], v[76:79]\n"
"v_mfma_f32_16x16x32_bf16 v[76:79], a[224:227], v[252:255], v[76:79]\n"
"s_sub_u32 s25, 1024, s13\n"
"s_and_b32 s25, s25, 3584\n"
"v_mfma_f32_16x16x32_bf16 v[76:79], a[220:223], v[236:239], v[76:79]\n"
"s_add_u32 s25, s25, 81920\n"
"v_cvt_pk_bf16_f32 v72, v72, v73\n"
"v_mfma_f32_16x16x32_bf16 v[76:79], a[216:219], a[252:255], v[76:79]\n"
"s_waitcnt lgkmcnt(0)\n"
"v_cvt_pk_bf16_f32 v73, v74, v75\n"
"v_mfma_f32_16x16x32_bf16 v[80:83], a[192:195], a[240:243], v[80:83]\n"
"ds_read_b64_tr_b16 a[216:217], v6 offset:24576\n"
"ds_read_b64_tr_b16 a[218:219], v6 offset:30720\n"
"v_mfma_f32_16x16x32_bf16 v[80:83], a[196:199], v[224:227], v[80:83]\n"
"ds_read_b64_tr_b16 a[220:221], v6 offset:0\n"
"ds_read_b64_tr_b16 a[222:223], v6 offset:6144\n"
"v_mfma_f32_16x16x32_bf16 v[80:83], a[200:203], v[240:243], v[80:83]\n"
"ds_read_b64_tr_b16 a[224:225], v2 offset:0\n"
"ds_read_b64_tr_b16 a[226:227], v2 offset:6144\n"
"v_mfma_f32_16x16x32_bf16 v[80:83], a[204:207], v[40:43], v[80:83]\n"
"ds_read_b64_tr_b16 a[228:229], v2 offset:12288\n"
"ds_read_b64_tr_b16 a[230:231], v2 offset:18432\n"
"v_mfma_f32_16x16x32_bf16 v[80:83], a[208:211], a[0:3], v[80:83]\n"
"ds_read_b64_tr_b16 a[232:233], v10 offset:12288\n"
"ds_read_b64_tr_b16 a[234:235], v10 offset:18432\n"
"v_mfma_f32_16x16x32_bf16 v[80:83], a[212:215], a[16:19], v[80:83]\n"
"ds_read_b64_tr_b16 a[236:237], v10 offset:24576\n"
"ds_read_b64_tr_b16 a[238:239], v10 offset:30720\n"
"v_mfma_f32_16x16x32_bf16 v[84:87], a[212:215], a[20:23], v[84:87]\n"
"buffer_store_dwordx2 v[72:73], v24, s[64:67], s25 offen sc1\n"
"s_sub_u32 s25, 1536, s13\n"
"v_mfma_f32_16x16x32_bf16 v[84:87], a[208:211], a[4:7], v[84:87]\n"
"s_and_b32 s25, s25, 3584\n"
"s_add_u32 s25, s25, 114688\n"
"v_mfma_f32_16x16x32_bf16 v[84:87], a[204:207], v[44:47], v[84:87]\n"
"v_cvt_pk_bf16_f32 v76, v76, v77\n"
"v_cvt_pk_bf16_f32 v77, v78, v79\n"
"v_mfma_f32_16x16x32_bf16 v[84:87], a[200:203], v[244:247], v[84:87]\n"
"buffer_store_dwordx2 v[76:77], v24, s[64:67], s25 offen sc1\n"
"s_sub_u32 s25, 2048, s13\n"
"v_mfma_f32_16x16x32_bf16 v[84:87], a[196:199], v[228:231], v[84:87]\n"
"s_and_b32 s25, s25, 3584\n"
"s_add_u32 s25, s25, 147456\n"
"v_mfma_f32_16x16x32_bf16 v[84:87], a[192:195], a[244:247], v[84:87]\n"
"v_cvt_pk_bf16_f32 v80, v80, v81\n"
"v_cvt_pk_bf16_f32 v81, v82, v83\n"
"v_mfma_f32_16x16x32_bf16 v[88:91], a[192:195], a[248:251], v[88:91]\n"
"buffer_store_dwordx2 v[80:81], v24, s[64:67], s25 offen sc1\n"
"v_mfma_f32_16x16x32_bf16 v[88:91], a[196:199], v[232:235], v[88:91]\n"
"v_mfma_f32_16x16x32_bf16 v[88:91], a[200:203], v[248:251], v[88:91]\n"
"v_mfma_f32_16x16x32_bf16 v[88:91], a[204:207], v[48:51], v[88:91]\n"
"s_sub_u32 s25, 2560, s13\n"
"s_and_b32 s25, s25, 3584\n"
"v_mfma_f32_16x16x32_bf16 v[88:91], a[208:211], a[8:11], v[88:91]\n"
"s_add_u32 s25, s25, 180224\n"
"v_cvt_pk_bf16_f32 v84, v84, v85\n"
"v_mfma_f32_16x16x32_bf16 v[88:91], a[212:215], a[24:27], v[88:91]\n"
"v_cvt_pk_bf16_f32 v85, v86, v87\n"
"buffer_store_dwordx2 v[84:85], v24, s[64:67], s25 offen sc1\n"
"v_mfma_f32_16x16x32_bf16 v[92:95], a[212:215], a[28:31], v[92:95]\n"
"v_mfma_f32_16x16x32_bf16 v[92:95], a[208:211], a[12:15], v[92:95]\n"
"v_mfma_f32_16x16x32_bf16 v[92:95], a[204:207], v[52:55], v[92:95]\n"
"v_mfma_f32_16x16x32_bf16 v[92:95], a[200:203], v[252:255], v[92:95]\n"
"s_sub_u32 s25, 3072, s13\n"
"s_and_b32 s25, s25, 3584\n"
"v_mfma_f32_16x16x32_bf16 v[92:95], a[196:199], v[236:239], v[92:95]\n"
"s_add_u32 s25, s25, 212992\n"
"v_cvt_pk_bf16_f32 v88, v88, v89\n"
"v_mfma_f32_16x16x32_bf16 v[92:95], a[192:195], a[252:255], v[92:95]\n"
"s_waitcnt lgkmcnt(0)\n"
"v_cvt_pk_bf16_f32 v89, v90, v91\n"
"v_mfma_f32_16x16x32_bf16 v[96:99], a[216:219], a[240:243], v[96:99]\n"
"ds_read_b64_tr_b16 a[192:193], v7 offset:24576\n"
"ds_read_b64_tr_b16 a[194:195], v7 offset:30720\n"
"v_mfma_f32_16x16x32_bf16 v[96:99], a[220:223], v[224:227], v[96:99]\n"
"ds_read_b64_tr_b16 a[196:197], v7 offset:0\n"
"ds_read_b64_tr_b16 a[198:199], v7 offset:6144\n"
"v_mfma_f32_16x16x32_bf16 v[96:99], a[224:227], v[240:243], v[96:99]\n"
"ds_read_b64_tr_b16 a[200:201], v3 offset:0\n"
"ds_read_b64_tr_b16 a[202:203], v3 offset:6144\n"
"v_mfma_f32_16x16x32_bf16 v[96:99], a[228:231], v[40:43], v[96:99]\n"
"ds_read_b64_tr_b16 a[204:205], v3 offset:12288\n"
"ds_read_b64_tr_b16 a[206:207], v3 offset:18432\n"
"v_mfma_f32_16x16x32_bf16 v[96:99], a[232:235], a[0:3], v[96:99]\n"
"ds_read_b64_tr_b16 a[208:209], v11 offset:12288\n"
"ds_read_b64_tr_b16 a[210:211], v11 offset:18432\n"
"v_mfma_f32_16x16x32_bf16 v[96:99], a[236:239], a[16:19], v[96:99]\n"
"ds_read_b64_tr_b16 a[212:213], v11 offset:24576\n"
"ds_read_b64_tr_b16 a[214:215], v11 offset:30720\n"
"v_mfma_f32_16x16x32_bf16 v[100:103], a[236:239], a[20:23], v[100:103]\n"
"buffer_store_dwordx2 v[88:89], v24, s[64:67], s25 offen sc1\n"
"s_sub_u32 s25, 3584, s13\n"
"v_mfma_f32_16x16x32_bf16 v[100:103], a[232:235], a[4:7], v[100:103]\n"
"s_and_b32 s25, s25, 3584\n"
"s_add_u32 s25, s25, 245760\n"
"v_mfma_f32_16x16x32_bf16 v[100:103], a[228:231], v[44:47], v[100:103]\n"
"v_cvt_pk_bf16_f32 v92, v92, v93\n"
"v_cvt_pk_bf16_f32 v93, v94, v95\n"
"v_mfma_f32_16x16x32_bf16 v[100:103], a[224:227], v[244:247], v[100:103]\n"
"buffer_store_dwordx2 v[92:93], v24, s[64:67], s25 offen sc1\n"
"s_add_u32 s25, s95, 32768\n"
"v_mfma_f32_16x16x32_bf16 v[100:103], a[220:223], v[228:231], v[100:103]\n"
"s_and_b32 s25, s25, 229376\n"
"s_add_u32 s25, s25, 2048\n"
"v_mfma_f32_16x16x32_bf16 v[100:103], a[216:219], a[244:247], v[100:103]\n"
"v_cvt_pk_bf16_f32 v96, v96, v97\n"
"v_cvt_pk_bf16_f32 v97, v98, v99\n"
"v_mfma_f32_16x16x32_bf16 v[104:107], a[216:219], a[248:251], v[104:107]\n"
"v_mfma_f32_16x16x32_bf16 v[104:107], a[220:223], v[232:235], v[104:107]\n"
"v_mfma_f32_16x16x32_bf16 v[104:107], a[224:227], v[248:251], v[104:107]\n"
"v_mfma_f32_16x16x32_bf16 v[104:107], a[228:231], v[48:51], v[104:107]\n"
"s_nop 1\n"
"v_cvt_pk_bf16_f32 v98, v100, v101\n"
"v_cvt_pk_bf16_f32 v99, v102, v103\n"
"v_mfma_f32_16x16x32_bf16 v[104:107], a[232:235], a[8:11], v[104:107]\n"
"buffer_store_dwordx4 v[96:99], v18, s[64:67], s25 offen sc1\n"
"v_mfma_f32_16x16x32_bf16 v[104:107], a[236:239], a[24:27], v[104:107]\n"
"v_mfma_f32_16x16x32_bf16 v[108:111], a[236:239], a[28:31], v[108:111]\n"
"v_mfma_f32_16x16x32_bf16 v[108:111], a[232:235], a[12:15], v[108:111]\n"
"v_mfma_f32_16x16x32_bf16 v[108:111], a[228:231], v[52:55], v[108:111]\n"
"v_mfma_f32_16x16x32_bf16 v[108:111], a[224:227], v[252:255], v[108:111]\n"
"s_nop 3\n"
"v_cvt_pk_bf16_f32 v104, v104, v105\n"
"v_cvt_pk_bf16_f32 v105, v106, v107\n"
"v_mfma_f32_16x16x32_bf16 v[108:111], a[220:223], v[236:239], v[108:111]\n"
"v_mfma_f32_16x16x32_bf16 v[108:111], a[216:219], a[252:255], v[108:111]\n"
"s_waitcnt lgkmcnt(0)\n"
"v_mfma_f32_16x16x32_bf16 v[112:115], a[192:195], a[240:243], v[112:115]\n"
"ds_read_b64_tr_b16 a[216:217], v4 offset:24704\n"
"ds_read_b64_tr_b16 a[218:219], v4 offset:30848\n"
"v_mfma_f32_16x16x32_bf16 v[112:115], a[196:199], v[224:227], v[112:115]\n"
"ds_read_b64_tr_b16 a[220:221], v4 offset:128\n"
"ds_read_b64_tr_b16 a[222:223], v4 offset:6272\n"
"v_mfma_f32_16x16x32_bf16 v[112:115], a[200:203], v[240:243], v[112:115]\n"
"ds_read_b64_tr_b16 a[224:225], v0 offset:128\n"
"ds_read_b64_tr_b16 a[226:227], v0 offset:6272\n"
"v_mfma_f32_16x16x32_bf16 v[112:115], a[204:207], v[40:43], v[112:115]\n"
"ds_read_b64_tr_b16 a[228:229], v0 offset:12416\n"
"ds_read_b64_tr_b16 a[230:231], v0 offset:18560\n"
"v_mfma_f32_16x16x32_bf16 v[112:115], a[208:211], a[0:3], v[112:115]\n"
"ds_read_b64_tr_b16 a[232:233], v8 offset:12416\n"
"ds_read_b64_tr_b16 a[234:235], v8 offset:18560\n"
"v_mfma_f32_16x16x32_bf16 v[112:115], a[212:215], a[16:19], v[112:115]\n"
"ds_read_b64_tr_b16 a[236:237], v8 offset:24704\n"
"ds_read_b64_tr_b16 a[238:239], v8 offset:30848\n"
"v_mfma_f32_16x16x32_bf16 v[116:119], a[212:215], a[20:23], v[116:119]\n"
"v_cvt_pk_bf16_f32 v106, v108, v109\n"
"v_cvt_pk_bf16_f32 v107, v110, v111\n"
"v_mfma_f32_16x16x32_bf16 v[116:119], a[208:211], a[4:7], v[116:119]\n"
"buffer_store_dwordx4 v[104:107], v18, s[64:67], s25 offen offset:1024 sc1\n"
"v_mfma_f32_16x16x32_bf16 v[116:119], a[204:207], v[44:47], v[116:119]\n"
"v_mfma_f32_16x16x32_bf16 v[116:119], a[200:203], v[244:247], v[116:119]\n"
"s_add_u32 s25, s95, 65536\n"
"s_and_b32 s25, s25, 229376\n"
"v_mfma_f32_16x16x32_bf16 v[116:119], a[196:199], v[228:231], v[116:119]\n"
"s_add_u32 s25, s25, 4096\n"
"v_cvt_pk_bf16_f32 v112, v112, v113\n"
"v_mfma_f32_16x16x32_bf16 v[116:119], a[192:195], a[244:247], v[116:119]\n"
"v_cvt_pk_bf16_f32 v113, v114, v115\n"
"v_mfma_f32_16x16x32_bf16 v[120:123], a[192:195], a[248:251], v[120:123]\n"
"v_mfma_f32_16x16x32_bf16 v[120:123], a[196:199], v[232:235], v[120:123]\n"
"v_mfma_f32_16x16x32_bf16 v[120:123], a[200:203], v[248:251], v[120:123]\n"
"v_mfma_f32_16x16x32_bf16 v[120:123], a[204:207], v[48:51], v[120:123]\n"
"s_nop 2\n"
"v_cvt_pk_bf16_f32 v114, v116, v117\n"
"v_cvt_pk_bf16_f32 v115, v118, v119\n"
"v_mfma_f32_16x16x32_bf16 v[120:123], a[208:211], a[8:11], v[120:123]\n"
"buffer_store_dwordx4 v[112:115], v18, s[64:67], s25 offen sc1\n"
"v_mfma_f32_16x16x32_bf16 v[120:123], a[212:215], a[24:27], v[120:123]\n"
"v_mfma_f32_16x16x32_bf16 v[124:127], a[212:215], a[28:31], v[124:127]\n"
"v_mfma_f32_16x16x32_bf16 v[124:127], a[208:211], a[12:15], v[124:127]\n"
"v_mfma_f32_16x16x32_bf16 v[124:127], a[204:207], v[52:55], v[124:127]\n"
"v_mfma_f32_16x16x32_bf16 v[124:127], a[200:203], v[252:255], v[124:127]\n"
"s_nop 3\n"
"v_cvt_pk_bf16_f32 v120, v120, v121\n"
"v_cvt_pk_bf16_f32 v121, v122, v123\n"
"v_mfma_f32_16x16x32_bf16 v[124:127], a[196:199], v[236:239], v[124:127]\n"
"v_mfma_f32_16x16x32_bf16 v[124:127], a[192:195], a[252:255], v[124:127]\n"
"s_waitcnt lgkmcnt(0)\n"
"v_mfma_f32_16x16x32_bf16 v[128:131], a[216:219], a[240:243], v[128:131]\n"
"ds_read_b64_tr_b16 a[192:193], v5 offset:24704\n"
"ds_read_b64_tr_b16 a[194:195], v5 offset:30848\n"
"v_mfma_f32_16x16x32_bf16 v[128:131], a[220:223], v[224:227], v[128:131]\n"
"ds_read_b64_tr_b16 a[196:197], v5 offset:128\n"
"ds_read_b64_tr_b16 a[198:199], v5 offset:6272\n"
"v_mfma_f32_16x16x32_bf16 v[128:131], a[224:227], v[240:243], v[128:131]\n"
"ds_read_b64_tr_b16 a[200:201], v1 offset:128\n"
"ds_read_b64_tr_b16 a[202:203], v1 offset:6272\n"
"v_mfma_f32_16x16x32_bf16 v[128:131], a[228:231], v[40:43], v[128:131]\n"
"ds_read_b64_tr_b16 a[204:205], v1 offset:12416\n"
"ds_read_b64_tr_b16 a[206:207], v1 offset:18560\n"
"v_mfma_f32_16x16x32_bf16 v[128:131], a[232:235], a[0:3], v[128:131]\n"
"ds_read_b64_tr_b16 a[208:209], v9 offset:12416\n"
"ds_read_b64_tr_b16 a[210:211], v9 offset:18560\n"
"v_mfma_f32_16x16x32_bf16 v[128:131], a[236:239], a[16:19], v[128:131]\n"
"ds_read_b64_tr_b16 a[212:213], v9 offset:24704\n"
"ds_read_b64_tr_b16 a[214:215], v9 offset:30848\n"
"v_mfma_f32_16x16x32_bf16 v[132:135], a[236:239], a[20:23], v[132:135]\n"
"v_cvt_pk_bf16_f32 v122, v124, v125\n"
"v_cvt_pk_bf16_f32 v123, v126, v127\n"
"v_mfma_f32_16x16x32_bf16 v[132:135], a[232:235], a[4:7], v[132:135]\n"
"buffer_store_dwordx4 v[120:123], v18, s[64:67], s25 offen offset:1024 sc1\n"
"v_mfma_f32_16x16x32_bf16 v[132:135], a[228:231], v[44:47], v[132:135]\n"
"v_mfma_f32_16x16x32_bf16 v[132:135], a[224:227], v[244:247], v[132:135]\n"
"s_add_u32 s25, s95, 98304\n"
"s_and_b32 s25, s25, 229376\n"
"v_mfma_f32_16x16x32_bf16 v[132:135], a[220:223], v[228:231], v[132:135]\n"
"s_add_u32 s25, s25, 6144\n"
"v_cvt_pk_bf16_f32 v128, v128, v129\n"
"v_mfma_f32_16x16x32_bf16 v[132:135], a[216:219], a[244:247], v[132:135]\n"
"v_cvt_pk_bf16_f32 v129, v130, v131\n"
"v_mfma_f32_16x16x32_bf16 v[136:139], a[216:219], a[248:251], v[136:139]\n"
"v_mfma_f32_16x16x32_bf16 v[136:139], a[220:223], v[232:235], v[136:139]\n"
"v_mfma_f32_16x16x32_bf16 v[136:139], a[224:227], v[248:251], v[136:139]\n"
"v_mfma_f32_16x16x32_bf16 v[136:139], a[228:231], v[48:51], v[136:139]\n"
"s_nop 2\n"
"v_cvt_pk_bf16_f32 v130, v132, v133\n"
"v_cvt_pk_bf16_f32 v131, v134, v135\n"
"v_mfma_f32_16x16x32_bf16 v[136:139], a[232:235], a[8:11], v[136:139]\n"
"buffer_store_dwordx4 v[128:131], v18, s[64:67], s25 offen sc1\n"
"v_mfma_f32_16x16x32_bf16 v[136:139], a[236:239], a[24:27], v[136:139]\n"
"v_mfma_f32_16x16x32_bf16 v[140:143], a[236:239], a[28:31], v[140:143]\n"
"v_mfma_f32_16x16x32_bf16 v[140:143], a[232:235], a[12:15], v[140:143]\n"
"v_mfma_f32_16x16x32_bf16 v[140:143], a[228:231], v[52:55], v[140:143]\n"
"v_mfma_f32_16x16x32_bf16 v[140:143], a[224:227], v[252:255], v[140:143]\n"
"s_nop 3\n"
"v_cvt_pk_bf16_f32 v136, v136, v137\n"
"v_cvt_pk_bf16_f32 v137, v138, v139\n"
"v_mfma_f32_16x16x32_bf16 v[140:143], a[220:223], v[236:239], v[140:143]\n"
"v_mfma_f32_16x16x32_bf16 v[140:143], a[216:219], a[252:255], v[140:143]\n"
"s_waitcnt lgkmcnt(0)\n"
"v_mfma_f32_16x16x32_bf16 v[144:147], a[192:195], a[240:243], v[144:147]\n"
"ds_read_b64_tr_b16 a[216:217], v6 offset:24704\n"
"ds_read_b64_tr_b16 a[218:219], v6 offset:30848\n"
"v_mfma_f32_16x16x32_bf16 v[144:147], a[196:199], v[224:227], v[144:147]\n"
"ds_read_b64_tr_b16 a[220:221], v6 offset:128\n"
"ds_read_b64_tr_b16 a[222:223], v6 offset:6272\n"
"v_mfma_f32_16x16x32_bf16 v[144:147], a[200:203], v[240:243], v[144:147]\n"
"ds_read_b64_tr_b16 a[224:225], v2 offset:128\n"
"ds_read_b64_tr_b16 a[226:227], v2 offset:6272\n"
"v_mfma_f32_16x16x32_bf16 v[144:147], a[204:207], v[40:43], v[144:147]\n"
"ds_read_b64_tr_b16 a[228:229], v2 offset:12416\n"
"ds_read_b64_tr_b16 a[230:231], v2 offset:18560\n"
"v_mfma_f32_16x16x32_bf16 v[144:147], a[208:211], a[0:3], v[144:147]\n"
"ds_read_b64_tr_b16 a[232:233], v10 offset:12416\n"
"ds_read_b64_tr_b16 a[234:235], v10 offset:18560\n"
"v_mfma_f32_16x16x32_bf16 v[144:147], a[212:215], a[16:19], v[144:147]\n"
"ds_read_b64_tr_b16 a[236:237], v10 offset:24704\n"
"ds_read_b64_tr_b16 a[238:239], v10 offset:30848\n"
"v_mfma_f32_16x16x32_bf16 v[148:151], a[212:215], a[20:23], v[148:151]\n"
"v_cvt_pk_bf16_f32 v138, v140, v141\n"
"v_cvt_pk_bf16_f32 v139, v142, v143\n"
"v_mfma_f32_16x16x32_bf16 v[148:151], a[208:211], a[4:7], v[148:151]\n"
"buffer_store_dwordx4 v[136:139], v18, s[64:67], s25 offen offset:1024 sc1\n"
"v_mfma_f32_16x16x32_bf16 v[148:151], a[204:207], v[44:47], v[148:151]\n"
"v_mfma_f32_16x16x32_bf16 v[148:151], a[200:203], v[244:247], v[148:151]\n"
"s_add_u32 s25, s95, 131072\n"
"s_and_b32 s25, s25, 229376\n"
"v_mfma_f32_16x16x32_bf16 v[148:151], a[196:199], v[228:231], v[148:151]\n"
"s_add_u32 s25, s25, 8192\n"
"v_cvt_pk_bf16_f32 v144, v144, v145\n"
"v_mfma_f32_16x16x32_bf16 v[148:151], a[192:195], a[244:247], v[148:151]\n"
"v_cvt_pk_bf16_f32 v145, v146, v147\n"
"v_mfma_f32_16x16x32_bf16 v[152:155], a[192:195], a[248:251], v[152:155]\n"
"v_mfma_f32_16x16x32_bf16 v[152:155], a[196:199], v[232:235], v[152:155]\n"
"v_mfma_f32_16x16x32_bf16 v[152:155], a[200:203], v[248:251], v[152:155]\n"
"v_mfma_f32_16x16x32_bf16 v[152:155], a[204:207], v[48:51], v[152:155]\n"
"s_nop 2\n"
"v_cvt_pk_bf16_f32 v146, v148, v149\n"
"v_cvt_pk_bf16_f32 v147, v150, v151\n"
"v_mfma_f32_16x16x32_bf16 v[152:155], a[208:211], a[8:11], v[152:155]\n"
"buffer_store_dwordx4 v[144:147], v18, s[64:67], s25 offen sc1\n"
"v_mfma_f32_16x16x32_bf16 v[152:155], a[212:215], a[24:27], v[152:155]\n"
"v_mfma_f32_16x16x32_bf16 v[156:159], a[212:215], a[28:31], v[156:159]\n"
"v_mfma_f32_16x16x32_bf16 v[156:159], a[208:211], a[12:15], v[156:159]\n"
"v_mfma_f32_16x16x32_bf16 v[156:159], a[204:207], v[52:55], v[156:159]\n"
"v_mfma_f32_16x16x32_bf16 v[156:159], a[200:203], v[252:255], v[156:159]\n"
"s_nop 3\n"
"v_cvt_pk_bf16_f32 v152, v152, v153\n"
"v_cvt_pk_bf16_f32 v153, v154, v155\n"
"v_mfma_f32_16x16x32_bf16 v[156:159], a[196:199], v[236:239], v[156:159]\n"
"v_mfma_f32_16x16x32_bf16 v[156:159], a[192:195], a[252:255], v[156:159]\n"
"s_waitcnt lgkmcnt(0)\n"
"v_mfma_f32_16x16x32_bf16 v[160:163], a[216:219], a[240:243], v[160:163]\n"
"ds_read_b64_tr_b16 a[192:193], v7 offset:24704\n"
"ds_read_b64_tr_b16 a[194:195], v7 offset:30848\n"
"v_mfma_f32_16x16x32_bf16 v[160:163], a[220:223], v[224:227], v[160:163]\n"
"ds_read_b64_tr_b16 a[196:197], v7 offset:128\n"
"ds_read_b64_tr_b16 a[198:199], v7 offset:6272\n"
"v_mfma_f32_16x16x32_bf16 v[160:163], a[224:227], v[240:243], v[160:163]\n"
"ds_read_b64_tr_b16 a[200:201], v3 offset:128\n"
"ds_read_b64_tr_b16 a[202:203], v3 offset:6272\n"
"v_mfma_f32_16x16x32_bf16 v[160:163], a[228:231], v[40:43], v[160:163]\n"
"ds_read_b64_tr_b16 a[204:205], v3 offset:12416\n"
"ds_read_b64_tr_b16 a[206:207], v3 offset:18560\n"
"v_mfma_f32_16x16x32_bf16 v[160:163], a[232:235], a[0:3], v[160:163]\n"
"ds_read_b64_tr_b16 a[208:209], v11 offset:12416\n"
"ds_read_b64_tr_b16 a[210:211], v11 offset:18560\n"
"v_mfma_f32_16x16x32_bf16 v[160:163], a[236:239], a[16:19], v[160:163]\n"
"ds_read_b64_tr_b16 a[212:213], v11 offset:24704\n"
"ds_read_b64_tr_b16 a[214:215], v11 offset:30848\n"
"v_mfma_f32_16x16x32_bf16 v[164:167], a[236:239], a[20:23], v[164:167]\n"
"v_cvt_pk_bf16_f32 v154, v156, v157\n"
"v_cvt_pk_bf16_f32 v155, v158, v159\n"
"v_mfma_f32_16x16x32_bf16 v[164:167], a[232:235], a[4:7], v[164:167]\n"
"buffer_store_dwordx4 v[152:155], v18, s[64:67], s25 offen offset:1024 sc1\n"
"v_mfma_f32_16x16x32_bf16 v[164:167], a[228:231], v[44:47], v[164:167]\n"
"v_mfma_f32_16x16x32_bf16 v[164:167], a[224:227], v[244:247], v[164:167]\n"
"s_add_u32 s25, s95, 163840\n"
"s_and_b32 s25, s25, 229376\n"
"v_mfma_f32_16x16x32_bf16 v[164:167], a[220:223], v[228:231], v[164:167]\n"
"s_add_u32 s25, s25, 10240\n"
"v_cvt_pk_bf16_f32 v160, v160, v161\n"
"v_mfma_f32_16x16x32_bf16 v[164:167], a[216:219], a[244:247], v[164:167]\n"
"v_cvt_pk_bf16_f32 v161, v162, v163\n"
"v_mfma_f32_16x16x32_bf16 v[168:171], a[216:219], a[248:251], v[168:171]\n"
"v_mfma_f32_16x16x32_bf16 v[168:171], a[220:223], v[232:235], v[168:171]\n"
"v_mfma_f32_16x16x32_bf16 v[168:171], a[224:227], v[248:251], v[168:171]\n"
"v_mfma_f32_16x16x32_bf16 v[168:171], a[228:231], v[48:51], v[168:171]\n"
"s_nop 2\n"
"v_cvt_pk_bf16_f32 v162, v164, v165\n"
"v_cvt_pk_bf16_f32 v163, v166, v167\n"
"v_mfma_f32_16x16x32_bf16 v[168:171], a[232:235], a[8:11], v[168:171]\n"
"buffer_store_dwordx4 v[160:163], v18, s[64:67], s25 offen sc1\n"
"v_mfma_f32_16x16x32_bf16 v[168:171], a[236:239], a[24:27], v[168:171]\n"
"v_mfma_f32_16x16x32_bf16 v[172:175], a[236:239], a[28:31], v[172:175]\n"
"v_mfma_f32_16x16x32_bf16 v[172:175], a[232:235], a[12:15], v[172:175]\n"
"v_mfma_f32_16x16x32_bf16 v[172:175], a[228:231], v[52:55], v[172:175]\n"
"v_mfma_f32_16x16x32_bf16 v[172:175], a[224:227], v[252:255], v[172:175]\n"
"s_nop 3\n"
"v_cvt_pk_bf16_f32 v168, v168, v169\n"
"v_cvt_pk_bf16_f32 v169, v170, v171\n"
"v_mfma_f32_16x16x32_bf16 v[172:175], a[220:223], v[236:239], v[172:175]\n"
"v_mfma_f32_16x16x32_bf16 v[172:175], a[216:219], a[252:255], v[172:175]\n"
"s_waitcnt lgkmcnt(0)\n"
"v_mfma_f32_16x16x32_bf16 v[176:179], a[192:195], a[240:243], v[176:179]\n"
"ds_read_b64_tr_b16 a[216:217], v4 offset:24832\n"
"ds_read_b64_tr_b16 a[218:219], v4 offset:30976\n"
"v_mfma_f32_16x16x32_bf16 v[176:179], a[196:199], v[224:227], v[176:179]\n"
"ds_read_b64_tr_b16 a[220:221], v4 offset:256\n"
"ds_read_b64_tr_b16 a[222:223], v4 offset:6400\n"
"v_mfma_f32_16x16x32_bf16 v[176:179], a[200:203], v[240:243], v[176:179]\n"
"ds_read_b64_tr_b16 a[224:225], v0 offset:256\n"
"ds_read_b64_tr_b16 a[226:227], v0 offset:6400\n"
"v_mfma_f32_16x16x32_bf16 v[176:179], a[204:207], v[40:43], v[176:179]\n"
"ds_read_b64_tr_b16 a[228:229], v0 offset:12544\n"
"ds_read_b64_tr_b16 a[230:231], v0 offset:18688\n"
"v_mfma_f32_16x16x32_bf16 v[176:179], a[208:211], a[0:3], v[176:179]\n"
"ds_read_b64_tr_b16 a[232:233], v8 offset:12544\n"
"ds_read_b64_tr_b16 a[234:235], v8 offset:18688\n"
"v_mfma_f32_16x16x32_bf16 v[176:179], a[212:215], a[16:19], v[176:179]\n"
"ds_read_b64_tr_b16 a[236:237], v8 offset:24832\n"
"ds_read_b64_tr_b16 a[238:239], v8 offset:30976\n"
"v_mfma_f32_16x16x32_bf16 v[180:183], a[212:215], a[20:23], v[180:183]\n"
"v_cvt_pk_bf16_f32 v170, v172, v173\n"
"v_cvt_pk_bf16_f32 v171, v174, v175\n"
"v_mfma_f32_16x16x32_bf16 v[180:183], a[208:211], a[4:7], v[180:183]\n"
"buffer_store_dwordx4 v[168:171], v18, s[64:67], s25 offen offset:1024 sc1\n"
"v_mfma_f32_16x16x32_bf16 v[180:183], a[204:207], v[44:47], v[180:183]\n"
"v_mfma_f32_16x16x32_bf16 v[180:183], a[200:203], v[244:247], v[180:183]\n"
"s_add_u32 s25, s95, 196608\n"
"s_and_b32 s25, s25, 229376\n"
"v_mfma_f32_16x16x32_bf16 v[180:183], a[196:199], v[228:231], v[180:183]\n"
"s_add_u32 s25, s25, 12288\n"
"v_cvt_pk_bf16_f32 v176, v176, v177\n"
"v_mfma_f32_16x16x32_bf16 v[180:183], a[192:195], a[244:247], v[180:183]\n"
"v_cvt_pk_bf16_f32 v177, v178, v179\n"
"v_mfma_f32_16x16x32_bf16 v[184:187], a[192:195], a[248:251], v[184:187]\n"
"v_mfma_f32_16x16x32_bf16 v[184:187], a[196:199], v[232:235], v[184:187]\n"
"v_mfma_f32_16x16x32_bf16 v[184:187], a[200:203], v[248:251], v[184:187]\n"
"v_mfma_f32_16x16x32_bf16 v[184:187], a[204:207], v[48:51], v[184:187]\n"
"s_nop 2\n"
"v_cvt_pk_bf16_f32 v178, v180, v181\n"
"v_cvt_pk_bf16_f32 v179, v182, v183\n"
"v_mfma_f32_16x16x32_bf16 v[184:187], a[208:211], a[8:11], v[184:187]\n"
"buffer_store_dwordx4 v[176:179], v18, s[64:67], s25 offen sc1\n"
"v_mfma_f32_16x16x32_bf16 v[184:187], a[212:215], a[24:27], v[184:187]\n"
"v_mfma_f32_16x16x32_bf16 v[188:191], a[212:215], a[28:31], v[188:191]\n"
"v_mfma_f32_16x16x32_bf16 v[188:191], a[208:211], a[12:15], v[188:191]\n"
"v_mfma_f32_16x16x32_bf16 v[188:191], a[204:207], v[52:55], v[188:191]\n"
"v_mfma_f32_16x16x32_bf16 v[188:191], a[200:203], v[252:255], v[188:191]\n"
"s_nop 3\n"
"v_cvt_pk_bf16_f32 v184, v184, v185\n"
"v_cvt_pk_bf16_f32 v185, v186, v187\n"
"v_mfma_f32_16x16x32_bf16 v[188:191], a[196:199], v[236:239], v[188:191]\n"
"v_mfma_f32_16x16x32_bf16 v[188:191], a[192:195], a[252:255], v[188:191]\n"
"s_waitcnt lgkmcnt(0)\n"
"v_mfma_f32_16x16x32_bf16 v[192:195], a[216:219], a[240:243], v[192:195]\n"
"ds_read_b64_tr_b16 a[192:193], v9 offset:37120\n"
"ds_read_b64_tr_b16 a[194:195], v9 offset:43264\n"
"v_mfma_f32_16x16x32_bf16 v[192:195], a[220:223], v[224:227], v[192:195]\n"
"ds_read_b64_tr_b16 a[196:197], v1 offset:24832\n"
"ds_read_b64_tr_b16 a[198:199], v1 offset:30976\n"
"v_mfma_f32_16x16x32_bf16 v[192:195], a[224:227], v[240:243], v[192:195]\n"
"ds_read_b64_tr_b16 a[200:201], v1 offset:37120\n"
"ds_read_b64_tr_b16 a[202:203], v1 offset:43264\n"
"v_mfma_f32_16x16x32_bf16 v[192:195], a[228:231], v[40:43], v[192:195]\n"
"ds_read_b64_tr_b16 a[204:205], v5 offset:37120\n"
"ds_read_b64_tr_b16 a[206:207], v5 offset:43264\n"
"v_mfma_f32_16x16x32_bf16 v[192:195], a[232:235], a[0:3], v[192:195]\n"
"ds_read_b64_tr_b16 a[208:209], v1 offset:49408\n"
"ds_read_b64_tr_b16 a[210:211], v1 offset:55552\n"
"v_mfma_f32_16x16x32_bf16 v[192:195], a[236:239], a[16:19], v[192:195]\n"
"ds_read_b64_tr_b16 a[212:213], v9 offset:256\n"
"ds_read_b64_tr_b16 a[214:215], v9 offset:6400\n"
"v_mfma_f32_16x16x32_bf16 v[196:199], a[236:239], a[20:23], v[196:199]\n"
"v_cvt_pk_bf16_f32 v186, v188, v189\n"
"v_cvt_pk_bf16_f32 v187, v190, v191\n"
"v_mfma_f32_16x16x32_bf16 v[196:199], a[232:235], a[4:7], v[196:199]\n"
"buffer_store_dwordx4 v[184:187], v18, s[64:67], s25 offen offset:1024 sc1\n"
"v_mfma_f32_16x16x32_bf16 v[196:199], a[228:231], v[44:47], v[196:199]\n"
"v_mfma_f32_16x16x32_bf16 v[196:199], a[224:227], v[244:247], v[196:199]\n"
"s_add_u32 s25, s95, 229376\n"
"s_and_b32 s25, s25, 229376\n"
"v_mfma_f32_16x16x32_bf16 v[196:199], a[220:223], v[228:231], v[196:199]\n"
"s_add_u32 s25, s25, 14336\n"
"v_cvt_pk_bf16_f32 v192, v192, v193\n"
"v_mfma_f32_16x16x32_bf16 v[196:199], a[216:219], a[244:247], v[196:199]\n"
"v_cvt_pk_bf16_f32 v193, v194, v195\n"
"v_mfma_f32_16x16x32_bf16 v[200:203], a[216:219], a[248:251], v[200:203]\n"
"v_mfma_f32_16x16x32_bf16 v[200:203], a[220:223], v[232:235], v[200:203]\n"
"v_mfma_f32_16x16x32_bf16 v[200:203], a[224:227], v[248:251], v[200:203]\n"
"v_mfma_f32_16x16x32_bf16 v[200:203], a[228:231], v[48:51], v[200:203]\n"
"s_nop 2\n"
"v_cvt_pk_bf16_f32 v194, v196, v197\n"
"v_cvt_pk_bf16_f32 v195, v198, v199\n"
"v_mfma_f32_16x16x32_bf16 v[200:203], a[232:235], a[8:11], v[200:203]\n"
"buffer_store_dwordx4 v[192:195], v18, s[64:67], s25 offen sc1\n"
"v_mfma_f32_16x16x32_bf16 v[200:203], a[236:239], a[24:27], v[200:203]\n"
"v_mfma_f32_16x16x32_bf16 v[204:207], a[236:239], a[28:31], v[204:207]\n"
"v_mfma_f32_16x16x32_bf16 v[204:207], a[232:235], a[12:15], v[204:207]\n"
"v_mfma_f32_16x16x32_bf16 v[204:207], a[228:231], v[52:55], v[204:207]\n"
"v_mfma_f32_16x16x32_bf16 v[204:207], a[224:227], v[252:255], v[204:207]\n"
"s_nop 3\n"
"v_cvt_pk_bf16_f32 v200, v200, v201\n"
"v_cvt_pk_bf16_f32 v201, v202, v203\n"
"v_mfma_f32_16x16x32_bf16 v[204:207], a[220:223], v[236:239], v[204:207]\n"
"v_mfma_f32_16x16x32_bf16 v[204:207], a[216:219], a[252:255], v[204:207]\n"
"s_waitcnt lgkmcnt(0)\n"
"v_mfma_f32_16x16x32_bf16 v[208:211], a[192:195], a[96:99], v[208:211]\n"
"ds_read_b64_tr_b16 a[216:217], v5 offset:24832\n"
"ds_read_b64_tr_b16 a[218:219], v5 offset:30976\n"
"v_mfma_f32_16x16x32_bf16 v[208:211], a[196:199], a[112:115], v[208:211]\n"
"ds_read_b64_tr_b16 a[220:221], v5 offset:256\n"
"ds_read_b64_tr_b16 a[222:223], v5 offset:6400\n"
"v_mfma_f32_16x16x32_bf16 v[208:211], a[200:203], a[128:131], v[208:211]\n"
"ds_read_b64_tr_b16 a[224:225], v1 offset:256\n"
"ds_read_b64_tr_b16 a[226:227], v1 offset:6400\n"
"v_mfma_f32_16x16x32_bf16 v[208:211], a[204:207], a[144:147], v[208:211]\n"
"ds_read_b64_tr_b16 a[228:229], v1 offset:12544\n"
"ds_read_b64_tr_b16 a[230:231], v1 offset:18688\n"
"v_mfma_f32_16x16x32_bf16 v[208:211], a[208:211], a[160:163], v[208:211]\n"
"ds_read_b64_tr_b16 a[232:233], v9 offset:12544\n"
"ds_read_b64_tr_b16 a[234:235], v9 offset:18688\n"
"v_mfma_f32_16x16x32_bf16 v[208:211], a[212:215], a[176:179], v[208:211]\n"
"ds_read_b64_tr_b16 a[236:237], v9 offset:24832\n"
"ds_read_b64_tr_b16 a[238:239], v9 offset:30976\n"
"v_mfma_f32_16x16x32_bf16 v[212:215], a[212:215], a[180:183], v[212:215]\n"
"v_cvt_pk_bf16_f32 v202, v204, v205\n"
"v_cvt_pk_bf16_f32 v203, v206, v207\n"
"v_mfma_f32_16x16x32_bf16 v[212:215], a[208:211], a[164:167], v[212:215]\n"
"buffer_store_dwordx4 v[200:203], v18, s[64:67], s25 offen offset:1024 sc1\n"
"v_mfma_f32_16x16x32_bf16 v[212:215], a[204:207], a[148:151], v[212:215]\n"
"s_and_b32 s30, 16711680, s93\n"
"v_add_u32 v12, 1, v61\n"
"v_mfma_f32_16x16x32_bf16 v[212:215], a[200:203], a[132:135], v[212:215]\n"
"v_cndmask_b32_e64 v13, 2.0, v60, s[30:31]\n"
"s_and_b32 s30, 16711680, s93\n"
"v_mfma_f32_16x16x32_bf16 v[212:215], a[196:199], a[116:119], v[212:215]\n"
"v_add_u32 v16, 2, v61\n"
"v_cndmask_b32_e64 v17, 2.0, v60, s[30:31]\n"
"v_mfma_f32_16x16x32_bf16 v[212:215], a[192:195], a[100:103], v[212:215]\n"
"s_mov_b32 s30, 16646144\n"
"v_cndmask_b32_e64 v14, 2.0, v60, s[30:31]\n"
"v_mfma_f32_16x16x32_bf16 v[216:219], a[192:195], a[104:107], v[216:219]\n"
"s_mov_b32 s34, 0\n"
"s_mov_b32 s29, 400000\n"
"v_mfma_f32_16x16x32_bf16 v[216:219], a[196:199], a[120:123], v[216:219]\n"
"v_mfma_f32_16x16x32_bf16 v[216:219], a[200:203], a[136:139], v[216:219]\n"
"v_mfma_f32_16x16x32_bf16 v[216:219], a[204:207], a[152:155], v[216:219]\n"
"v_mfma_f32_16x16x32_bf16 v[216:219], a[208:211], a[168:171], v[216:219]\n"
"v_mfma_f32_16x16x32_bf16 v[216:219], a[212:215], a[184:187], v[216:219]\n"
"v_mfma_f32_16x16x32_bf16 v[220:223], a[212:215], a[188:191], v[220:223]\n"
"buffer_load_dword v15, v14, s[72:75], 0 offen sc1\n"
"v_mfma_f32_16x16x32_bf16 v[220:223], a[208:211], a[172:175], v[220:223]\n"
"v_mfma_f32_16x16x32_bf16 v[220:223], a[204:207], a[156:159], v[220:223]\n"
"v_mfma_f32_16x16x32_bf16 v[220:223], a[200:203], a[140:143], v[220:223]\n"
"v_mfma_f32_16x16x32_bf16 v[220:223], a[196:199], a[124:127], v[220:223]\n"
"v_mfma_f32_16x16x32_bf16 v[220:223], a[192:195], a[108:111], v[220:223]\n"
"s_waitcnt lgkmcnt(0)\n"
"v_mfma_f32_16x16x32_bf16 v[208:211], a[216:219], a[240:243], v[208:211]\n"
"v_mfma_f32_16x16x32_bf16 v[208:211], a[220:223], v[224:227], v[208:211]\n"
"v_mfma_f32_16x16x32_bf16 v[208:211], a[224:227], v[240:243], v[208:211]\n"
"v_mfma_f32_16x16x32_bf16 v[208:211], a[228:231], v[40:43], v[208:211]\n"
"v_mfma_f32_16x16x32_bf16 v[208:211], a[232:235], a[0:3], v[208:211]\n"
"v_mfma_f32_16x16x32_bf16 v[208:211], a[236:239], a[16:19], v[208:211]\n"
"v_mfma_f32_16x16x32_bf16 v[212:215], a[236:239], a[20:23], v[212:215]\n"
"v_mfma_f32_16x16x32_bf16 v[212:215], a[232:235], a[4:7], v[212:215]\n"
"v_mfma_f32_16x16x32_bf16 v[212:215], a[228:231], v[44:47], v[212:215]\n"
"v_mfma_f32_16x16x32_bf16 v[212:215], a[224:227], v[244:247], v[212:215]\n"
"v_mfma_f32_16x16x32_bf16 v[212:215], a[220:223], v[228:231], v[212:215]\n"
"v_mfma_f32_16x16x32_bf16 v[212:215], a[216:219], a[244:247], v[212:215]\n"
"v_mfma_f32_16x16x32_bf16 v[216:219], a[216:219], a[248:251], v[216:219]\n"
"v_mfma_f32_16x16x32_bf16 v[216:219], a[220:223], v[232:235], v[216:219]\n"
"v_mfma_f32_16x16x32_bf16 v[216:219], a[224:227], v[248:251], v[216:219]\n"
"v_mfma_f32_16x16x32_bf16 v[216:219], a[228:231], v[48:51], v[216:219]\n"
"v_mfma_f32_16x16x32_bf16 v[216:219], a[232:235], a[8:11], v[216:219]\n"
"v_mfma_f32_16x16x32_bf16 v[216:219], a[236:239], a[24:27], v[216:219]\n"
"v_mfma_f32_16x16x32_bf16 v[220:223], a[236:239], a[28:31], v[220:223]\n"
"v_mfma_f32_16x16x32_bf16 v[220:223], a[232:235], a[12:15], v[220:223]\n"
"v_mfma_f32_16x16x32_bf16 v[220:223], a[228:231], v[52:55], v[220:223]\n"
"v_mfma_f32_16x16x32_bf16 v[220:223], a[224:227], v[252:255], v[220:223]\n"
"v_mfma_f32_16x16x32_bf16 v[220:223], a[220:223], v[236:239], v[220:223]\n"
"v_mfma_f32_16x16x32_bf16 v[220:223], a[216:219], a[252:255], v[220:223]\n"
"s_waitcnt vmcnt(0)\n"
"s_barrier\n"
"buffer_store_dword v12, v13, s[72:75], 0 offen sc1\n"
"buffer_load_dwordx2 v[64:65], v24, s[64:67], s18 offen sc1\n"
".Lmha_gl:\n"
"v_cmp_ne_u32 vcc, v15, v61\n"
"s_nop 4\n"
"s_and_b32 s3, vcc_lo, 0xfe0000\n"
"s_andn2_b32 s3, s3, s34\n"
"s_bitcmp1_b32 s3, 17\n"
"s_cbranch_scc0 .Lmha_gs1\n"
"buffer_load_dwordx4 v[80:83], v18, s[64:67], s14 offen offset:2048 sc1\n"
"buffer_load_dwordx4 v[84:87], v18, s[64:67], s14 offen offset:3072 sc1\n"
"buffer_load_dwordx2 v[66:67], v24, s[64:67], s18 offen offset:512 sc1\n"
".Lmha_gs1:\n"
"s_bitcmp1_b32 s3, 18\n"
"s_cbranch_scc0 .Lmha_gs2\n"
"buffer_load_dwordx4 v[88:91], v18, s[64:67], s15 offen sc1\n"
"buffer_load_dwordx4 v[92:95], v18, s[64:67], s15 offen offset:1024 sc1\n"
"buffer_load_dwordx2 v[68:69], v24, s[64:67], s18 offen offset:1024 sc1\n"
".Lmha_gs2:\n"
"s_bitcmp1_b32 s3, 19\n"
"s_cbranch_scc0 .Lmha_gs3\n"
"buffer_load_dwordx4 v[96:99], v18, s[64:67], s15 offen offset:2048 sc1\n"
"buffer_load_dwordx4 v[100:103], v18, s[64:67], s15 offen offset:3072 sc1\n"
"buffer_load_dwordx2 v[70:71], v24, s[64:67], s18 offen offset:1536 sc1\n"
".Lmha_gs3:\n"
"s_bitcmp1_b32 s3, 20\n"
"s_cbranch_scc0 .Lmha_gs4\n"
"buffer_load_dwordx4 v[104:107], v18, s[64:67], s16 offen sc1\n"
"buffer_load_dwordx4 v[108:111], v18, s[64:67], s16 offen offset:1024 sc1\n"
"buffer_load_dwordx2 v[72:73], v24, s[64:67], s18 offen offset:2048 sc1\n"
".Lmha_gs4:\n"
"s_bitcmp1_b32 s3, 21\n"
"s_cbranch_scc0 .Lmha_gs5\n"
"buffer_load_dwordx4 v[112:115], v18, s[64:67], s16 offen offset:2048 sc1\n"
"buffer_load_dwordx4 v[116:119], v18, s[64:67], s16 offen offset:3072 sc1\n"
"buffer_load_dwordx2 v[74:75], v24, s[64:67], s18 offen offset:2560 sc1\n"
".Lmha_gs5:\n"
"s_bitcmp1_b32 s3, 22\n"
"s_cbranch_scc0 .Lmha_gs6\n"
"buffer_load_dwordx4 v[120:123], v18, s[64:67], s17 offen sc1\n"
"buffer_load_dwordx4 v[124:127], v18, s[64:67], s17 offen offset:1024 sc1\n"
"buffer_load_dwordx2 v[76:77], v24, s[64:67], s18 offen offset:3072 sc1\n"
".Lmha_gs6:\n"
"s_bitcmp1_b32 s3, 23\n"
"s_cbranch_scc0 .Lmha_gs7\n"
"buffer_load_dwordx4 v[128:131], v18, s[64:67], s17 offen offset:2048 sc1\n"
"buffer_load_dwordx4 v[132:135], v18, s[64:67], s17 offen offset:3072 sc1\n"
"buffer_load_dwordx2 v[78:79], v24, s[64:67], s18 offen offset:3584 sc1\n"
".Lmha_gs7:\n"
"s_or_b32 s34, s34, s3\n"
"s_cmp_eq_u32 s34, 16646144\n"
"s_cbranch_scc1 .Lmha_gd\n"
"buffer_load_dword v15, v14, s[72:75], 0 offen sc1\n"
"s_sub_u32 s29, s29, 1\n"
"s_cmp_lg_u32 s29, 0\n"
"s_waitcnt vmcnt(0)\n"
"s_cbranch_scc1 .Lmha_gl\n"
".Lmha_gd:\n"
"buffer_store_dword v16, v17, s[72:75], 0 offen sc1\n"
"s_waitcnt vmcnt(1)\n"
"v_mfma_f32_16x16x32_bf16 v[224:227], v[34:37], v[64:67], 0\n"
"v_mfma_f32_16x16x32_bf16 v[224:227], v[34:37], v[68:71], v[224:227]\n"
"v_mfma_f32_16x16x32_bf16 v[224:227], v[34:37], v[72:75], v[224:227]\n"
"v_mfma_f32_16x16x32_bf16 v[224:227], v[34:37], v[76:79], v[224:227]\n"
"v_mfma_f32_16x16x32_bf16 v[208:211], v[36:39], v[80:83], v[208:211]\n"
"v_mfma_f32_16x16x32_bf16 v[212:215], v[32:35], v[80:83], v[212:215]\n"
"v_mfma_f32_16x16x32_bf16 v[216:219], v[36:39], v[84:87], v[216:219]\n"
"v_mfma_f32_16x16x32_bf16 v[220:223], v[32:35], v[84:87], v[220:223]\n"
"v_mfma_f32_16x16x32_bf16 v[208:211], v[36:39], v[88:91], v[208:211]\n"
"v_mfma_f32_16x16x32_bf16 v[212:215], v[32:35], v[88:91], v[212:215]\n"
"v_mfma_f32_16x16x32_bf16 v[216:219], v[36:39], v[92:95], v[216:219]\n"
"v_mfma_f32_16x16x32_bf16 v[220:223], v[32:35], v[92:95], v[220:223]\n"
"v_mfma_f32_16x16x32_bf16 v[208:211], v[36:39], v[96:99], v[208:211]\n"
"v_mfma_f32_16x16x32_bf16 v[212:215], v[32:35], v[96:99], v[212:215]\n"
"v_mfma_f32_16x16x32_bf16 v[216:219], v[36:39], v[100:103], v[216:219]\n"
"v_mfma_f32_16x16x32_bf16 v[220:223], v[32:35], v[100:103], v[220:223]\n"
"v_mfma_f32_16x16x32_bf16 v[208:211], v[36:39], v[104:107], v[208:211]\n"
"v_mfma_f32_16x16x32_bf16 v[212:215], v[32:35], v[104:107], v[212:215]\n"
"v_mfma_f32_16x16x32_bf16 v[216:219], v[36:39], v[108:111], v[216:219]\n"
"v_mfma_f32_16x16x32_bf16 v[220:223], v[32:35], v[108:111], v[220:223]\n"
"v_mfma_f32_16x16x32_bf16 v[208:211], v[36:39], v[112:115], v[208:211]\n"
"v_mfma_f32_16x16x32_bf16 v[212:215], v[32:35], v[112:115], v[212:215]\n"
"v_mfma_f32_16x16x32_bf16 v[216:219], v[36:39], v[116:119], v[216:219]\n"
"v_mfma_f32_16x16x32_bf16 v[220:223], v[32:35], v[116:119], v[220:223]\n"
"v_mfma_f32_16x16x32_bf16 v[208:211], v[36:39], v[120:123], v[208:211]\n"
"v_mfma_f32_16x16x32_bf16 v[212:215], v[32:35], v[120:123], v[212:215]\n"
"v_mfma_f32_16x16x32_bf16 v[216:219], v[36:39], v[124:127], v[216:219]\n"
"v_mfma_f32_16x16x32_bf16 v[220:223], v[32:35], v[124:127], v[220:223]\n"
"v_mfma_f32_16x16x32_bf16 v[208:211], v[36:39], v[128:131], v[208:211]\n"
"v_mfma_f32_16x16x32_bf16 v[212:215], v[32:35], v[128:131], v[212:215]\n"
"v_mfma_f32_16x16x32_bf16 v[216:219], v[36:39], v[132:135], v[216:219]\n"
"v_mfma_f32_16x16x32_bf16 v[220:223], v[32:35], v[132:135], v[220:223]\n"
"s_nop 4\n"
"v_cvt_pk_bf16_f32 v208, v208, v209\n"
"v_cvt_pk_bf16_f32 v209, v210, v211\n"
"buffer_store_dwordx2 v[208:209], v59, s[68:71], s8 offen\n"
"v_cvt_pk_bf16_f32 v212, v212, v213\n"
"v_cvt_pk_bf16_f32 v213, v214, v215\n"
"buffer_store_dwordx2 v[212:213], v59, s[68:71], s9 offen\n"
"v_cvt_pk_bf16_f32 v216, v216, v217\n"
"v_cvt_pk_bf16_f32 v217, v218, v219\n"
"buffer_store_dwordx2 v[216:217], v59, s[68:71], s10 offen\n"
"v_cvt_pk_bf16_f32 v220, v220, v221\n"
"v_cvt_pk_bf16_f32 v221, v222, v223\n"
"buffer_store_dwordx2 v[220:221], v59, s[68:71], s11 offen\n"
"v_cvt_pk_bf16_f32 v224, v224, v225\n"
"v_cvt_pk_bf16_f32 v225, v226, v227\n"
"buffer_store_dwordx2 v[224:225], v59, s[68:71], s12 offen\n"
"s_endpgm\n"
".Lmha_f_spin:\n"
"s_mov_b32 s29, 400000\n"
".Lmha_f_sl:\n"
"buffer_load_dword v15, v14, s[72:75], 0 offen sc1\n"
"s_waitcnt vmcnt(0)\n"
"v_cmp_ne_u32 vcc, v15, v61\n"
"s_nop 4\n"
"s_and_b32 s33, vcc_lo, s3\n"
"s_cmp_eq_u32 s33, s3\n"
"s_cbranch_scc1 .Lmha_f_ok\n"
"s_sleep 1\n"
"s_sub_u32 s29, s29, 1\n"
"s_cmp_lg_u32 s29, 0\n"
"s_cbranch_scc1 .Lmha_f_sl\n"
"s_branch .Lmha_f_ok\n"
".Lmha_cend:\n"
".Lmha_rare_tf:\n"
"s_mov_b32 s28, 0\n"
"s_mov_b32 s35, 0xff800000\n"
"s_branch .Lmha_rb0\n"
".Lmha_rare_t0:\n"
"s_mov_b32 s28, 1\n"
"s_mov_b32 s35, 0\n"
"s_branch .Lmha_rb1\n"
".Lmha_rare_l1:\n"
"s_mov_b32 s28, 2\n"
"s_mov_b32 s35, 0\n"
"s_branch .Lmha_rb0\n"
".Lmha_rare_l2:\n"
"s_mov_b32 s28, 3\n"
"s_mov_b32 s35, 0\n"
"s_branch .Lmha_rb1\n"
".Lmha_rare_l3:\n"
"s_mov_b32 s28, 4\n"
"s_mov_b32 s35, 0\n"
"s_branch .Lmha_rb0\n"
".Lmha_rare_l4:\n"
"s_mov_b32 s28, 5\n"
"s_mov_b32 s35, 0\n"
"s_branch .Lmha_rb1\n"
".Lmha_rare_l5:\n"
"s_mov_b32 s28, 6\n"
"s_mov_b32 s35, 0\n"
"s_branch .Lmha_rb0\n"
".Lmha_rare_l0:\n"
"s_mov_b32 s28, 7\n"
"s_mov_b32 s35, 0\n"
"s_branch .Lmha_rb1\n"
".Lmha_rb0:\n"
"s_nop 9\n"
"v_max3_f32 v208, v80, v81, v82\n"
"v_max3_f32 v212, v83, v112, v113\n"
"v_max3_f32 v209, v84, v85, v86\n"
"v_max3_f32 v213, v87, v116, v117\n"
"v_max3_f32 v210, v88, v89, v90\n"
"v_max3_f32 v214, v91, v120, v121\n"
"v_max3_f32 v211, v92, v93, v94\n"
"v_max3_f32 v215, v95, v124, v125\n"
"v_max3_f32 v208, v208, v114, v115\n"
"v_max3_f32 v209, v209, v118, v119\n"
"v_max3_f32 v210, v210, v122, v123\n"
"v_max3_f32 v211, v211, v126, v127\n"
"v_max_f32 v208, v208, v212\n"
"v_max_f32 v209, v209, v213\n"
"v_max_f32 v210, v210, v214\n"
"v_max_f32 v211, v211, v215\n"
"v_mov_b32 v212, v208\n"
"v_mov_b32 v213, v209\n"
"v_mov_b32 v214, v210\n"
"v_mov_b32 v215, v211\n"
"v_permlane32_swap_b32 v208, v212\n"
"v_permlane32_swap_b32 v209, v213\n"
"v_permlane32_swap_b32 v210, v214\n"
"v_permlane32_swap_b32 v211, v215\n"
"v_max_f32 v208, v208, v212\n"
"v_max_f32 v209, v209, v213\n"
"v_max_f32 v210, v210, v214\n"
"v_max_f32 v211, v211, v215\n"
"v_mov_b32 v212, v208\n"
"v_mov_b32 v213, v209\n"
"v_mov_b32 v214, v210\n"
"v_mov_b32 v215, v211\n"
"v_permlane16_swap_b32 v208, v212\n"
"v_permlane16_swap_b32 v209, v213\n"
"v_permlane16_swap_b32 v210, v214\n"
"v_permlane16_swap_b32 v211, v215\n"
"v_max_f32 v208, v208, v212\n"
"v_max_f32 v209, v209, v213\n"
"v_max_f32 v210, v210, v214\n"
"v_max_f32 v211, v211, v215\n"
"v_max_f32 v208, s35, v208\n"
"v_max_f32 v209, s35, v209\n"
"v_max_f32 v210, s35, v210\n"
"v_max_f32 v211, s35, v211\n"
"v_exp_f32_e64 v216, -v208\n"
"v_sub_f32 v40, v40, v208\n"
"v_sub_f32 v41, v41, v208\n"
"v_sub_f32 v42, v42, v208\n"
"v_sub_f32 v43, v43, v208\n"
"v_sub_f32 v80, v80, v208\n"
"v_sub_f32 v81, v81, v208\n"
"v_sub_f32 v82, v82, v208\n"
"v_sub_f32 v83, v83, v208\n"
"v_sub_f32 v112, v112, v208\n"
"v_sub_f32 v113, v113, v208\n"
"v_sub_f32 v114, v114, v208\n"
"v_sub_f32 v115, v115, v208\n"
"v_mul_f32 v20, v20, v216\n"
"v_exp_f32_e64 v217, -v209\n"
"v_sub_f32 v44, v44, v209\n"
"v_sub_f32 v45, v45, v209\n"
"v_sub_f32 v46, v46, v209\n"
"v_sub_f32 v47, v47, v209\n"
"v_sub_f32 v84, v84, v209\n"
"v_sub_f32 v85, v85, v209\n"
"v_sub_f32 v86, v86, v209\n"
"v_sub_f32 v87, v87, v209\n"
"v_sub_f32 v116, v116, v209\n"
"v_sub_f32 v117, v117, v209\n"
"v_sub_f32 v118, v118, v209\n"
"v_sub_f32 v119, v119, v209\n"
"v_mul_f32 v21, v21, v217\n"
"v_exp_f32_e64 v218, -v210\n"
"v_sub_f32 v48, v48, v210\n"
"v_sub_f32 v49, v49, v210\n"
"v_sub_f32 v50, v50, v210\n"
"v_sub_f32 v51, v51, v210\n"
"v_sub_f32 v88, v88, v210\n"
"v_sub_f32 v89, v89, v210\n"
"v_sub_f32 v90, v90, v210\n"
"v_sub_f32 v91, v91, v210\n"
"v_sub_f32 v120, v120, v210\n"
"v_sub_f32 v121, v121, v210\n"
"v_sub_f32 v122, v122, v210\n"
"v_sub_f32 v123, v123, v210\n"
"v_mul_f32 v22, v22, v218\n"
"v_exp_f32_e64 v219, -v211\n"
"v_sub_f32 v52, v52, v211\n"
"v_sub_f32 v53, v53, v211\n"
"v_sub_f32 v54, v54, v211\n"
"v_sub_f32 v55, v55, v211\n"
"v_sub_f32 v92, v92, v211\n"
"v_sub_f32 v93, v93, v211\n"
"v_sub_f32 v94, v94, v211\n"
"v_sub_f32 v95, v95, v211\n"
"v_sub_f32 v124, v124, v211\n"
"v_sub_f32 v125, v125, v211\n"
"v_sub_f32 v126, v126, v211\n"
"v_sub_f32 v127, v127, v211\n"
"v_mul_f32 v23, v23, v219\n"
"v_exp_f32 v32, v80\n"
"v_exp_f32 v33, v81\n"
"v_exp_f32 v34, v82\n"
"v_exp_f32 v35, v83\n"
"v_add_f32 v24, v32, v33\n"
"v_cvt_pk_bf16_f32 v144, v32, v33\n"
"v_exp_f32 v36, v84\n"
"v_add_f32 v24, v24, v34\n"
"v_exp_f32 v37, v85\n"
"v_add_f32 v24, v24, v35\n"
"v_cvt_pk_bf16_f32 v145, v34, v35\n"
"v_exp_f32 v38, v86\n"
"v_exp_f32 v39, v87\n"
"v_add_f32 v25, v36, v37\n"
"v_cvt_pk_bf16_f32 v148, v36, v37\n"
"v_exp_f32 v32, v88\n"
"v_add_f32 v25, v25, v38\n"
"v_exp_f32 v33, v89\n"
"v_add_f32 v25, v25, v39\n"
"v_cvt_pk_bf16_f32 v149, v38, v39\n"
"v_exp_f32 v34, v90\n"
"v_exp_f32 v35, v91\n"
"v_add_f32 v26, v32, v33\n"
"v_cvt_pk_bf16_f32 v152, v32, v33\n"
"v_exp_f32 v36, v92\n"
"v_add_f32 v26, v26, v34\n"
"v_exp_f32 v37, v93\n"
"v_add_f32 v26, v26, v35\n"
"v_cvt_pk_bf16_f32 v153, v34, v35\n"
"v_exp_f32 v38, v94\n"
"v_exp_f32 v39, v95\n"
"v_add_f32 v27, v36, v37\n"
"v_cvt_pk_bf16_f32 v156, v36, v37\n"
"v_exp_f32 v32, v112\n"
"v_add_f32 v27, v27, v38\n"
"v_exp_f32 v33, v113\n"
"v_add_f32 v27, v27, v39\n"
"v_cvt_pk_bf16_f32 v157, v38, v39\n"
"v_exp_f32 v34, v114\n"
"v_add_f32 v24, v24, v32\n"
"v_exp_f32 v35, v115\n"
"v_add_f32 v24, v24, v33\n"
"v_cvt_pk_bf16_f32 v146, v32, v33\n"
"v_exp_f32 v36, v116\n"
"v_add_f32 v24, v24, v34\n"
"v_exp_f32 v37, v117\n"
"v_add_f32 v24, v24, v35\n"
"v_cvt_pk_bf16_f32 v147, v34, v35\n"
"v_exp_f32 v38, v118\n"
"v_add_f32 v25, v25, v36\n"
"v_exp_f32 v39, v119\n"
"v_add_f32 v25, v25, v37\n"
"v_cvt_pk_bf16_f32 v150, v36, v37\n"
"v_exp_f32 v32, v120\n"
"v_add_f32 v25, v25, v38\n"
"v_exp_f32 v33, v121\n"
"v_add_f32 v25, v25, v39\n"
"v_cvt_pk_bf16_f32 v151, v38, v39\n"
"v_exp_f32 v34, v122\n"
"v_add_f32 v26, v26, v32\n"
"v_exp_f32 v35, v123\n"
"v_add_f32 v26, v26, v33\n"
"v_cvt_pk_bf16_f32 v154, v32, v33\n"
"v_exp_f32 v36, v124\n"
"v_add_f32 v26, v26, v34\n"
"v_exp_f32 v37, v125\n"
"v_add_f32 v26, v26, v35\n"
"v_cvt_pk_bf16_f32 v155, v34, v35\n"
"v_exp_f32 v38, v126\n"
"v_add_f32 v27, v27, v36\n"
"v_exp_f32 v39, v127\n"
"v_add_f32 v27, v27, v37\n"
"v_cvt_pk_bf16_f32 v158, v36, v37\n"
"v_add_f32 v27, v27, v38\n"
"v_add_f32 v27, v27, v39\n"
"v_cvt_pk_bf16_f32 v159, v38, v39\n"
"s_nop 2\n"
"s_cmp_eq_u32 s28, 0\n"
"s_cbranch_scc1 .Lmha_ret_tf\n"
"s_branch .Lmha_ro\n"
".Lmha_rb1:\n"
"s_nop 9\n"
"v_max3_f32 v208, v80, v81, v82\n"
"v_max3_f32 v212, v83, v112, v113\n"
"v_max3_f32 v209, v84, v85, v86\n"
"v_max3_f32 v213, v87, v116, v117\n"
"v_max3_f32 v210, v88, v89, v90\n"
"v_max3_f32 v214, v91, v120, v121\n"
"v_max3_f32 v211, v92, v93, v94\n"
"v_max3_f32 v215, v95, v124, v125\n"
"v_max3_f32 v208, v208, v114, v115\n"
"v_max3_f32 v209, v209, v118, v119\n"
"v_max3_f32 v210, v210, v122, v123\n"
"v_max3_f32 v211, v211, v126, v127\n"
"v_max_f32 v208, v208, v212\n"
"v_max_f32 v209, v209, v213\n"
"v_max_f32 v210, v210, v214\n"
"v_max_f32 v211, v211, v215\n"
"v_mov_b32 v212, v208\n"
"v_mov_b32 v213, v209\n"
"v_mov_b32 v214, v210\n"
"v_mov_b32 v215, v211\n"
"v_permlane32_swap_b32 v208, v212\n"
"v_permlane32_swap_b32 v209, v213\n"
"v_permlane32_swap_b32 v210, v214\n"
"v_permlane32_swap_b32 v211, v215\n"
"v_max_f32 v208, v208, v212\n"
"v_max_f32 v209, v209, v213\n"
"v_max_f32 v210, v210, v214\n"
"v_max_f32 v211, v211, v215\n"
"v_mov_b32 v212, v208\n"
"v_mov_b32 v213, v209\n"
"v_mov_b32 v214, v210\n"
"v_mov_b32 v215, v211\n"
"v_permlane16_swap_b32 v208, v212\n"
"v_permlane16_swap_b32 v209, v213\n"
"v_permlane16_swap_b32 v210, v214\n"
"v_permlane16_swap_b32 v211, v215\n"
"v_max_f32 v208, v208, v212\n"
"v_max_f32 v209, v209, v213\n"
"v_max_f32 v210, v210, v214\n"
"v_max_f32 v211, v211, v215\n"
"v_max_f32 v208, s35, v208\n"
"v_max_f32 v209, s35, v209\n"
"v_max_f32 v210, s35, v210\n"
"v_max_f32 v211, s35, v211\n"
"v_exp_f32_e64 v216, -v208\n"
"v_sub_f32 v40, v40, v208\n"
"v_sub_f32 v41, v41, v208\n"
"v_sub_f32 v42, v42, v208\n"
"v_sub_f32 v43, v43, v208\n"
"v_sub_f32 v80, v80, v208\n"
"v_sub_f32 v81, v81, v208\n"
"v_sub_f32 v82, v82, v208\n"
"v_sub_f32 v83, v83, v208\n"
"v_sub_f32 v112, v112, v208\n"
"v_sub_f32 v113, v113, v208\n"
"v_sub_f32 v114, v114, v208\n"
"v_sub_f32 v115, v115, v208\n"
"v_mul_f32 v20, v20, v216\n"
"v_exp_f32_e64 v217, -v209\n"
"v_sub_f32 v44, v44, v209\n"
"v_sub_f32 v45, v45, v209\n"
"v_sub_f32 v46, v46, v209\n"
"v_sub_f32 v47, v47, v209\n"
"v_sub_f32 v84, v84, v209\n"
"v_sub_f32 v85, v85, v209\n"
"v_sub_f32 v86, v86, v209\n"
"v_sub_f32 v87, v87, v209\n"
"v_sub_f32 v116, v116, v209\n"
"v_sub_f32 v117, v117, v209\n"
"v_sub_f32 v118, v118, v209\n"
"v_sub_f32 v119, v119, v209\n"
"v_mul_f32 v21, v21, v217\n"
"v_exp_f32_e64 v218, -v210\n"
"v_sub_f32 v48, v48, v210\n"
"v_sub_f32 v49, v49, v210\n"
"v_sub_f32 v50, v50, v210\n"
"v_sub_f32 v51, v51, v210\n"
"v_sub_f32 v88, v88, v210\n"
"v_sub_f32 v89, v89, v210\n"
"v_sub_f32 v90, v90, v210\n"
"v_sub_f32 v91, v91, v210\n"
"v_sub_f32 v120, v120, v210\n"
"v_sub_f32 v121, v121, v210\n"
"v_sub_f32 v122, v122, v210\n"
"v_sub_f32 v123, v123, v210\n"
"v_mul_f32 v22, v22, v218\n"
"v_exp_f32_e64 v219, -v211\n"
"v_sub_f32 v52, v52, v211\n"
"v_sub_f32 v53, v53, v211\n"
"v_sub_f32 v54, v54, v211\n"
"v_sub_f32 v55, v55, v211\n"
"v_sub_f32 v92, v92, v211\n"
"v_sub_f32 v93, v93, v211\n"
"v_sub_f32 v94, v94, v211\n"
"v_sub_f32 v95, v95, v211\n"
"v_sub_f32 v124, v124, v211\n"
"v_sub_f32 v125, v125, v211\n"
"v_sub_f32 v126, v126, v211\n"
"v_sub_f32 v127, v127, v211\n"
"v_mul_f32 v23, v23, v219\n"
"v_exp_f32 v32, v80\n"
"v_exp_f32 v33, v81\n"
"v_exp_f32 v34, v82\n"
"v_exp_f32 v35, v83\n"
"v_add_f32 v24, v32, v33\n"
"v_cvt_pk_bf16_f32 v176, v32, v33\n"
"v_exp_f32 v36, v84\n"
"v_add_f32 v24, v24, v34\n"
"v_exp_f32 v37, v85\n"
"v_add_f32 v24, v24, v35\n"
"v_cvt_pk_bf16_f32 v177, v34, v35\n"
"v_exp_f32 v38, v86\n"
"v_exp_f32 v39, v87\n"
"v_add_f32 v25, v36, v37\n"
"v_cvt_pk_bf16_f32 v180, v36, v37\n"
"v_exp_f32 v32, v88\n"
"v_add_f32 v25, v25, v38\n"
"v_exp_f32 v33, v89\n"
"v_add_f32 v25, v25, v39\n"
"v_cvt_pk_bf16_f32 v181, v38, v39\n"
"v_exp_f32 v34, v90\n"
"v_exp_f32 v35, v91\n"
"v_add_f32 v26, v32, v33\n"
"v_cvt_pk_bf16_f32 v184, v32, v33\n"
"v_exp_f32 v36, v92\n"
"v_add_f32 v26, v26, v34\n"
"v_exp_f32 v37, v93\n"
"v_add_f32 v26, v26, v35\n"
"v_cvt_pk_bf16_f32 v185, v34, v35\n"
"v_exp_f32 v38, v94\n"
"v_exp_f32 v39, v95\n"
"v_add_f32 v27, v36, v37\n"
"v_cvt_pk_bf16_f32 v188, v36, v37\n"
"v_exp_f32 v32, v112\n"
"v_add_f32 v27, v27, v38\n"
"v_exp_f32 v33, v113\n"
"v_add_f32 v27, v27, v39\n"
"v_cvt_pk_bf16_f32 v189, v38, v39\n"
"v_exp_f32 v34, v114\n"
"v_add_f32 v24, v24, v32\n"
"v_exp_f32 v35, v115\n"
"v_add_f32 v24, v24, v33\n"
"v_cvt_pk_bf16_f32 v178, v32, v33\n"
"v_exp_f32 v36, v116\n"
"v_add_f32 v24, v24, v34\n"
"v_exp_f32 v37, v117\n"
"v_add_f32 v24, v24, v35\n"
"v_cvt_pk_bf16_f32 v179, v34, v35\n"
"v_exp_f32 v38, v118\n"
"v_add_f32 v25, v25, v36\n"
"v_exp_f32 v39, v119\n"
"v_add_f32 v25, v25, v37\n"
"v_cvt_pk_bf16_f32 v182, v36, v37\n"
"v_exp_f32 v32, v120\n"
"v_add_f32 v25, v25, v38\n"
"v_exp_f32 v33, v121\n"
"v_add_f32 v25, v25, v39\n"
"v_cvt_pk_bf16_f32 v183, v38, v39\n"
"v_exp_f32 v34, v122\n"
"v_add_f32 v26, v26, v32\n"
"v_exp_f32 v35, v123\n"
"v_add_f32 v26, v26, v33\n"
"v_cvt_pk_bf16_f32 v186, v32, v33\n"
"v_exp_f32 v36, v124\n"
"v_add_f32 v26, v26, v34\n"
"v_exp_f32 v37, v125\n"
"v_add_f32 v26, v26, v35\n"
"v_cvt_pk_bf16_f32 v187, v34, v35\n"
"v_exp_f32 v38, v126\n"
"v_add_f32 v27, v27, v36\n"
"v_exp_f32 v39, v127\n"
"v_add_f32 v27, v27, v37\n"
"v_cvt_pk_bf16_f32 v190, v36, v37\n"
"v_add_f32 v27, v27, v38\n"
"v_add_f32 v27, v27, v39\n"
"v_cvt_pk_bf16_f32 v191, v38, v39\n"
"s_nop 2\n"
"s_cmp_eq_u32 s28, 0\n"
"s_cbranch_scc1 .Lmha_ret_tf\n"
"s_branch .Lmha_ro\n"
".Lmha_ro:\n"
"s_nop 1\n"
"v_accvgpr_read_b32 v240, a0\n"
"v_accvgpr_read_b32 v241, a1\n"
"v_accvgpr_read_b32 v242, a2\n"
"v_accvgpr_read_b32 v243, a3\n"
"v_mul_f32 v240, v240, v216\n"
"v_mul_f32 v241, v241, v216\n"
"v_mul_f32 v242, v242, v216\n"
"v_mul_f32 v243, v243, v216\n"
"v_accvgpr_write_b32 a0, v240\n"
"v_accvgpr_write_b32 a1, v241\n"
"v_accvgpr_write_b32 a2, v242\n"
"v_accvgpr_write_b32 a3, v243\n"
"v_accvgpr_read_b32 v244, a4\n"
"v_accvgpr_read_b32 v245, a5\n"
"v_accvgpr_read_b32 v246, a6\n"
"v_accvgpr_read_b32 v247, a7\n"
"v_mul_f32 v244, v244, v217\n"
"v_mul_f32 v245, v245, v217\n"
"v_mul_f32 v246, v246, v217\n"
"v_mul_f32 v247, v247, v217\n"
"v_accvgpr_write_b32 a4, v244\n"
"v_accvgpr_write_b32 a5, v245\n"
"v_accvgpr_write_b32 a6, v246\n"
"v_accvgpr_write_b32 a7, v247\n"
"v_accvgpr_read_b32 v248, a8\n"
"v_accvgpr_read_b32 v249, a9\n"
"v_accvgpr_read_b32 v250, a10\n"
"v_accvgpr_read_b32 v251, a11\n"
"v_mul_f32 v248, v248, v218\n"
"v_mul_f32 v249, v249, v218\n"
"v_mul_f32 v250, v250, v218\n"
"v_mul_f32 v251, v251, v218\n"
"v_accvgpr_write_b32 a8, v248\n"
"v_accvgpr_write_b32 a9, v249\n"
"v_accvgpr_write_b32 a10, v250\n"
"v_accvgpr_write_b32 a11, v251\n"
"v_accvgpr_read_b32 v240, a12\n"
"v_accvgpr_read_b32 v241, a13\n"
"v_accvgpr_read_b32 v242, a14\n"
"v_accvgpr_read_b32 v243, a15\n"
"v_mul_f32 v240, v240, v219\n"
"v_mul_f32 v241, v241, v219\n"
"v_mul_f32 v242, v242, v219\n"
"v_mul_f32 v243, v243, v219\n"
"v_accvgpr_write_b32 a12, v240\n"
"v_accvgpr_write_b32 a13, v241\n"
"v_accvgpr_write_b32 a14, v242\n"
"v_accvgpr_write_b32 a15, v243\n"
"v_accvgpr_read_b32 v244, a16\n"
"v_accvgpr_read_b32 v245, a17\n"
"v_accvgpr_read_b32 v246, a18\n"
"v_accvgpr_read_b32 v247, a19\n"
"v_mul_f32 v244, v244, v216\n"
"v_mul_f32 v245, v245, v216\n"
"v_mul_f32 v246, v246, v216\n"
"v_mul_f32 v247, v247, v216\n"
"v_accvgpr_write_b32 a16, v244\n"
"v_accvgpr_write_b32 a17, v245\n"
"v_accvgpr_write_b32 a18, v246\n"
"v_accvgpr_write_b32 a19, v247\n"
"v_accvgpr_read_b32 v248, a20\n"
"v_accvgpr_read_b32 v249, a21\n"
"v_accvgpr_read_b32 v250, a22\n"
"v_accvgpr_read_b32 v251, a23\n"
"v_mul_f32 v248, v248, v217\n"
"v_mul_f32 v249, v249, v217\n"
"v_mul_f32 v250, v250, v217\n"
"v_mul_f32 v251, v251, v217\n"
"v_accvgpr_write_b32 a20, v248\n"
"v_accvgpr_write_b32 a21, v249\n"
"v_accvgpr_write_b32 a22, v250\n"
"v_accvgpr_write_b32 a23, v251\n"
"v_accvgpr_read_b32 v240, a24\n"
"v_accvgpr_read_b32 v241, a25\n"
"v_accvgpr_read_b32 v242, a26\n"
"v_accvgpr_read_b32 v243, a27\n"
"v_mul_f32 v240, v240, v218\n"
"v_mul_f32 v241, v241, v218\n"
"v_mul_f32 v242, v242, v218\n"
"v_mul_f32 v243, v243, v218\n"
"v_accvgpr_write_b32 a24, v240\n"
"v_accvgpr_write_b32 a25, v241\n"
"v_accvgpr_write_b32 a26, v242\n"
"v_accvgpr_write_b32 a27, v243\n"
"v_accvgpr_read_b32 v244, a28\n"
"v_accvgpr_read_b32 v245, a29\n"
"v_accvgpr_read_b32 v246, a30\n"
"v_accvgpr_read_b32 v247, a31\n"
"v_mul_f32 v244, v244, v219\n"
"v_mul_f32 v245, v245, v219\n"
"v_mul_f32 v246, v246, v219\n"
"v_mul_f32 v247, v247, v219\n"
"v_accvgpr_write_b32 a28, v244\n"
"v_accvgpr_write_b32 a29, v245\n"
"v_accvgpr_write_b32 a30, v246\n"
"v_accvgpr_write_b32 a31, v247\n"
"v_accvgpr_read_b32 v248, a32\n"
"v_accvgpr_read_b32 v249, a33\n"
"v_accvgpr_read_b32 v250, a34\n"
"v_accvgpr_read_b32 v251, a35\n"
"v_mul_f32 v248, v248, v216\n"
"v_mul_f32 v249, v249, v216\n"
"v_mul_f32 v250, v250, v216\n"
"v_mul_f32 v251, v251, v216\n"
"v_accvgpr_write_b32 a32, v248\n"
"v_accvgpr_write_b32 a33, v249\n"
"v_accvgpr_write_b32 a34, v250\n"
"v_accvgpr_write_b32 a35, v251\n"
"v_accvgpr_read_b32 v240, a36\n"
"v_accvgpr_read_b32 v241, a37\n"
"v_accvgpr_read_b32 v242, a38\n"
"v_accvgpr_read_b32 v243, a39\n"
"v_mul_f32 v240, v240, v217\n"
"v_mul_f32 v241, v241, v217\n"
"v_mul_f32 v242, v242, v217\n"
"v_mul_f32 v243, v243, v217\n"
"v_accvgpr_write_b32 a36, v240\n"
"v_accvgpr_write_b32 a37, v241\n"
"v_accvgpr_write_b32 a38, v242\n"
"v_accvgpr_write_b32 a39, v243\n"
"v_accvgpr_read_b32 v244, a40\n"
"v_accvgpr_read_b32 v245, a41\n"
"v_accvgpr_read_b32 v246, a42\n"
"v_accvgpr_read_b32 v247, a43\n"
"v_mul_f32 v244, v244, v218\n"
"v_mul_f32 v245, v245, v218\n"
"v_mul_f32 v246, v246, v218\n"
"v_mul_f32 v247, v247, v218\n"
"v_accvgpr_write_b32 a40, v244\n"
"v_accvgpr_write_b32 a41, v245\n"
"v_accvgpr_write_b32 a42, v246\n"
"v_accvgpr_write_b32 a43, v247\n"
"v_accvgpr_read_b32 v248, a44\n"
"v_accvgpr_read_b32 v249, a45\n"
"v_accvgpr_read_b32 v250, a46\n"
"v_accvgpr_read_b32 v251, a47\n"
"v_mul_f32 v248, v248, v219\n"
"v_mul_f32 v249, v249, v219\n"
"v_mul_f32 v250, v250, v219\n"
"v_mul_f32 v251, v251, v219\n"
"v_accvgpr_write_b32 a44, v248\n"
"v_accvgpr_write_b32 a45, v249\n"
"v_accvgpr_write_b32 a46, v250\n"
"v_accvgpr_write_b32 a47, v251\n"
"v_accvgpr_read_b32 v240, a48\n"
"v_accvgpr_read_b32 v241, a49\n"
"v_accvgpr_read_b32 v242, a50\n"
"v_accvgpr_read_b32 v243, a51\n"
"v_mul_f32 v240, v240, v216\n"
"v_mul_f32 v241, v241, v216\n"
"v_mul_f32 v242, v242, v216\n"
"v_mul_f32 v243, v243, v216\n"
"v_accvgpr_write_b32 a48, v240\n"
"v_accvgpr_write_b32 a49, v241\n"
"v_accvgpr_write_b32 a50, v242\n"
"v_accvgpr_write_b32 a51, v243\n"
"v_accvgpr_read_b32 v244, a52\n"
"v_accvgpr_read_b32 v245, a53\n"
"v_accvgpr_read_b32 v246, a54\n"
"v_accvgpr_read_b32 v247, a55\n"
"v_mul_f32 v244, v244, v217\n"
"v_mul_f32 v245, v245, v217\n"
"v_mul_f32 v246, v246, v217\n"
"v_mul_f32 v247, v247, v217\n"
"v_accvgpr_write_b32 a52, v244\n"
"v_accvgpr_write_b32 a53, v245\n"
"v_accvgpr_write_b32 a54, v246\n"
"v_accvgpr_write_b32 a55, v247\n"
"v_accvgpr_read_b32 v248, a56\n"
"v_accvgpr_read_b32 v249, a57\n"
"v_accvgpr_read_b32 v250, a58\n"
"v_accvgpr_read_b32 v251, a59\n"
"v_mul_f32 v248, v248, v218\n"
"v_mul_f32 v249, v249, v218\n"
"v_mul_f32 v250, v250, v218\n"
"v_mul_f32 v251, v251, v218\n"
"v_accvgpr_write_b32 a56, v248\n"
"v_accvgpr_write_b32 a57, v249\n"
"v_accvgpr_write_b32 a58, v250\n"
"v_accvgpr_write_b32 a59, v251\n"
"v_accvgpr_read_b32 v240, a60\n"
"v_accvgpr_read_b32 v241, a61\n"
"v_accvgpr_read_b32 v242, a62\n"
"v_accvgpr_read_b32 v243, a63\n"
"v_mul_f32 v240, v240, v219\n"
"v_mul_f32 v241, v241, v219\n"
"v_mul_f32 v242, v242, v219\n"
"v_mul_f32 v243, v243, v219\n"
"v_accvgpr_write_b32 a60, v240\n"
"v_accvgpr_write_b32 a61, v241\n"
"v_accvgpr_write_b32 a62, v242\n"
"v_accvgpr_write_b32 a63, v243\n"
"v_accvgpr_read_b32 v244, a64\n"
"v_accvgpr_read_b32 v245, a65\n"
"v_accvgpr_read_b32 v246, a66\n"
"v_accvgpr_read_b32 v247, a67\n"
"v_mul_f32 v244, v244, v216\n"
"v_mul_f32 v245, v245, v216\n"
"v_mul_f32 v246, v246, v216\n"
"v_mul_f32 v247, v247, v216\n"
"v_accvgpr_write_b32 a64, v244\n"
"v_accvgpr_write_b32 a65, v245\n"
"v_accvgpr_write_b32 a66, v246\n"
"v_accvgpr_write_b32 a67, v247\n"
"v_accvgpr_read_b32 v248, a68\n"
"v_accvgpr_read_b32 v249, a69\n"
"v_accvgpr_read_b32 v250, a70\n"
"v_accvgpr_read_b32 v251, a71\n"
"v_mul_f32 v248, v248, v217\n"
"v_mul_f32 v249, v249, v217\n"
"v_mul_f32 v250, v250, v217\n"
"v_mul_f32 v251, v251, v217\n"
"v_accvgpr_write_b32 a68, v248\n"
"v_accvgpr_write_b32 a69, v249\n"
"v_accvgpr_write_b32 a70, v250\n"
"v_accvgpr_write_b32 a71, v251\n"
"v_accvgpr_read_b32 v240, a72\n"
"v_accvgpr_read_b32 v241, a73\n"
"v_accvgpr_read_b32 v242, a74\n"
"v_accvgpr_read_b32 v243, a75\n"
"v_mul_f32 v240, v240, v218\n"
"v_mul_f32 v241, v241, v218\n"
"v_mul_f32 v242, v242, v218\n"
"v_mul_f32 v243, v243, v218\n"
"v_accvgpr_write_b32 a72, v240\n"
"v_accvgpr_write_b32 a73, v241\n"
"v_accvgpr_write_b32 a74, v242\n"
"v_accvgpr_write_b32 a75, v243\n"
"v_accvgpr_read_b32 v244, a76\n"
"v_accvgpr_read_b32 v245, a77\n"
"v_accvgpr_read_b32 v246, a78\n"
"v_accvgpr_read_b32 v247, a79\n"
"v_mul_f32 v244, v244, v219\n"
"v_mul_f32 v245, v245, v219\n"
"v_mul_f32 v246, v246, v219\n"
"v_mul_f32 v247, v247, v219\n"
"v_accvgpr_write_b32 a76, v244\n"
"v_accvgpr_write_b32 a77, v245\n"
"v_accvgpr_write_b32 a78, v246\n"
"v_accvgpr_write_b32 a79, v247\n"
"v_accvgpr_read_b32 v248, a80\n"
"v_accvgpr_read_b32 v249, a81\n"
"v_accvgpr_read_b32 v250, a82\n"
"v_accvgpr_read_b32 v251, a83\n"
"v_mul_f32 v248, v248, v216\n"
"v_mul_f32 v249, v249, v216\n"
"v_mul_f32 v250, v250, v216\n"
"v_mul_f32 v251, v251, v216\n"
"v_accvgpr_write_b32 a80, v248\n"
"v_accvgpr_write_b32 a81, v249\n"
"v_accvgpr_write_b32 a82, v250\n"
"v_accvgpr_write_b32 a83, v251\n"
"v_accvgpr_read_b32 v240, a84\n"
"v_accvgpr_read_b32 v241, a85\n"
"v_accvgpr_read_b32 v242, a86\n"
"v_accvgpr_read_b32 v243, a87\n"
"v_mul_f32 v240, v240, v217\n"
"v_mul_f32 v241, v241, v217\n"
"v_mul_f32 v242, v242, v217\n"
"v_mul_f32 v243, v243, v217\n"
"v_accvgpr_write_b32 a84, v240\n"
"v_accvgpr_write_b32 a85, v241\n"
"v_accvgpr_write_b32 a86, v242\n"
"v_accvgpr_write_b32 a87, v243\n"
"v_accvgpr_read_b32 v244, a88\n"
"v_accvgpr_read_b32 v245, a89\n"
"v_accvgpr_read_b32 v246, a90\n"
"v_accvgpr_read_b32 v247, a91\n"
"v_mul_f32 v244, v244, v218\n"
"v_mul_f32 v245, v245, v218\n"
"v_mul_f32 v246, v246, v218\n"
"v_mul_f32 v247, v247, v218\n"
"v_accvgpr_write_b32 a88, v244\n"
"v_accvgpr_write_b32 a89, v245\n"
"v_accvgpr_write_b32 a90, v246\n"
"v_accvgpr_write_b32 a91, v247\n"
"v_accvgpr_read_b32 v248, a92\n"
"v_accvgpr_read_b32 v249, a93\n"
"v_accvgpr_read_b32 v250, a94\n"
"v_accvgpr_read_b32 v251, a95\n"
"v_mul_f32 v248, v248, v219\n"
"v_mul_f32 v249, v249, v219\n"
"v_mul_f32 v250, v250, v219\n"
"v_mul_f32 v251, v251, v219\n"
"v_accvgpr_write_b32 a92, v248\n"
"v_accvgpr_write_b32 a93, v249\n"
"v_accvgpr_write_b32 a94, v250\n"
"v_accvgpr_write_b32 a95, v251\n"
"v_accvgpr_read_b32 v240, a96\n"
"v_accvgpr_read_b32 v241, a97\n"
"v_accvgpr_read_b32 v242, a98\n"
"v_accvgpr_read_b32 v243, a99\n"
"v_mul_f32 v240, v240, v216\n"
"v_mul_f32 v241, v241, v216\n"
"v_mul_f32 v242, v242, v216\n"
"v_mul_f32 v243, v243, v216\n"
"v_accvgpr_write_b32 a96, v240\n"
"v_accvgpr_write_b32 a97, v241\n"
"v_accvgpr_write_b32 a98, v242\n"
"v_accvgpr_write_b32 a99, v243\n"
"v_accvgpr_read_b32 v244, a100\n"
"v_accvgpr_read_b32 v245, a101\n"
"v_accvgpr_read_b32 v246, a102\n"
"v_accvgpr_read_b32 v247, a103\n"
"v_mul_f32 v244, v244, v217\n"
"v_mul_f32 v245, v245, v217\n"
"v_mul_f32 v246, v246, v217\n"
"v_mul_f32 v247, v247, v217\n"
"v_accvgpr_write_b32 a100, v244\n"
"v_accvgpr_write_b32 a101, v245\n"
"v_accvgpr_write_b32 a102, v246\n"
"v_accvgpr_write_b32 a103, v247\n"
"v_accvgpr_read_b32 v248, a104\n"
"v_accvgpr_read_b32 v249, a105\n"
"v_accvgpr_read_b32 v250, a106\n"
"v_accvgpr_read_b32 v251, a107\n"
"v_mul_f32 v248, v248, v218\n"
"v_mul_f32 v249, v249, v218\n"
"v_mul_f32 v250, v250, v218\n"
"v_mul_f32 v251, v251, v218\n"
"v_accvgpr_write_b32 a104, v248\n"
"v_accvgpr_write_b32 a105, v249\n"
"v_accvgpr_write_b32 a106, v250\n"
"v_accvgpr_write_b32 a107, v251\n"
"v_accvgpr_read_b32 v240, a108\n"
"v_accvgpr_read_b32 v241, a109\n"
"v_accvgpr_read_b32 v242, a110\n"
"v_accvgpr_read_b32 v243, a111\n"
"v_mul_f32 v240, v240, v219\n"
"v_mul_f32 v241, v241, v219\n"
"v_mul_f32 v242, v242, v219\n"
"v_mul_f32 v243, v243, v219\n"
"v_accvgpr_write_b32 a108, v240\n"
"v_accvgpr_write_b32 a109, v241\n"
"v_accvgpr_write_b32 a110, v242\n"
"v_accvgpr_write_b32 a111, v243\n"
"v_accvgpr_read_b32 v244, a112\n"
"v_accvgpr_read_b32 v245, a113\n"
"v_accvgpr_read_b32 v246, a114\n"
"v_accvgpr_read_b32 v247, a115\n"
"v_mul_f32 v244, v244, v216\n"
"v_mul_f32 v245, v245, v216\n"
"v_mul_f32 v246, v246, v216\n"
"v_mul_f32 v247, v247, v216\n"
"v_accvgpr_write_b32 a112, v244\n"
"v_accvgpr_write_b32 a113, v245\n"
"v_accvgpr_write_b32 a114, v246\n"
"v_accvgpr_write_b32 a115, v247\n"
"v_accvgpr_read_b32 v248, a116\n"
"v_accvgpr_read_b32 v249, a117\n"
"v_accvgpr_read_b32 v250, a118\n"
"v_accvgpr_read_b32 v251, a119\n"
"v_mul_f32 v248, v248, v217\n"
"v_mul_f32 v249, v249, v217\n"
"v_mul_f32 v250, v250, v217\n"
"v_mul_f32 v251, v251, v217\n"
"v_accvgpr_write_b32 a116, v248\n"
"v_accvgpr_write_b32 a117, v249\n"
"v_accvgpr_write_b32 a118, v250\n"
"v_accvgpr_write_b32 a119, v251\n"
"v_accvgpr_read_b32 v240, a120\n"
"v_accvgpr_read_b32 v241, a121\n"
"v_accvgpr_read_b32 v242, a122\n"
"v_accvgpr_read_b32 v243, a123\n"
"v_mul_f32 v240, v240, v218\n"
"v_mul_f32 v241, v241, v218\n"
"v_mul_f32 v242, v242, v218\n"
"v_mul_f32 v243, v243, v218\n"
"v_accvgpr_write_b32 a120, v240\n"
"v_accvgpr_write_b32 a121, v241\n"
"v_accvgpr_write_b32 a122, v242\n"
"v_accvgpr_write_b32 a123, v243\n"
"v_accvgpr_read_b32 v244, a124\n"
"v_accvgpr_read_b32 v245, a125\n"
"v_accvgpr_read_b32 v246, a126\n"
"v_accvgpr_read_b32 v247, a127\n"
"v_mul_f32 v244, v244, v219\n"
"v_mul_f32 v245, v245, v219\n"
"v_mul_f32 v246, v246, v219\n"
"v_mul_f32 v247, v247, v219\n"
"v_accvgpr_write_b32 a124, v244\n"
"v_accvgpr_write_b32 a125, v245\n"
"v_accvgpr_write_b32 a126, v246\n"
"v_accvgpr_write_b32 a127, v247\n"
"v_accvgpr_read_b32 v248, a128\n"
"v_accvgpr_read_b32 v249, a129\n"
"v_accvgpr_read_b32 v250, a130\n"
"v_accvgpr_read_b32 v251, a131\n"
"v_mul_f32 v248, v248, v216\n"
"v_mul_f32 v249, v249, v216\n"
"v_mul_f32 v250, v250, v216\n"
"v_mul_f32 v251, v251, v216\n"
"v_accvgpr_write_b32 a128, v248\n"
"v_accvgpr_write_b32 a129, v249\n"
"v_accvgpr_write_b32 a130, v250\n"
"v_accvgpr_write_b32 a131, v251\n"
"v_accvgpr_read_b32 v240, a132\n"
"v_accvgpr_read_b32 v241, a133\n"
"v_accvgpr_read_b32 v242, a134\n"
"v_accvgpr_read_b32 v243, a135\n"
"v_mul_f32 v240, v240, v217\n"
"v_mul_f32 v241, v241, v217\n"
"v_mul_f32 v242, v242, v217\n"
"v_mul_f32 v243, v243, v217\n"
"v_accvgpr_write_b32 a132, v240\n"
"v_accvgpr_write_b32 a133, v241\n"
"v_accvgpr_write_b32 a134, v242\n"
"v_accvgpr_write_b32 a135, v243\n"
"v_accvgpr_read_b32 v244, a136\n"
"v_accvgpr_read_b32 v245, a137\n"
"v_accvgpr_read_b32 v246, a138\n"
"v_accvgpr_read_b32 v247, a139\n"
"v_mul_f32 v244, v244, v218\n"
"v_mul_f32 v245, v245, v218\n"
"v_mul_f32 v246, v246, v218\n"
"v_mul_f32 v247, v247, v218\n"
"v_accvgpr_write_b32 a136, v244\n"
"v_accvgpr_write_b32 a137, v245\n"
"v_accvgpr_write_b32 a138, v246\n"
"v_accvgpr_write_b32 a139, v247\n"
"v_accvgpr_read_b32 v248, a140\n"
"v_accvgpr_read_b32 v249, a141\n"
"v_accvgpr_read_b32 v250, a142\n"
"v_accvgpr_read_b32 v251, a143\n"
"v_mul_f32 v248, v248, v219\n"
"v_mul_f32 v249, v249, v219\n"
"v_mul_f32 v250, v250, v219\n"
"v_mul_f32 v251, v251, v219\n"
"v_accvgpr_write_b32 a140, v248\n"
"v_accvgpr_write_b32 a141, v249\n"
"v_accvgpr_write_b32 a142, v250\n"
"v_accvgpr_write_b32 a143, v251\n"
"v_accvgpr_read_b32 v240, a144\n"
"v_accvgpr_read_b32 v241, a145\n"
"v_accvgpr_read_b32 v242, a146\n"
"v_accvgpr_read_b32 v243, a147\n"
"v_mul_f32 v240, v240, v216\n"
"v_mul_f32 v241, v241, v216\n"
"v_mul_f32 v242, v242, v216\n"
"v_mul_f32 v243, v243, v216\n"
"v_accvgpr_write_b32 a144, v240\n"
"v_accvgpr_write_b32 a145, v241\n"
"v_accvgpr_write_b32 a146, v242\n"
"v_accvgpr_write_b32 a147, v243\n"
"v_accvgpr_read_b32 v244, a148\n"
"v_accvgpr_read_b32 v245, a149\n"
"v_accvgpr_read_b32 v246, a150\n"
"v_accvgpr_read_b32 v247, a151\n"
"v_mul_f32 v244, v244, v217\n"
"v_mul_f32 v245, v245, v217\n"
"v_mul_f32 v246, v246, v217\n"
"v_mul_f32 v247, v247, v217\n"
"v_accvgpr_write_b32 a148, v244\n"
"v_accvgpr_write_b32 a149, v245\n"
"v_accvgpr_write_b32 a150, v246\n"
"v_accvgpr_write_b32 a151, v247\n"
"v_accvgpr_read_b32 v248, a152\n"
"v_accvgpr_read_b32 v249, a153\n"
"v_accvgpr_read_b32 v250, a154\n"
"v_accvgpr_read_b32 v251, a155\n"
"v_mul_f32 v248, v248, v218\n"
"v_mul_f32 v249, v249, v218\n"
"v_mul_f32 v250, v250, v218\n"
"v_mul_f32 v251, v251, v218\n"
"v_accvgpr_write_b32 a152, v248\n"
"v_accvgpr_write_b32 a153, v249\n"
"v_accvgpr_write_b32 a154, v250\n"
"v_accvgpr_write_b32 a155, v251\n"
"v_accvgpr_read_b32 v240, a156\n"
"v_accvgpr_read_b32 v241, a157\n"
"v_accvgpr_read_b32 v242, a158\n"
"v_accvgpr_read_b32 v243, a159\n"
"v_mul_f32 v240, v240, v219\n"
"v_mul_f32 v241, v241, v219\n"
"v_mul_f32 v242, v242, v219\n"
"v_mul_f32 v243, v243, v219\n"
"v_accvgpr_write_b32 a156, v240\n"
"v_accvgpr_write_b32 a157, v241\n"
"v_accvgpr_write_b32 a158, v242\n"
"v_accvgpr_write_b32 a159, v243\n"
"v_accvgpr_read_b32 v244, a160\n"
"v_accvgpr_read_b32 v245, a161\n"
"v_accvgpr_read_b32 v246, a162\n"
"v_accvgpr_read_b32 v247, a163\n"
"v_mul_f32 v244, v244, v216\n"
"v_mul_f32 v245, v245, v216\n"
"v_mul_f32 v246, v246, v216\n"
"v_mul_f32 v247, v247, v216\n"
"v_accvgpr_write_b32 a160, v244\n"
"v_accvgpr_write_b32 a161, v245\n"
"v_accvgpr_write_b32 a162, v246\n"
"v_accvgpr_write_b32 a163, v247\n"
"v_accvgpr_read_b32 v248, a164\n"
"v_accvgpr_read_b32 v249, a165\n"
"v_accvgpr_read_b32 v250, a166\n"
"v_accvgpr_read_b32 v251, a167\n"
"v_mul_f32 v248, v248, v217\n"
"v_mul_f32 v249, v249, v217\n"
"v_mul_f32 v250, v250, v217\n"
"v_mul_f32 v251, v251, v217\n"
"v_accvgpr_write_b32 a164, v248\n"
"v_accvgpr_write_b32 a165, v249\n"
"v_accvgpr_write_b32 a166, v250\n"
"v_accvgpr_write_b32 a167, v251\n"
"v_accvgpr_read_b32 v240, a168\n"
"v_accvgpr_read_b32 v241, a169\n"
"v_accvgpr_read_b32 v242, a170\n"
"v_accvgpr_read_b32 v243, a171\n"
"v_mul_f32 v240, v240, v218\n"
"v_mul_f32 v241, v241, v218\n"
"v_mul_f32 v242, v242, v218\n"
"v_mul_f32 v243, v243, v218\n"
"v_accvgpr_write_b32 a168, v240\n"
"v_accvgpr_write_b32 a169, v241\n"
"v_accvgpr_write_b32 a170, v242\n"
"v_accvgpr_write_b32 a171, v243\n"
"v_accvgpr_read_b32 v244, a172\n"
"v_accvgpr_read_b32 v245, a173\n"
"v_accvgpr_read_b32 v246, a174\n"
"v_accvgpr_read_b32 v247, a175\n"
"v_mul_f32 v244, v244, v219\n"
"v_mul_f32 v245, v245, v219\n"
"v_mul_f32 v246, v246, v219\n"
"v_mul_f32 v247, v247, v219\n"
"v_accvgpr_write_b32 a172, v244\n"
"v_accvgpr_write_b32 a173, v245\n"
"v_accvgpr_write_b32 a174, v246\n"
"v_accvgpr_write_b32 a175, v247\n"
"v_accvgpr_read_b32 v248, a176\n"
"v_accvgpr_read_b32 v249, a177\n"
"v_accvgpr_read_b32 v250, a178\n"
"v_accvgpr_read_b32 v251, a179\n"
"v_mul_f32 v248, v248, v216\n"
"v_mul_f32 v249, v249, v216\n"
"v_mul_f32 v250, v250, v216\n"
"v_mul_f32 v251, v251, v216\n"
"v_accvgpr_write_b32 a176, v248\n"
"v_accvgpr_write_b32 a177, v249\n"
"v_accvgpr_write_b32 a178, v250\n"
"v_accvgpr_write_b32 a179, v251\n"
"v_accvgpr_read_b32 v240, a180\n"
"v_accvgpr_read_b32 v241, a181\n"
"v_accvgpr_read_b32 v242, a182\n"
"v_accvgpr_read_b32 v243, a183\n"
"v_mul_f32 v240, v240, v217\n"
"v_mul_f32 v241, v241, v217\n"
"v_mul_f32 v242, v242, v217\n"
"v_mul_f32 v243, v243, v217\n"
"v_accvgpr_write_b32 a180, v240\n"
"v_accvgpr_write_b32 a181, v241\n"
"v_accvgpr_write_b32 a182, v242\n"
"v_accvgpr_write_b32 a183, v243\n"
"v_accvgpr_read_b32 v244, a184\n"
"v_accvgpr_read_b32 v245, a185\n"
"v_accvgpr_read_b32 v246, a186\n"
"v_accvgpr_read_b32 v247, a187\n"
"v_mul_f32 v244, v244, v218\n"
"v_mul_f32 v245, v245, v218\n"
"v_mul_f32 v246, v246, v218\n"
"v_mul_f32 v247, v247, v218\n"
"v_accvgpr_write_b32 a184, v244\n"
"v_accvgpr_write_b32 a185, v245\n"
"v_accvgpr_write_b32 a186, v246\n"
"v_accvgpr_write_b32 a187, v247\n"
"v_accvgpr_read_b32 v248, a188\n"
"v_accvgpr_read_b32 v249, a189\n"
"v_accvgpr_read_b32 v250, a190\n"
"v_accvgpr_read_b32 v251, a191\n"
"v_mul_f32 v248, v248, v219\n"
"v_mul_f32 v249, v249, v219\n"
"v_mul_f32 v250, v250, v219\n"
"v_mul_f32 v251, v251, v219\n"
"v_accvgpr_write_b32 a188, v248\n"
"v_accvgpr_write_b32 a189, v249\n"
"v_accvgpr_write_b32 a190, v250\n"
"v_accvgpr_write_b32 a191, v251\n"
"s_nop 2\n"
"s_cmp_eq_u32 s28, 0\n"
"s_cbranch_scc1 .Lmha_ret_tf\n"
"s_cmp_eq_u32 s28, 1\n"
"s_cbranch_scc1 .Lmha_ret_t0\n"
"s_cmp_eq_u32 s28, 2\n"
"s_cbranch_scc1 .Lmha_ret_l1\n"
"s_cmp_eq_u32 s28, 3\n"
"s_cbranch_scc1 .Lmha_ret_l2\n"
"s_cmp_eq_u32 s28, 4\n"
"s_cbranch_scc1 .Lmha_ret_l3\n"
"s_cmp_eq_u32 s28, 5\n"
"s_cbranch_scc1 .Lmha_ret_l4\n"
"s_cmp_eq_u32 s28, 6\n"
"s_cbranch_scc1 .Lmha_ret_l5\n"
"s_cmp_eq_u32 s28, 7\n"
"s_cbranch_scc1 .Lmha_ret_l0\n"
".Lmha_end:\n"
    : "+{v0}"(tid), "+{s[4:5]}"(ka), "+{s6}"(bi), "+{s7}"(lb)
    :
    : "memory", "vcc", "scc", "v1", "v2", "v3", "v4", "v5", "v6", "v7", "v8", "v9", "v10", "v11", "v12", "v13", "v14", "v15", "v16", "v17", "v18", "v19", "v20", "v21",
      "v22", "v23", "v24", "v25", "v26", "v27", "v28", "v29", "v30", "v31", "v32", "v33", "v34", "v35", "v36", "v37", "v38", "v39", "v40", "v41", "v42", "v43", "v44", "v45",
      "v46", "v47", "v48", "v49", "v50", "v51", "v52", "v53", "v54", "v55", "v56", "v57", "v58", "v59", "v60", "v61", "v62", "v63", "v64", "v65", "v66", "v67", "v68", "v69",
      "v70", "v71", "v72", "v73", "v74", "v75", "v76", "v77", "v78", "v79", "v80", "v81", "v82", "v83", "v84", "v85", "v86", "v87", "v88", "v89", "v90", "v91", "v92", "v93",
      "v94", "v95", "v96", "v97", "v98", "v99", "v100", "v101", "v102", "v103", "v104", "v105", "v106", "v107", "v108", "v109", "v110", "v111", "v112", "v113", "v114", "v115", "v116", "v117",
      "v118", "v119", "v120", "v121", "v122", "v123", "v124", "v125", "v126", "v127", "v128", "v129", "v130", "v131", "v132", "v133", "v134", "v135", "v136", "v137", "v138", "v139", "v140", "v141",
      "v142", "v143", "v144", "v145", "v146", "v147", "v148", "v149", "v150", "v151", "v152", "v153", "v154", "v155", "v156", "v157", "v158", "v159", "v160", "v161", "v162", "v163", "v164", "v165",
      "v166", "v167", "v168", "v169", "v170", "v171", "v172", "v173", "v174", "v175", "v176", "v177", "v178", "v179", "v180", "v181", "v182", "v183", "v184", "v185", "v186", "v187", "v188", "v189",
      "v190", "v191", "v192", "v193", "v194", "v195", "v196", "v197", "v198", "v199", "v200", "v201", "v202", "v203", "v204", "v205", "v206", "v207", "v208", "v209", "v210", "v211", "v212", "v213",
      "v214", "v215", "v216", "v217", "v218", "v219", "v220", "v221", "v222", "v223", "v224", "v225", "v226", "v227", "v228", "v229", "v230", "v231", "v232", "v233", "v234", "v235", "v236", "v237",
      "v238", "v239", "v240", "v241", "v242", "v243", "v244", "v245", "v246", "v247", "v248", "v249", "v250", "v251", "v252", "v253", "v254", "v255", "a0", "a1", "a2", "a3", "a4", "a5",
      "a6", "a7", "a8", "a9", "a10", "a11", "a12", "a13", "a14", "a15", "a16", "a17", "a18", "a19", "a20", "a21", "a22", "a23", "a24", "a25", "a26", "a27", "a28", "a29",
      "a30", "a31", "a32", "a33", "a34", "a35", "a36", "a37", "a38", "a39", "a40", "a41", "a42", "a43", "a44", "a45", "a46", "a47", "a48", "a49", "a50", "a51", "a52", "a53",
      "a54", "a55", "a56", "a57", "a58", "a59", "a60", "a61", "a62", "a63", "a64", "a65", "a66", "a67", "a68", "a69", "a70", "a71", "a72", "a73", "a74", "a75", "a76", "a77",
      "a78", "a79", "a80", "a81", "a82", "a83", "a84", "a85", "a86", "a87", "a88", "a89", "a90", "a91", "a92", "a93", "a94", "a95", "a96", "a97", "a98", "a99", "a100", "a101",
      "a102", "a103", "a104", "a105", "a106", "a107", "a108", "a109", "a110", "a111", "a112", "a113", "a114", "a115", "a116", "a117", "a118", "a119", "a120", "a121", "a122", "a123", "a124", "a125",
      "a126", "a127", "a128", "a129", "a130", "a131", "a132", "a133", "a134", "a135", "a136", "a137", "a138", "a139", "a140", "a141", "a142", "a143", "a144", "a145", "a146", "a147", "a148", "a149",
      "a150", "a151", "a152", "a153", "a154", "a155", "a156", "a157", "a158", "a159", "a160", "a161", "a162", "a163", "a164", "a165", "a166", "a167", "a168", "a169", "a170", "a171", "a172", "a173",
      "a174", "a175", "a176", "a177", "a178", "a179", "a180", "a181", "a182", "a183", "a184", "a185", "a186", "a187", "a188", "a189", "a190", "a191", "a192", "a193", "a194", "a195", "a196", "a197",
      "a198", "a199", "a200", "a201", "a202", "a203", "a204", "a205", "a206", "a207", "a208", "a209", "a210", "a211", "a212", "a213", "a214", "a215", "a216", "a217", "a218", "a219", "a220", "a221",
      "a222", "a223", "a224", "a225", "a226", "a227", "a228", "a229", "a230", "a231", "a232", "a233", "a234", "a235", "a236", "a237", "a238", "a239", "a240", "a241", "a242", "a243", "a244", "a245",
      "a246", "a247", "a248", "a249", "a250", "a251", "a252", "a253", "a254", "a255", "s3", "s8", "s9", "s10", "s11", "s12", "s13", "s14", "s15", "s16", "s17", "s18", "s19", "s20",
      "s21", "s22", "s23", "s24", "s25", "s26", "s27", "s28", "s29", "s30", "s31", "s33", "s34", "s35", "s36", "s37", "s38", "s39", "s40", "s41", "s42", "s43", "s44", "s45",
      "s46", "s47", "s48", "s49", "s50", "s51", "s52", "s53", "s54", "s55", "s56", "s57", "s58", "s59", "s60", "s61", "s62", "s63", "s64", "s65", "s66", "s67", "s68", "s69",
      "s70", "s71", "s72", "s73", "s74", "s75", "s76", "s77", "s78", "s79", "s80", "s81", "s82", "s83", "s84", "s85", "s86", "s87", "s88", "s89", "s90", "s91", "s92", "s93",
      "s94", "s95", "s96", "s97", "s98", "s99"
  );
}

extern "C" void kernel_launch(void* const* d_in, const int* in_sizes, int n_in, void* d_out, int out_size, void* d_ws, size_t ws_size,
                              hipStream_t stream) {
  mha_fused<<<256, 256, 0, stream>>>((const char*)d_in[0], (const char*)d_in[1], (const char*)d_in[2], (const char*)d_in[3], (const char*)d_in[4],
                                     (char*)d_out, (char*)d_ws);
}
```
